```python
import torch

def _softmax(x, axis=-1):
    return torch.softmax(x, dim=axis)
def _gelu(x):
    return torch.nn.functional.gelu(x, approximate="tanh")
def _silu(x):
    return torch.nn.functional.silu(x)
def _sigmoid(x):
    return torch.sigmoid(x)
def _layernorm(x, axis=-1, eps=1e-5):
    m = x.mean(dim=axis, keepdim=True)
    v = x.var(dim=axis, keepdim=True, unbiased=False)
    return (x - m) / torch.sqrt(v + eps)
def _scatter_add(tgt, idx, src, axis=0):
    return tgt.clone().index_add_(axis, idx, src)
def _segment_sum(src, seg, n):
    out = torch.zeros((n,) + src.shape[1:], dtype=src.dtype)
    return out.index_add_(0, seg, src)
def _pad(x, pw):
    flat = tuple(v for pair in reversed(pw) for v in pair)
    return torch.nn.functional.pad(x, flat)

kernel_name = 'concat_heads__mha_block_cached__mha_block_cached__decode'

def setup_inputs():

    _w = (lambda a: (lambda t: t.to(torch.bfloat16) if t.is_floating_point() else t)(torch.as_tensor(a)))
    def _A_setup():
        B, Sq, D = 2, 1, 448
        H, Sk, Dh = 16, 4096, 256
        return {
            "x": _w((lambda *s: torch.randn(*s, dtype=torch.bfloat16) * 0.8)(B, Sq, D)),
            "k_cache": _w((lambda *s: torch.randn(*s, dtype=torch.bfloat16) * 0.8)(B, H, Sk, Dh)),
            "v_cache": _w((lambda *s: torch.randn(*s, dtype=torch.bfloat16) * 0.05)(B, H, Sk, Dh)),
            "wq": _w((lambda *s: torch.randn(*s, dtype=torch.bfloat16) * 0.8)(D, H * Dh) * (D ** -0.5)),
            "wo": _w((lambda *s: torch.randn(*s, dtype=torch.bfloat16) * 0.05)(H * Dh, D) * ((H * Dh) ** -0.5)),
        }
    _A_in = _A_setup()

    def _B_setup():
        B, Sq, D = 2, 1, 448
        H, Sk, Dh = 16, 4096, 256
        return {
            "x": _w((lambda *s: torch.randn(*s, dtype=torch.bfloat16) * 0.8)(B, Sq, D)),
            "k_cache": _w((lambda *s: torch.randn(*s, dtype=torch.bfloat16) * 0.8)(B, H, Sk, Dh)),
            "v_cache": _w((lambda *s: torch.randn(*s, dtype=torch.bfloat16) * 0.05)(B, H, Sk, Dh)),
            "wq": _w((lambda *s: torch.randn(*s, dtype=torch.bfloat16) * 0.8)(D, H * Dh) * (D ** -0.5)),
            "wo": _w((lambda *s: torch.randn(*s, dtype=torch.bfloat16) * 0.05)(H * Dh, D) * ((H * Dh) ** -0.5)),
        }
    _B_in = _B_setup()

    return {
        "A_x": _A_in["x"],
        "A_k_cache": _A_in["k_cache"],
        "A_v_cache": _A_in["v_cache"],
        "A_wq": _A_in["wq"],
        "A_wo": _A_in["wo"],
        "B_x": _B_in["x"],
        "B_k_cache": _B_in["k_cache"],
        "B_v_cache": _B_in["v_cache"],
        "B_wq": _B_in["wq"],
        "B_wo": _B_in["wo"],
    }

def reference(A_x, A_k_cache, A_v_cache, A_wq, A_wo, B_x, B_k_cache, B_v_cache, B_wq, B_wo):
    def _A_ref(x, k_cache, v_cache, wq, wo):
        B, Sq, D = x.shape
        H, Dh = 16, 256
        q = (lambda x, *d: x.permute(*d))((x @ wq).reshape(B, Sq, H, Dh), 0, 2, 1, 3)
        scores = torch.einsum("bhqd,bhkd->bhqk", q, k_cache) * (Dh ** -0.5)
        p = _softmax(scores, axis=-1)
        o = torch.einsum("bhqk,bhkd->bhqd", p, v_cache)
        return (lambda x, *d: x.permute(*d))(o, 0, 2, 1, 3).reshape(B, Sq, H * Dh) @ wo

    def _B_ref(x, k_cache, v_cache, wq, wo):
        B, Sq, D = x.shape
        H, Dh = 16, 256
        q = (lambda x, *d: x.permute(*d))((x @ wq).reshape(B, Sq, H, Dh), 0, 2, 1, 3)
        scores = torch.einsum("bhqd,bhkd->bhqk", q, k_cache) * (Dh ** -0.5)
        p = _softmax(scores, axis=-1)
        o = torch.einsum("bhqk,bhkd->bhqd", p, v_cache)
        return (lambda x, *d: x.permute(*d))(o, 0, 2, 1, 3).reshape(B, Sq, H * Dh) @ wo

    a = _A_ref(A_x, A_k_cache, A_v_cache, A_wq, A_wo)
    b = _B_ref(B_x, B_k_cache, B_v_cache, B_wq, B_wo)
    return torch.concatenate([a, b], axis=-1)
```

```cpp
#include <hip/hip_runtime.h>
#include <hip/hip_bf16.h>
#include <stdint.h>

#define WIN 16
#define KV_AUX 2
#define W_AUX 0
#define FUSED 1

namespace {

typedef unsigned v4u __attribute__((ext_vector_type(4)));
typedef unsigned v2u __attribute__((ext_vector_type(2)));
typedef float f32x4 __attribute__((ext_vector_type(4)));
typedef __bf16 bf16x8 __attribute__((ext_vector_type(8)));
typedef __bf16 bf16x2 __attribute__((ext_vector_type(2)));
#define GAS __attribute__((address_space(1)))
typedef GAS unsigned gu32;
typedef GAS unsigned long long gu64;
#define RLX_AGENT __ATOMIC_RELAXED, __HIP_MEMORY_SCOPE_AGENT

constexpr int DM = 448, NH = 16, SK = 4096, DH = 256;
constexpr int NW = 16, NTHR = NW * 64;
constexpr int N_WQ = 14, N_K = 32, N_V = 32, N_WO = 16;
constexpr int I_K = N_WQ, I_V = I_K + N_K, I_WO = I_V + N_V, N_ITEMS = I_WO + N_WO;
constexpr int W = WIN;
static_assert(W >= N_WO && W >= N_WQ, "window holds the weight slices");
constexpr int RSRC_W3 = 0x00020000;
constexpr size_t WS_PKT = 4096;
constexpr int PKT_GRAN = 512;
constexpr int RED_STR = 260, Y_STR = 512;
constexpr int L_RED = 0, L_Y = L_RED + NW * RED_STR, L_Q = L_Y + NW * Y_STR, L_P = L_Q + 128, L_ML = L_P + NW * 32,
              L_END = L_ML + NW * 2;

__device__ __forceinline__ float dot2(unsigned a, unsigned b, float c) {
  return __builtin_amdgcn_fdot2_f32_bf16(__builtin_bit_cast(bf16x2, a), __builtin_bit_cast(bf16x2, b), c, false);
}
__device__ __forceinline__ unsigned cvtpk(float lo, float hi) {
  bf16x2 r = {(__bf16)lo, (__bf16)hi};
  return __builtin_bit_cast(unsigned, r);
}
template <int CTRL> __device__ __forceinline__ float dpp(float x) {
  return __builtin_bit_cast(float, __builtin_amdgcn_mov_dpp(__builtin_bit_cast(int, x), CTRL, 0xf, 0xf, true));
}
constexpr int XOR1 = 0xB1, XOR2 = 0x4E, XOR7 = 0x141, XOR15 = 0x140, XOR8 = 0x128;
__device__ __forceinline__ float row_sum(float x) {
  x += dpp<XOR1>(x); x += dpp<XOR2>(x); x += dpp<XOR7>(x); x += dpp<XOR15>(x); return x;
}
__device__ __forceinline__ float row_max(float x) {
  x = fmaxf(x, dpp<XOR1>(x)); x = fmaxf(x, dpp<XOR2>(x)); x = fmaxf(x, dpp<XOR7>(x)); x = fmaxf(x, dpp<XOR15>(x)); return x;
}
__device__ __forceinline__ float xrow_sum(float x) {
  auto s = __builtin_amdgcn_permlane16_swap(__float_as_uint(x), __float_as_uint(x), false, false);
  x = __uint_as_float(s[0]) + __uint_as_float(s[1]);
  auto t = __builtin_amdgcn_permlane32_swap(__float_as_uint(x), __float_as_uint(x), false, false);
  return __uint_as_float(t[0]) + __uint_as_float(t[1]);
}
__device__ __forceinline__ float xrow_max(float x) {
  auto s = __builtin_amdgcn_permlane16_swap(__float_as_uint(x), __float_as_uint(x), false, false);
  x = fmaxf(__uint_as_float(s[0]), __uint_as_float(s[1]));
  auto t = __builtin_amdgcn_permlane32_swap(__float_as_uint(x), __float_as_uint(x), false, false);
  return fmaxf(__uint_as_float(t[0]), __uint_as_float(t[1]));
}
__device__ __forceinline__ v4u diag(unsigned pair, const unsigned (&sel)[4]) {
  v4u a;
  a.x = __builtin_amdgcn_perm(0u, pair, sel[0]);
  a.y = __builtin_amdgcn_perm(0u, pair, sel[1]);
  a.z = __builtin_amdgcn_perm(0u, pair, sel[2]);
  a.w = __builtin_amdgcn_perm(0u, pair, sel[3]);
  return a;
}
__device__ __forceinline__ f32x4 mfma(v4u a, v4u b, f32x4 c) {
  return __builtin_amdgcn_mfma_f32_16x16x32_bf16(__builtin_bit_cast(bf16x8, a), __builtin_bit_cast(bf16x8, b), c, 0, 0, 0);
}
__device__ __forceinline__ unsigned next_epoch(unsigned e) { return e * 0x9E3779B1u + 0x7F4A7C15u; }

__global__ void __launch_bounds__(NTHR, 4)
mha_decode(const __bf16* __restrict__ xA, const __bf16* __restrict__ kA, const __bf16* __restrict__ vA,
           const __bf16* __restrict__ wqA, const __bf16* __restrict__ woA,
           const __bf16* __restrict__ xB, const __bf16* __restrict__ kB, const __bf16* __restrict__ vB,
           const __bf16* __restrict__ wqB, const __bf16* __restrict__ woB,
           __bf16* __restrict__ out, unsigned char* __restrict__ ws) {
  __shared__ __attribute__((aligned(16))) float lds[L_END];
  const int bid = blockIdx.x;
  const int rk = bid >> 3, bh = (bid & 7) * 4 + (rk >> 3), br = bh >> 4, head = bh & 15, b = (rk >> 2) & 1, split = rk & 3;
  const int grp = br * 2 + b, prod = head * 4 + split;
  const int tid = threadIdx.x, lane = tid & 63, w = __builtin_amdgcn_readfirstlane(tid >> 6);
  const __bf16* x = br ? xB : xA;
  const __bf16* kc = br ? kB : kA;
  const __bf16* vc = br ? vB : vA;
  const __bf16* wq = br ? wqB : wqA;
  const __bf16* wo = br ? woB : woA;
  const size_t kvoff = ((size_t)(b * NH + head) * SK + split * 1024) * DH;
  const __amdgpu_buffer_rsrc_t rq = __builtin_amdgcn_make_buffer_rsrc((void*)(wq + head * DH), 0, (DM - 1) * 8192 + 512, RSRC_W3);
  const __amdgpu_buffer_rsrc_t rkc = __builtin_amdgcn_make_buffer_rsrc((void*)(kc + kvoff), 0, 1024 * 512, RSRC_W3);
  const __amdgpu_buffer_rsrc_t rvc = __builtin_amdgcn_make_buffer_rsrc((void*)(vc + kvoff), 0, 1024 * 512, RSRC_W3);
  const __amdgpu_buffer_rsrc_t ro = __builtin_amdgcn_make_buffer_rsrc((void*)(wo + head * DH * DM), 0, DH * DM * 2, RSRC_W3);
  const int hl = lane >> 5, l31 = lane & 31;
  const int vo_q = hl * 8192 + l31 * 16;
  const int vo_k = (lane >> 3) * 512 + (lane & 7) * 16;
  const int vo_v = lane * 16;
  const int vo_o0 = hl * (DM * 2) + l31 * 16;
  const int vo_o1 = l31 < 24 ? vo_o0 + 512 : 0x40000000;

  v4u ring[W];
  auto issue = [&](int i) {
    if (i < I_K) ring[i % W] = __builtin_amdgcn_raw_buffer_load_b128(rq, vo_q, (28 * w + 2 * i) * 8192, W_AUX);
    else if (i < I_V) { const int j = i - I_K;
      ring[i % W] = __builtin_amdgcn_raw_buffer_load_b128(rkc, vo_k + (j & 3) * 128, w * 4096 + (j >> 2) * 65536, KV_AUX); }
    else if (i < I_WO) { const int j = i - I_V;
      ring[i % W] = __builtin_amdgcn_raw_buffer_load_b128(rvc, vo_v + (j & 3) * 1024, w * 4096 + (j >> 2) * 65536, KV_AUX); }
    else if (i < N_ITEMS) { const int j = i - I_WO;
      ring[i % W] = __builtin_amdgcn_raw_buffer_load_b128(ro, (j & 1) ? vo_o1 : vo_o0, (16 * w + 2 * (j >> 1)) * (DM * 2), W_AUX); }
  };

#pragma unroll
  for (int i = 0; i < N_WQ; ++i) issue(i);
  __builtin_amdgcn_sched_barrier(0);
  unsigned xp[N_WQ];
  {
    const unsigned* xw = (const unsigned*)(x + b * DM + 28 * w);
#pragma unroll
    for (int i = 0; i < N_WQ; ++i) xp[i] = xw[i];
  }
  __builtin_amdgcn_s_barrier();
  __builtin_amdgcn_sched_barrier(0);
#pragma unroll
  for (int i = N_WQ; i < W; ++i) issue(i);
  __builtin_amdgcn_sched_barrier(0);

  unsigned sel[4];
  {
    const int row = lane & 15, j = row & 7;
    const bool act = ((lane >> 4) & 1) == (row >> 3);
    const unsigned two = hl ? 0x0302u : 0x0100u;
    const unsigned s = (j & 1) ? ((two << 16) | 0x0c0cu) : (0x0c0c0000u | two);
#pragma unroll
    for (int i = 0; i < 4; ++i) sel[i] = (act && (j >> 1) == i) ? s : 0x0c0c0c0cu;
  }
  const int dim4 = 128 * hl + 8 * (lane & 15) + 4 * ((lane >> 4) & 1);

  {
    f32x4 acc = {0.f, 0.f, 0.f, 0.f};
#pragma unroll
    for (int i = 0; i < N_WQ; ++i) {
      acc = mfma(diag(xp[i], sel), ring[i % W], acc);
      __builtin_amdgcn_sched_barrier(0);
      issue(i + W);
      __builtin_amdgcn_sched_barrier(0);
    }
    *(f32x4*)&lds[L_RED + w * RED_STR + dim4] = acc;
  }
  __syncthreads();
  {
    f32x4 t = *(const f32x4*)&lds[L_RED + (lane & 15) * RED_STR + 16 * w + 4 * (lane >> 4)];
    t.x = row_sum(t.x); t.y = row_sum(t.y); t.z = row_sum(t.z); t.w = row_sum(t.w);
    const v2u o = {cvtpk(t.x, t.y), cvtpk(t.z, t.w)};
    *(v2u*)&lds[L_Q + 8 * w + 2 * (lane >> 4)] = o;
  }
  __syncthreads();
  v4u qr[4];
#pragma unroll
  for (int i = 0; i < 4; ++i) qr[i] = *(const v4u*)&lds[L_Q + 32 * i + 4 * (lane & 7)];

  float xs[8];
#pragma unroll
  for (int g = 0; g < 8; ++g) {
    float d0 = 0.f, d1 = 0.f;
#pragma unroll
    for (int i = 0; i < 4; ++i) {
      const int it = I_K + 4 * g + i;
      const v4u cur = ring[it % W];
      d0 = dot2(cur.x, qr[i].x, d0); d1 = dot2(cur.y, qr[i].y, d1);
      d0 = dot2(cur.z, qr[i].z, d0); d1 = dot2(cur.w, qr[i].w, d1);
      __builtin_amdgcn_sched_barrier(0);
      issue(it + W);
      __builtin_amdgcn_sched_barrier(0);
    }
    xs[g] = d0 + d1;
  }
  {
    const bool b4 = lane & 4, b2 = lane & 2, b1 = lane & 1;
#pragma unroll
    for (int h = 0; h < 4; ++h) { const float lo = xs[h] + dpp<XOR7>(xs[h]), hi = xs[h + 4] + dpp<XOR7>(xs[h + 4]); xs[h] = b4 ? hi : lo; }
#pragma unroll
    for (int h = 0; h < 2; ++h) { const float lo = xs[h] + dpp<XOR2>(xs[h]), hi = xs[h + 2] + dpp<XOR2>(xs[h + 2]); xs[h] = b2 ? hi : lo; }
    { const float lo = xs[0] + dpp<XOR1>(xs[0]), hi = xs[1] + dpp<XOR1>(xs[1]); xs[0] = b1 ? hi : lo; }
  }
  {
    const float s = xs[0] * (0.0625f * 1.4426950408889634f);
    const float m = xrow_max(row_max(s));
    const float p = __builtin_amdgcn_exp2f(s - m);
    const float l = xrow_sum(row_sum(p));
    ((__bf16*)&lds[L_P + w * 32])[(lane & 7) * 8 + (lane >> 3)] = (__bf16)p;
    lds[L_ML + 2 * w + (lane & 1)] = (lane & 1) ? l : m;
  }
  __builtin_amdgcn_sched_barrier(0);

  {
    f32x4 acc = {0.f, 0.f, 0.f, 0.f};
#pragma unroll
    for (int g = 0; g < 8; ++g) {
      const v4u pp = *(const v4u*)&lds[L_P + w * 32 + 4 * g];
#pragma unroll
      for (int i = 0; i < 4; ++i) {
        const int it = I_V + 4 * g + i;
        acc = mfma(diag(pp[i], sel), ring[it % W], acc);
        __builtin_amdgcn_sched_barrier(0);
        issue(it + W);
        __builtin_amdgcn_sched_barrier(0);
      }
    }
    *(f32x4*)&lds[L_RED + w * RED_STR + dim4] = acc;
  }
  __syncthreads();

  float Mwg, Lwg;
  {
    f32x4 t = *(const f32x4*)&lds[L_RED + (lane & 15) * RED_STR + 16 * w + 4 * (lane >> 4)];
    const float mw = lds[L_ML + 2 * (lane & 15)], lw = lds[L_ML + 2 * (lane & 15) + 1];
    Mwg = row_max(mw);
    const float f = __builtin_amdgcn_exp2f(mw - Mwg);
    Lwg = row_sum(lw * f);
    t.x = row_sum(t.x * f); t.y = row_sum(t.y * f); t.z = row_sum(t.z * f); t.w = row_sum(t.w * f);
    const unsigned o0 = cvtpk(t.x, t.y), o1 = cvtpk(t.z, t.w);
    f32x4 y0 = {0.f, 0.f, 0.f, 0.f}, y1 = {0.f, 0.f, 0.f, 0.f};
#pragma unroll
    for (int t2 = 0; t2 < 8; ++t2) {
      const unsigned pr = __builtin_amdgcn_readlane((t2 & 1) ? o1 : o0, 16 * (t2 >> 1));
      const v4u a = diag(pr, sel);
      y0 = mfma(a, ring[(I_WO + 2 * t2) % W], y0);
      y1 = mfma(a, ring[(I_WO + 2 * t2 + 1) % W], y1);
    }
    *(f32x4*)&lds[L_Y + w * Y_STR + dim4] = y0;
    *(f32x4*)&lds[L_Y + w * Y_STR + 256 + dim4] = y1;
  }
  __syncthreads();

  gu32* ep = (gu32*)(ws + grp * 256);
  gu64* pk = (gu64*)(ws + WS_PKT) + (size_t)grp * 64 * PKT_GRAN;
  const unsigned tag = next_epoch(__hip_atomic_load(ep, RLX_AGENT));
  const unsigned long long th = (unsigned long long)tag << 32;
  if (tid < DM) {
    float s = 0.f;
#pragma unroll
    for (int i = 0; i < NW; ++i) s += lds[L_Y + i * Y_STR + tid];
    __hip_atomic_store(pk + prod * PKT_GRAN + tid, th | __float_as_uint(s), RLX_AGENT);
  } else if (tid < DM + 2) {
    __hip_atomic_store(pk + prod * PKT_GRAN + tid, th | __float_as_uint(tid == DM ? Mwg : Lwg), RLX_AGENT);
  }
  if (w == NW - 1 && prod < DM / 8) {
    gu64* src = pk + lane * PKT_GRAN;
    float v[8], m, l;
    for (unsigned spins = 0;; ++spins) {
      bool ok = true;
#pragma unroll
      for (int j = 0; j < 8; ++j) { const unsigned long long g = __hip_atomic_load(src + prod * 8 + j, RLX_AGENT);
        v[j] = __uint_as_float((unsigned)g); ok &= (unsigned)(g >> 32) == tag; }
      { const unsigned long long g = __hip_atomic_load(src + DM, RLX_AGENT); m = __uint_as_float((unsigned)g); ok &= (unsigned)(g >> 32) == tag; }
      { const unsigned long long g = __hip_atomic_load(src + DM + 1, RLX_AGENT); l = __uint_as_float((unsigned)g); ok &= (unsigned)(g >> 32) == tag; }
      if (__all(ok)) break;
      if (spins > (1u << 20)) { m = __builtin_nanf(""); break; }
      __builtin_amdgcn_s_sleep(1);
    }
    float M = fmaxf(m, dpp<XOR1>(m)); M = fmaxf(M, dpp<XOR2>(M));
    const float f = __builtin_amdgcn_exp2f(m - M);
    float L = l * f; L += dpp<XOR1>(L); L += dpp<XOR2>(L);
    const float c = f / L;
#pragma unroll
    for (int j = 0; j < 8; ++j) v[j] *= c;
    const bool b4 = lane & 4, b2 = lane & 2, b1 = lane & 1;
#pragma unroll
    for (int h = 0; h < 4; ++h) { const float lo = v[h] + dpp<XOR7>(v[h]), hi = v[h + 4] + dpp<XOR7>(v[h + 4]); v[h] = b4 ? hi : lo; }
#pragma unroll
    for (int h = 0; h < 2; ++h) { const float lo = v[h] + dpp<XOR2>(v[h]), hi = v[h + 2] + dpp<XOR2>(v[h + 2]); v[h] = b2 ? hi : lo; }
    { const float lo = v[0] + dpp<XOR1>(v[0]), hi = v[1] + dpp<XOR1>(v[1]); v[0] = b1 ? hi : lo; }
    float r = v[0] + dpp<XOR8>(v[0]);
    r = xrow_sum(r);
    if (lane < 8) out[b * (2 * DM) + br * DM + prod * 8 + lane] = (__bf16)r;
    if (prod == 0 && lane == 0) __hip_atomic_store(ep, tag, RLX_AGENT);
  }
}

}

extern "C" void kernel_launch(void* const* d_in, const int* in_sizes, int n_in, void* d_out, int out_size, void* d_ws,
                              size_t ws_size, hipStream_t stream) {
  (void)in_sizes; (void)n_in; (void)out_size; (void)ws_size;
  mha_decode<<<256, NTHR, 0, stream>>>((const __bf16*)d_in[0], (const __bf16*)d_in[1], (const __bf16*)d_in[2],
                                       (const __bf16*)d_in[3], (const __bf16*)d_in[4], (const __bf16*)d_in[5],
                                       (const __bf16*)d_in[6], (const __bf16*)d_in[7], (const __bf16*)d_in[8],
                                       (const __bf16*)d_in[9], (__bf16*)d_out, (unsigned char*)d_ws);
}
```

```cpp
#include <hip/hip_runtime.h>
#include <hip/hip_bf16.h>
#include <stdint.h>
#include <utility>

#define W1 4
#define W2 3
#define RO 3
#define BURST 2
#define W3 W2
#define N3 0
#define ROT 1
#define Q_AUX 0
#define KV_AUX 2
#define W_AUX 0

#define ST(k)

namespace {

typedef unsigned v4u __attribute__((ext_vector_type(4)));
typedef unsigned v2u __attribute__((ext_vector_type(2)));
typedef float f32x4 __attribute__((ext_vector_type(4)));
typedef float f32x2 __attribute__((ext_vector_type(2)));
typedef __bf16 bf16x8 __attribute__((ext_vector_type(8)));
typedef __bf16 bf16x2 __attribute__((ext_vector_type(2)));
typedef __amdgpu_buffer_rsrc_t rsrc_t;

constexpr int DM = 448, NH = 16, SK = 4096, DH = 256;
constexpr int NW = 8, NTHR = NW * 64;
constexpr int N_Q = 28, N_K = 64, N_V = 64, N_O = 32;
constexpr int RSRC_W3 = 0x00020000, AUX_SC1 = 16, OOB = 0x40000000;
constexpr int WS_ML = 4096, WS_PKT = 8192, PKT_BYTES = DM * 8, WS_END = WS_PKT + 256 * PKT_BYTES;
constexpr int RED_STR = 260, Y_STR = 512;
constexpr int L_RED = 0, L_Y = L_RED + NW * RED_STR, L_Q = L_Y + NW * Y_STR, L_P = L_Q + 128, L_ML = L_P + NW * 64,
              L_END = L_ML + NW * 2;

enum Op : int { IQ, IK, IV, IO, WAITV, CQ, QRED, CK, SOFTMAX, CV, MLX, PEERLD, PEERCHK, SETUP };
struct Sched { int n; int op[640]; int a[640]; };
constexpr Sched make_sched() {
  Sched s{};
  int kind[256] = {}, idx[256] = {}, n = 0;
  auto add = [&](int k, int i) { kind[n] = k; idx[n] = i; ++n; };
  add(IK, 0); add(IQ, 0); add(IV, 0); add(IO, 0);
  for (int j = 1; j < N_Q; ++j) { add(IK, j); add(IQ, j); }
  const int n1 = n;
  int o = 1, cnt = 0;
  for (int j = N_Q; j < N_K + N_V; ++j) {
    if (j == N_K) continue;
    if (j < N_K) add(IK, j); else add(IV, j - N_K);
    if (++cnt >= RO + (j < N_Q + 8 ? 100 : 0) && o < N_O) { add(IO, o++); cnt = 0; }
  }
  while (o < N_O) add(IO, o++);
  int posK[N_K] = {}, posV[N_V] = {}, posQ[N_Q] = {};
  for (int i = 0; i < n; ++i) { if (kind[i] == IK) posK[idx[i]] = i; if (kind[i] == IV) posV[idx[i]] = i; if (kind[i] == IQ) posQ[idx[i]] = i; }
  auto emit = [&](int op, int a) { s.op[s.n] = op; s.a[s.n] = a; ++s.n; };
  int nq = 0, nk = 0, nv = 0, i = 0;
  bool qr = false, pr = false;
  while (i < n) {
    const int W = i < n1 + W1 ? W1 : (i >= n - N3 ? W3 : W2);
    if (i >= W && i >= 4) emit(WAITV, W - 1);
    emit(kind[i], idx[i]);
    if (i == 3) emit(SETUP, 0);
    const int landed = i - W;
    ++i;
    while (nq < N_Q && posQ[nq] <= landed) emit(CQ, nq++);
    if (nq == N_Q && !qr) { for (int t = 0; t < BURST && i < n; ++t, ++i) emit(kind[i], idx[i]); emit(QRED, 0); qr = true; continue; }
    if (qr) { for (int t = 0; t < 3 && nk < N_K && posK[nk] <= landed; ++t) emit(CK, nk++); }
    if (nk == N_K && !pr) { for (int t = 0; t < BURST && i < n; ++t, ++i) emit(kind[i], idx[i]); emit(SOFTMAX, 0); pr = true; continue; }
    if (pr) { for (int t = 0; t < 3 && nv < N_V && posV[nv] <= landed; ++t) {
      emit(CV, nv++);
      if (nv == 8) emit(MLX, 0);
      if (nv == N_V - 12) emit(PEERLD, 0);
      if (nv == N_V - 5) emit(PEERCHK, 0); } }
  }
  while (nv < N_V) { emit(CV, nv++); if (nv == N_V - 5) emit(PEERCHK, 0); }
  return s;
}
constexpr Sched S = make_sched();

__device__ __forceinline__ float dot2(unsigned a, unsigned b, float c) {
  return __builtin_amdgcn_fdot2_f32_bf16(__builtin_bit_cast(bf16x2, a), __builtin_bit_cast(bf16x2, b), c, false);
}
__device__ __forceinline__ unsigned cvtpk(float lo, float hi) {
  bf16x2 r = {(__bf16)lo, (__bf16)hi};
  return __builtin_bit_cast(unsigned, r);
}
template <int CTRL> __device__ __forceinline__ float dpp(float x) {
  return __builtin_bit_cast(float, __builtin_amdgcn_mov_dpp(__builtin_bit_cast(int, x), CTRL, 0xf, 0xf, true));
}
constexpr int XOR1 = 0xB1, XOR2 = 0x4E, XOR7 = 0x141, XOR15 = 0x140, ROR4 = 0x124, ROR8 = 0x128;
__device__ __forceinline__ float sum8(float x) { x += dpp<XOR1>(x); x += dpp<XOR2>(x); x += dpp<XOR7>(x); return x; }
__device__ __forceinline__ float max8(float x) { x = fmaxf(x, dpp<XOR1>(x)); x = fmaxf(x, dpp<XOR2>(x)); x = fmaxf(x, dpp<XOR7>(x)); return x; }
__device__ __forceinline__ float xrow_sum(float x) {
  auto s = __builtin_amdgcn_permlane16_swap(__float_as_uint(x), __float_as_uint(x), false, false);
  x = __uint_as_float(s[0]) + __uint_as_float(s[1]);
  auto t = __builtin_amdgcn_permlane32_swap(__float_as_uint(x), __float_as_uint(x), false, false);
  return __uint_as_float(t[0]) + __uint_as_float(t[1]);
}
__device__ __forceinline__ float xrow_max(float x) {
  auto s = __builtin_amdgcn_permlane16_swap(__float_as_uint(x), __float_as_uint(x), false, false);
  x = fmaxf(__uint_as_float(s[0]), __uint_as_float(s[1]));
  auto t = __builtin_amdgcn_permlane32_swap(__float_as_uint(x), __float_as_uint(x), false, false);
  return fmaxf(__uint_as_float(t[0]), __uint_as_float(t[1]));
}
__device__ __forceinline__ f32x4 mfma(v4u a, v4u b, f32x4 c) {
  return __builtin_amdgcn_mfma_f32_16x16x32_bf16(__builtin_bit_cast(bf16x8, a), __builtin_bit_cast(bf16x8, b), c, 0, 0, 0);
}
__device__ __forceinline__ unsigned next_epoch(unsigned e) { return e * 0x9E3779B1u + 0x7F4A7C15u; }

struct Ctx {
  rsrc_t rq, rk, rv, ro, rw, rout;
  rsrc_t rx;
  unsigned xv;
  int rot;
  unsigned ep0, touch;
  int ep_off;
  int w, lane, vo_q, vo_k, vo_v, vo_o0, vo_o1, dim4, ml_off;
  unsigned sel[4], tag;
  float* lds;
  v4u qb[N_Q], kb[N_K], vb[N_V], ob[N_O], qr[4], pp;
  f32x4 acc;
  float d0, d1, xs[8], s0, mw, Mwg, scale;
  v2u peer;
  __device__ __forceinline__ v4u diag(unsigned pair) const {
    v4u a;
    a.x = __builtin_amdgcn_perm(0u, pair, sel[0]); a.y = __builtin_amdgcn_perm(0u, pair, sel[1]);
    a.z = __builtin_amdgcn_perm(0u, pair, sel[2]); a.w = __builtin_amdgcn_perm(0u, pair, sel[3]);
    return a;
  }
  __device__ __forceinline__ float treduce() {
    const bool b4 = lane & 4, b2 = lane & 2, b1 = lane & 1;
    float t[4], u[2];
#pragma unroll
    for (int h = 0; h < 4; ++h) { const float lo = xs[h] + dpp<XOR7>(xs[h]), hi = xs[h + 4] + dpp<XOR7>(xs[h + 4]); t[h] = b4 ? hi : lo; }
#pragma unroll
    for (int h = 0; h < 2; ++h) { const float lo = t[h] + dpp<XOR2>(t[h]), hi = t[h + 2] + dpp<XOR2>(t[h + 2]); u[h] = b2 ? hi : lo; }
    const float lo = u[0] + dpp<XOR1>(u[0]), hi = u[1] + dpp<XOR1>(u[1]);
    return b1 ? hi : lo;
  }
};

__device__ __forceinline__ bool peer_ok(const Ctx& c) { return __all(c.peer.y == c.tag); }

template <int I> __device__ __forceinline__ void step(Ctx& c) {
  constexpr int op = S.op[I], a = S.a[I];
  float* lds = c.lds;
  const int w = c.w, lane = c.lane;
  if constexpr (op == IQ) {
    int t = a + c.rot; t = t >= N_Q ? t - N_Q : t;
    c.qb[a] = __builtin_amdgcn_raw_buffer_load_b128(c.rq, c.vo_q, (N_Q * w + t) * 16384, Q_AUX);
  }
  else if constexpr (op == IK) c.kb[a] = __builtin_amdgcn_raw_buffer_load_b128(c.rk, c.vo_k + (a & 3) * 128, w * 4096 + (a >> 2) * 32768, KV_AUX);
  else if constexpr (op == IV) c.vb[a] = __builtin_amdgcn_raw_buffer_load_b128(c.rv, c.vo_v + (a & 3) * 1024, w * 4096 + (a >> 2) * 32768, KV_AUX);
  else if constexpr (op == IO) c.ob[a] = __builtin_amdgcn_raw_buffer_load_b128(c.ro, (a & 1) ? c.vo_o1 : c.vo_o0, (32 * w + 2 * (a >> 1)) * (DM * 2), W_AUX);
  else if constexpr (op == WAITV) __builtin_amdgcn_s_waitcnt((a & 15) | ((a >> 4) << 14) | 0x0F70);
  else if constexpr (op == SETUP) {
    c.ep0 = __builtin_amdgcn_raw_buffer_load_b32(c.rw, c.ep_off, 0, AUX_SC1);
    c.touch = __builtin_amdgcn_raw_buffer_load_b32(c.rout, 0, 0, 0);
    { int t = lane + c.rot; t = t >= N_Q ? t - N_Q : t;
      c.xv = __builtin_amdgcn_raw_buffer_load_b32(c.rx, (N_Q * w + t) * 4, 0, 0); }
    const int row = lane & 15, j = row & 7, hl = lane >> 5;
    const bool act = ((lane >> 4) & 1) == (row >> 3);
    const unsigned two = hl ? 0x0302u : 0x0100u;
    const unsigned s = (j & 1) ? ((two << 16) | 0x0c0cu) : (0x0c0c0000u | two);
#pragma unroll
    for (int i = 0; i < 4; ++i) c.sel[i] = (act && (j >> 1) == i) ? s : 0x0c0c0c0cu;
    c.dim4 = 128 * hl + 8 * (lane & 15) + 4 * ((lane >> 4) & 1);
    c.acc = f32x4{0.f, 0.f, 0.f, 0.f};
  } else if constexpr (op == CQ) c.acc = mfma(c.diag(__builtin_amdgcn_readlane(c.xv, a)), c.qb[a], c.acc);
  else if constexpr (op == QRED) {
    ST(2);
    *(f32x4*)&lds[L_RED + w * RED_STR + c.dim4] = c.acc;
    __syncthreads();
    f32x4 t = *(const f32x4*)&lds[L_RED + (lane & 7) * RED_STR + 32 * w + 4 * (lane >> 3)];
    t.x = sum8(t.x); t.y = sum8(t.y); t.z = sum8(t.z); t.w = sum8(t.w);
    const v2u o = {cvtpk(t.x, t.y), cvtpk(t.z, t.w)};
    *(v2u*)&lds[L_Q + 16 * w + 2 * (lane >> 3)] = o;
    __syncthreads();
#pragma unroll
    for (int i = 0; i < 4; ++i) c.qr[i] = *(const v4u*)&lds[L_Q + 32 * i + 4 * (lane & 7)];
    c.acc = f32x4{0.f, 0.f, 0.f, 0.f};
    c.tag = next_epoch(c.ep0);
    asm volatile("" :: "v"(c.touch));
    ST(3);
  } else if constexpr (op == CK) {
    constexpr int i = a & 3;
    if constexpr (i == 0) { c.d0 = 0.f; c.d1 = 0.f; }
    const v4u k = c.kb[a], q = c.qr[i];
    c.d0 = dot2(k.x, q.x, c.d0); c.d1 = dot2(k.y, q.y, c.d1); c.d0 = dot2(k.z, q.z, c.d0); c.d1 = dot2(k.w, q.w, c.d1);
    if constexpr (i == 3) c.xs[(a >> 2) & 7] = c.d0 + c.d1;
    if constexpr (a == 31) c.s0 = c.treduce();
  } else if constexpr (op == SOFTMAX) {
    ST(4);
    constexpr float C2 = 0.0625f * 1.4426950408889634f;
    const float s0 = c.s0 * C2, s1 = c.treduce() * C2;
    float m = max8(fmaxf(s0, s1)); m = fmaxf(m, dpp<XOR15>(m)); m = xrow_max(m);
    const float p0 = __builtin_amdgcn_exp2f(s0 - m), p1 = __builtin_amdgcn_exp2f(s1 - m);
    __bf16* pw = (__bf16*)&lds[L_P + w * 64] + (lane & 7) * 8 + (lane >> 3);
    pw[0] = (__bf16)p0; pw[64] = (__bf16)p1;
    float l = sum8(p0 + p1); l += dpp<XOR15>(l); l = xrow_sum(l);
    lds[L_ML + 2 * w + (lane & 1)] = (lane & 1) ? l : m;
    c.mw = m;
    c.pp = *(const v4u*)&lds[L_P + w * 64];
  } else if constexpr (op == CV) {
    constexpr int i = a & 3;
    c.acc = mfma(c.diag(c.pp[i]), c.vb[a], c.acc);
    if constexpr (i == 3 && a + 1 < N_V) c.pp = *(const v4u*)&lds[L_P + w * 64 + (a + 1)];
  } else if constexpr (op == MLX) {
    __syncthreads();
    const f32x2 ml = *(const f32x2*)&lds[L_ML + 2 * (lane & 7)];
    c.Mwg = max8(ml.x);
    const float L = sum8(ml.y * __builtin_amdgcn_exp2f(ml.x - c.Mwg));
    const v4u g = {__float_as_uint(c.Mwg), c.tag, __float_as_uint(L), c.tag};
    __builtin_amdgcn_raw_buffer_store_b128(g, c.rw, (w == 0 && lane == 0) ? c.ml_off : OOB, 0, AUX_SC1);
  } else if constexpr (op == PEERLD) {
    c.peer = __builtin_amdgcn_raw_buffer_load_b64(c.rw, (c.ml_off & ~63) + (lane & 7) * 8, 0, AUX_SC1);
  } else if constexpr (op == PEERCHK) {
    for (unsigned spins = 0; !peer_ok(c); ++spins) {
      if (spins > (1u << 16)) { c.peer.x = 0x7fc00000u; break; }
      __builtin_amdgcn_s_sleep(1);
      asm volatile("" ::: "memory");
      c.peer = __builtin_amdgcn_raw_buffer_load_b64(c.rw, (c.ml_off & ~63) + (lane & 7) * 8, 0, AUX_SC1);
    }
    const float v = __uint_as_float(c.peer.x);
    const float vx = dpp<XOR1>(v);
    const float m = (lane & 1) ? vx : v, l = (lane & 1) ? v : 0.f;
    float M = fmaxf(m, dpp<XOR2>(m)); M = fmaxf(M, dpp<XOR7>(M));
    const float L = sum8(l * __builtin_amdgcn_exp2f(m - M));
    c.scale = __builtin_amdgcn_exp2f(c.mw - M) / L;
  }
  __builtin_amdgcn_sched_barrier(0);
}
template <int... I> __device__ __forceinline__ void run(Ctx& c, std::integer_sequence<int, I...>) { (step<I>(c), ...); }

__global__ void __launch_bounds__(NTHR, 2)
mha_decode(const __bf16* __restrict__ xA, const __bf16* __restrict__ kA, const __bf16* __restrict__ vA,
           const __bf16* __restrict__ wqA, const __bf16* __restrict__ woA,
           const __bf16* __restrict__ xB, const __bf16* __restrict__ kB, const __bf16* __restrict__ vB,
           const __bf16* __restrict__ wqB, const __bf16* __restrict__ woB,
           __bf16* __restrict__ out, unsigned char* __restrict__ ws) {
  __shared__ __attribute__((aligned(16))) float lds[L_END];
  Ctx c;
  ST(0);
  const int bid = blockIdx.x;
  const int rk = bid >> 3, bh = (bid & 7) * 4 + (rk >> 3), br = bh >> 4, head = bh & 15, b = (rk >> 2) & 1, split = rk & 3;
  const int grp = br * 2 + b, prod = head * 4 + split;
  const int tid = threadIdx.x, lane = tid & 63, w = __builtin_amdgcn_readfirstlane(tid >> 6);
  const __bf16* x = br ? xB : xA;
  const __bf16* kc = br ? kB : kA;
  const __bf16* vc = br ? vB : vA;
  const __bf16* wq = br ? wqB : wqA;
  const __bf16* wo = br ? woB : woA;
  const size_t kvoff = ((size_t)(b * NH + head) * SK + split * 1024) * DH;
  c.rq = __builtin_amdgcn_make_buffer_rsrc((void*)(wq + head * DH), 0, (DM - 1) * 8192 + 512, RSRC_W3);
  c.rk = __builtin_amdgcn_make_buffer_rsrc((void*)(kc + kvoff), 0, 1024 * 512, RSRC_W3);
  c.rv = __builtin_amdgcn_make_buffer_rsrc((void*)(vc + kvoff), 0, 1024 * 512, RSRC_W3);
  c.ro = __builtin_amdgcn_make_buffer_rsrc((void*)(wo + head * DH * DM), 0, DH * DM * 2, RSRC_W3);
  c.rw = __builtin_amdgcn_make_buffer_rsrc((void*)ws, 0, WS_END, RSRC_W3);
  c.rout = __builtin_amdgcn_make_buffer_rsrc((void*)out, 0, 4 * DM * 2, RSRC_W3);
  const int hl = lane >> 5, l31 = lane & 31;
  c.w = w; c.lane = lane; c.lds = lds;
  c.vo_q = hl * 8192 + l31 * 16;
  c.vo_k = (lane >> 3) * 512 + (lane & 7) * 16;
  c.vo_v = lane * 16;
  c.vo_o0 = hl * (DM * 2) + l31 * 16;
  c.vo_o1 = l31 < 24 ? c.vo_o0 + 512 : OOB;
  c.ml_off = WS_ML + (grp * 64 + prod) * 16;
  c.ep_off = grp * 256;
  c.rx = __builtin_amdgcn_make_buffer_rsrc((void*)(x + b * DM), 0, DM * 2, RSRC_W3);
  c.rot = ROT ? ((rk & 7) * 7) >> 1 : 0;

  run(c, std::make_integer_sequence<int, S.n>{});

  ST(5);
  *(f32x4*)&lds[L_RED + w * RED_STR + c.dim4] = c.acc * c.scale;
  __syncthreads();
  ST(6);
  {
    f32x4 t = *(const f32x4*)&lds[L_RED + (lane & 7) * RED_STR + 32 * w + 4 * (lane >> 3)];
    t.x = sum8(t.x); t.y = sum8(t.y); t.z = sum8(t.z); t.w = sum8(t.w);
    const unsigned o0 = cvtpk(t.x, t.y), o1 = cvtpk(t.z, t.w);
    f32x4 y0 = {0.f, 0.f, 0.f, 0.f}, y1 = {0.f, 0.f, 0.f, 0.f};
#pragma unroll
    for (int t2 = 0; t2 < 16; ++t2) {
      const unsigned pr = __builtin_amdgcn_readlane((t2 & 1) ? o1 : o0, 8 * (t2 >> 1));
      const v4u a = c.diag(pr);
      y0 = mfma(a, c.ob[2 * t2], y0);
      y1 = mfma(a, c.ob[2 * t2 + 1], y1);
    }
    *(f32x4*)&lds[L_Y + w * Y_STR + c.dim4] = y0;
    *(f32x4*)&lds[L_Y + w * Y_STR + 256 + c.dim4] = y1;
  }
  __syncthreads();
  ST(7);
  const int pk = WS_PKT + grp * 64 * PKT_BYTES;
  if (w < 4) {
    f32x2 s = {0.f, 0.f};
#pragma unroll
    for (int i = 0; i < NW; ++i) s += *(const f32x2*)&lds[L_Y + i * Y_STR + 2 * tid];
    const v4u g = {__float_as_uint(s.x), c.tag, __float_as_uint(s.y), c.tag};
    __builtin_amdgcn_raw_buffer_store_b128(g, c.rw, tid < DM / 2 ? pk + prod * PKT_BYTES + tid * 16 : OOB, 0, AUX_SC1);
    ST(8);
  } else if (w == NW - 1 && prod < DM / 8) {
    const int off = pk + (lane >> 2) * PKT_BYTES + prod * 64 + (lane & 3) * 16;
    v4u g[4];
    bool bad = false;
    for (unsigned spins = 0;; ++spins) {
      bool ok = true;
      asm volatile("" ::: "memory");
#pragma unroll
      for (int i = 0; i < 4; ++i) g[i] = __builtin_amdgcn_raw_buffer_load_b128(c.rw, off, i * 16 * PKT_BYTES, AUX_SC1);
#pragma unroll
      for (int i = 0; i < 4; ++i) ok &= (g[i].y == c.tag) & (g[i].w == c.tag);
      if (__all(ok)) break;
      if (spins > (1u << 16)) { bad = true; break; }
      __builtin_amdgcn_s_sleep(1);
    }
    float r0 = (__uint_as_float(g[0].x) + __uint_as_float(g[1].x)) + (__uint_as_float(g[2].x) + __uint_as_float(g[3].x));
    float r1 = (__uint_as_float(g[0].z) + __uint_as_float(g[1].z)) + (__uint_as_float(g[2].z) + __uint_as_float(g[3].z));
    r0 += dpp<ROR4>(r0); r1 += dpp<ROR4>(r1);
    r0 += dpp<ROR8>(r0); r1 += dpp<ROR8>(r1);
    r0 = xrow_sum(r0); r1 = xrow_sum(r1);
    const unsigned o = bad ? 0x7fc07fc0u : cvtpk(r0, r1);
    __builtin_amdgcn_raw_buffer_store_b32(o, c.rout, lane < 4 ? (b * (2 * DM) + br * DM + prod * 8 + lane * 2) * 2 : OOB, 0, 0);
    __builtin_amdgcn_raw_buffer_store_b32(c.tag, c.rw, (prod == 0 && lane == 0) ? grp * 256 : OOB, 0, AUX_SC1);
    ST(9);
  }
}

}

extern "C" void kernel_launch(void* const* d_in, const int* in_sizes, int n_in, void* d_out, int out_size, void* d_ws,
                              size_t ws_size, hipStream_t stream) {
  (void)in_sizes; (void)n_in; (void)out_size; (void)ws_size;
  mha_decode<<<256, NTHR, 0, stream>>>((const __bf16*)d_in[0], (const __bf16*)d_in[1], (const __bf16*)d_in[2],
                                       (const __bf16*)d_in[3], (const __bf16*)d_in[4], (const __bf16*)d_in[5],
                                       (const __bf16*)d_in[6], (const __bf16*)d_in[7], (const __bf16*)d_in[8],
                                       (const __bf16*)d_in[9], (__bf16*)d_out, (unsigned char*)d_ws);
}
```

```cpp
#define STAMPS
#define PROBE 9
#define PROBE_K 0
#define PROBE_WG -1
#include <hip/hip_runtime.h>
#include <hip/hip_bf16.h>
#include <stdint.h>
#include <utility>

#define W1 4
#define W2 3
#define RO 3
#define BURST 2
#define W3 W2
#define N3 0
#define ROT 1
#define Q_AUX 0
#define KV_AUX 2
#define W_AUX 0

#define STAMP_OFF (8u<<20)
#define STAMP_NW 8
#define STAMP_N 10
#define ST(k) do { __builtin_amdgcn_sched_barrier(0); asm volatile("s_memrealtime %0\n\ts_waitcnt lgkmcnt(0)" : "=s"(c.stv[k]) :: "memory"); __builtin_amdgcn_sched_barrier(0); } while (0)

namespace {

typedef unsigned v4u __attribute__((ext_vector_type(4)));
typedef unsigned v2u __attribute__((ext_vector_type(2)));
typedef float f32x4 __attribute__((ext_vector_type(4)));
typedef float f32x2 __attribute__((ext_vector_type(2)));
typedef __bf16 bf16x8 __attribute__((ext_vector_type(8)));
typedef __bf16 bf16x2 __attribute__((ext_vector_type(2)));
typedef __amdgpu_buffer_rsrc_t rsrc_t;

constexpr int DM = 448, NH = 16, SK = 4096, DH = 256;
constexpr int NW = 8, NTHR = NW * 64;
constexpr int N_Q = 28, N_K = 64, N_V = 64, N_O = 32;
constexpr int RSRC_W3 = 0x00020000, AUX_SC1 = 16, OOB = 0x40000000;
constexpr int WS_ML = 4096, WS_PKT = 8192, PKT_BYTES = DM * 8, WS_END = WS_PKT + 256 * PKT_BYTES;
constexpr int RED_STR = 260, Y_STR = 512;
constexpr int L_RED = 0, L_Y = L_RED + NW * RED_STR, L_Q = L_Y + NW * Y_STR, L_P = L_Q + 128, L_ML = L_P + NW * 64,
              L_END = L_ML + NW * 2;

enum Op : int { IQ, IK, IV, IO, WAITV, CQ, QRED, CK, SOFTMAX, CV, MLX, PEERLD, PEERCHK, SETUP };
struct Sched { int n; int op[640]; int a[640]; };
constexpr Sched make_sched() {
  Sched s{};
  int kind[256] = {}, idx[256] = {}, n = 0;
  auto add = [&](int k, int i) { kind[n] = k; idx[n] = i; ++n; };
  add(IK, 0); add(IQ, 0); add(IV, 0); add(IO, 0);
  for (int j = 1; j < N_Q; ++j) { add(IK, j); add(IQ, j); }
  const int n1 = n;
  int o = 1, cnt = 0;
  for (int j = N_Q; j < N_K + N_V; ++j) {
    if (j == N_K) continue;
    if (j < N_K) add(IK, j); else add(IV, j - N_K);
    if (++cnt >= RO + (j < N_Q + 8 ? 100 : 0) && o < N_O) { add(IO, o++); cnt = 0; }
  }
  while (o < N_O) add(IO, o++);
  int posK[N_K] = {}, posV[N_V] = {}, posQ[N_Q] = {};
  for (int i = 0; i < n; ++i) { if (kind[i] == IK) posK[idx[i]] = i; if (kind[i] == IV) posV[idx[i]] = i; if (kind[i] == IQ) posQ[idx[i]] = i; }
  auto emit = [&](int op, int a) { s.op[s.n] = op; s.a[s.n] = a; ++s.n; };
  int nq = 0, nk = 0, nv = 0, i = 0;
  bool qr = false, pr = false;
  while (i < n) {
    const int W = i < n1 + W1 ? W1 : (i >= n - N3 ? W3 : W2);
    if (i >= W && i >= 4) emit(WAITV, W - 1);
    emit(kind[i], idx[i]);
    if (i == 3) emit(SETUP, 0);
    const int landed = i - W;
    ++i;
    while (nq < N_Q && posQ[nq] <= landed) emit(CQ, nq++);
    if (nq == N_Q && !qr) { for (int t = 0; t < BURST && i < n; ++t, ++i) emit(kind[i], idx[i]); emit(QRED, 0); qr = true; continue; }
    if (qr) { for (int t = 0; t < 3 && nk < N_K && posK[nk] <= landed; ++t) emit(CK, nk++); }
    if (nk == N_K && !pr) { for (int t = 0; t < BURST && i < n; ++t, ++i) emit(kind[i], idx[i]); emit(SOFTMAX, 0); pr = true; continue; }
    if (pr) { for (int t = 0; t < 3 && nv < N_V && posV[nv] <= landed; ++t) {
      emit(CV, nv++);
      if (nv == 8) emit(MLX, 0);
      if (nv == N_V - 12) emit(PEERLD, 0);
      if (nv == N_V - 5) emit(PEERCHK, 0); } }
  }
  while (nv < N_V) { emit(CV, nv++); if (nv == N_V - 5) emit(PEERCHK, 0); }
  return s;
}
constexpr Sched S = make_sched();

__device__ __forceinline__ float dot2(unsigned a, unsigned b, float c) {
  return __builtin_amdgcn_fdot2_f32_bf16(__builtin_bit_cast(bf16x2, a), __builtin_bit_cast(bf16x2, b), c, false);
}
__device__ __forceinline__ unsigned cvtpk(float lo, float hi) {
  bf16x2 r = {(__bf16)lo, (__bf16)hi};
  return __builtin_bit_cast(unsigned, r);
}
template <int CTRL> __device__ __forceinline__ float dpp(float x) {
  return __builtin_bit_cast(float, __builtin_amdgcn_mov_dpp(__builtin_bit_cast(int, x), CTRL, 0xf, 0xf, true));
}
constexpr int XOR1 = 0xB1, XOR2 = 0x4E, XOR7 = 0x141, XOR15 = 0x140, ROR4 = 0x124, ROR8 = 0x128;
__device__ __forceinline__ float sum8(float x) { x += dpp<XOR1>(x); x += dpp<XOR2>(x); x += dpp<XOR7>(x); return x; }
__device__ __forceinline__ float max8(float x) { x = fmaxf(x, dpp<XOR1>(x)); x = fmaxf(x, dpp<XOR2>(x)); x = fmaxf(x, dpp<XOR7>(x)); return x; }
__device__ __forceinline__ float xrow_sum(float x) {
  auto s = __builtin_amdgcn_permlane16_swap(__float_as_uint(x), __float_as_uint(x), false, false);
  x = __uint_as_float(s[0]) + __uint_as_float(s[1]);
  auto t = __builtin_amdgcn_permlane32_swap(__float_as_uint(x), __float_as_uint(x), false, false);
  return __uint_as_float(t[0]) + __uint_as_float(t[1]);
}
__device__ __forceinline__ float xrow_max(float x) {
  auto s = __builtin_amdgcn_permlane16_swap(__float_as_uint(x), __float_as_uint(x), false, false);
  x = fmaxf(__uint_as_float(s[0]), __uint_as_float(s[1]));
  auto t = __builtin_amdgcn_permlane32_swap(__float_as_uint(x), __float_as_uint(x), false, false);
  return fmaxf(__uint_as_float(t[0]), __uint_as_float(t[1]));
}
__device__ __forceinline__ f32x4 mfma(v4u a, v4u b, f32x4 c) {
  return __builtin_amdgcn_mfma_f32_16x16x32_bf16(__builtin_bit_cast(bf16x8, a), __builtin_bit_cast(bf16x8, b), c, 0, 0, 0);
}
__device__ __forceinline__ unsigned next_epoch(unsigned e) { return e * 0x9E3779B1u + 0x7F4A7C15u; }

struct Ctx {
  rsrc_t rq, rk, rv, ro, rw, rout;
  rsrc_t rx;
  unsigned xv;
  int rot;
  unsigned ep0, touch;
  int ep_off;
  int w, lane, vo_q, vo_k, vo_v, vo_o0, vo_o1, dim4, ml_off;
  unsigned sel[4], tag;
  float* lds;
  v4u qb[N_Q], kb[N_K], vb[N_V], ob[N_O], qr[4], pp;
  f32x4 acc;
  float d0, d1, xs[8], s0, mw, Mwg, scale;
  v2u peer;
  unsigned long long stv[10];
  __device__ __forceinline__ v4u diag(unsigned pair) const {
    v4u a;
    a.x = __builtin_amdgcn_perm(0u, pair, sel[0]); a.y = __builtin_amdgcn_perm(0u, pair, sel[1]);
    a.z = __builtin_amdgcn_perm(0u, pair, sel[2]); a.w = __builtin_amdgcn_perm(0u, pair, sel[3]);
    return a;
  }
  __device__ __forceinline__ float treduce() {
    const bool b4 = lane & 4, b2 = lane & 2, b1 = lane & 1;
    float t[4], u[2];
#pragma unroll
    for (int h = 0; h < 4; ++h) { const float lo = xs[h] + dpp<XOR7>(xs[h]), hi = xs[h + 4] + dpp<XOR7>(xs[h + 4]); t[h] = b4 ? hi : lo; }
#pragma unroll
    for (int h = 0; h < 2; ++h) { const float lo = t[h] + dpp<XOR2>(t[h]), hi = t[h + 2] + dpp<XOR2>(t[h + 2]); u[h] = b2 ? hi : lo; }
    const float lo = u[0] + dpp<XOR1>(u[0]), hi = u[1] + dpp<XOR1>(u[1]);
    return b1 ? hi : lo;
  }
};

__device__ __forceinline__ bool peer_ok(const Ctx& c) { return __all(c.peer.y == c.tag); }

template <int I> __device__ __forceinline__ void step(Ctx& c) {
  constexpr int op = S.op[I], a = S.a[I];
  float* lds = c.lds;
  const int w = c.w, lane = c.lane;
  if constexpr (op == IQ) {
    int t = a + c.rot; t = t >= N_Q ? t - N_Q : t;
    c.qb[a] = __builtin_amdgcn_raw_buffer_load_b128(c.rq, c.vo_q, (N_Q * w + t) * 16384, Q_AUX);
  }
  else if constexpr (op == IK) c.kb[a] = __builtin_amdgcn_raw_buffer_load_b128(c.rk, c.vo_k + (a & 3) * 128, w * 4096 + (a >> 2) * 32768, KV_AUX);
  else if constexpr (op == IV) c.vb[a] = __builtin_amdgcn_raw_buffer_load_b128(c.rv, c.vo_v + (a & 3) * 1024, w * 4096 + (a >> 2) * 32768, KV_AUX);
  else if constexpr (op == IO) c.ob[a] = __builtin_amdgcn_raw_buffer_load_b128(c.ro, (a & 1) ? c.vo_o1 : c.vo_o0, (32 * w + 2 * (a >> 1)) * (DM * 2), W_AUX);
  else if constexpr (op == WAITV) __builtin_amdgcn_s_waitcnt((a & 15) | ((a >> 4) << 14) | 0x0F70);
  else if constexpr (op == SETUP) {
    c.ep0 = __builtin_amdgcn_raw_buffer_load_b32(c.rw, c.ep_off, 0, AUX_SC1);
    c.touch = __builtin_amdgcn_raw_buffer_load_b32(c.rout, 0, 0, 0);
    { int t = lane + c.rot; t = t >= N_Q ? t - N_Q : t;
      c.xv = __builtin_amdgcn_raw_buffer_load_b32(c.rx, (N_Q * w + t) * 4, 0, 0); }
    const int row = lane & 15, j = row & 7, hl = lane >> 5;
    const bool act = ((lane >> 4) & 1) == (row >> 3);
    const unsigned two = hl ? 0x0302u : 0x0100u;
    const unsigned s = (j & 1) ? ((two << 16) | 0x0c0cu) : (0x0c0c0000u | two);
#pragma unroll
    for (int i = 0; i < 4; ++i) c.sel[i] = (act && (j >> 1) == i) ? s : 0x0c0c0c0cu;
    c.dim4 = 128 * hl + 8 * (lane & 15) + 4 * ((lane >> 4) & 1);
    c.acc = f32x4{0.f, 0.f, 0.f, 0.f};
  } else if constexpr (op == CQ) c.acc = mfma(c.diag(__builtin_amdgcn_readlane(c.xv, a)), c.qb[a], c.acc);
  else if constexpr (op == QRED) {
    ST(2);
    *(f32x4*)&lds[L_RED + w * RED_STR + c.dim4] = c.acc;
    __syncthreads();
    f32x4 t = *(const f32x4*)&lds[L_RED + (lane & 7) * RED_STR + 32 * w + 4 * (lane >> 3)];
    t.x = sum8(t.x); t.y = sum8(t.y); t.z = sum8(t.z); t.w = sum8(t.w);
    const v2u o = {cvtpk(t.x, t.y), cvtpk(t.z, t.w)};
    *(v2u*)&lds[L_Q + 16 * w + 2 * (lane >> 3)] = o;
    __syncthreads();
#pragma unroll
    for (int i = 0; i < 4; ++i) c.qr[i] = *(const v4u*)&lds[L_Q + 32 * i + 4 * (lane & 7)];
    c.acc = f32x4{0.f, 0.f, 0.f, 0.f};
    c.tag = next_epoch(c.ep0);
    asm volatile("" :: "v"(c.touch));
    ST(3);
  } else if constexpr (op == CK) {
    constexpr int i = a & 3;
    if constexpr (i == 0) { c.d0 = 0.f; c.d1 = 0.f; }
    const v4u k = c.kb[a], q = c.qr[i];
    c.d0 = dot2(k.x, q.x, c.d0); c.d1 = dot2(k.y, q.y, c.d1); c.d0 = dot2(k.z, q.z, c.d0); c.d1 = dot2(k.w, q.w, c.d1);
    if constexpr (i == 3) c.xs[(a >> 2) & 7] = c.d0 + c.d1;
    if constexpr (a == 31) c.s0 = c.treduce();
  } else if constexpr (op == SOFTMAX) {
    ST(4);
    constexpr float C2 = 0.0625f * 1.4426950408889634f;
    const float s0 = c.s0 * C2, s1 = c.treduce() * C2;
    float m = max8(fmaxf(s0, s1)); m = fmaxf(m, dpp<XOR15>(m)); m = xrow_max(m);
    const float p0 = __builtin_amdgcn_exp2f(s0 - m), p1 = __builtin_amdgcn_exp2f(s1 - m);
    __bf16* pw = (__bf16*)&lds[L_P + w * 64] + (lane & 7) * 8 + (lane >> 3);
    pw[0] = (__bf16)p0; pw[64] = (__bf16)p1;
    float l = sum8(p0 + p1); l += dpp<XOR15>(l); l = xrow_sum(l);
    lds[L_ML + 2 * w + (lane & 1)] = (lane & 1) ? l : m;
    c.mw = m;
    c.pp = *(const v4u*)&lds[L_P + w * 64];
  } else if constexpr (op == CV) {
    constexpr int i = a & 3;
    c.acc = mfma(c.diag(c.pp[i]), c.vb[a], c.acc);
    if constexpr (i == 3 && a + 1 < N_V) c.pp = *(const v4u*)&lds[L_P + w * 64 + (a + 1)];
  } else if constexpr (op == MLX) {
    __syncthreads();
    const f32x2 ml = *(const f32x2*)&lds[L_ML + 2 * (lane & 7)];
    c.Mwg = max8(ml.x);
    const float L = sum8(ml.y * __builtin_amdgcn_exp2f(ml.x - c.Mwg));
    const v4u g = {__float_as_uint(c.Mwg), c.tag, __float_as_uint(L), c.tag};
    __builtin_amdgcn_raw_buffer_store_b128(g, c.rw, (w == 0 && lane == 0) ? c.ml_off : OOB, 0, AUX_SC1);
  } else if constexpr (op == PEERLD) {
    c.peer = __builtin_amdgcn_raw_buffer_load_b64(c.rw, (c.ml_off & ~63) + (lane & 7) * 8, 0, AUX_SC1);
  } else if constexpr (op == PEERCHK) {
    for (unsigned spins = 0; !peer_ok(c); ++spins) {
      if (spins > (1u << 16)) { c.peer.x = 0x7fc00000u; break; }
      __builtin_amdgcn_s_sleep(1);
      asm volatile("" ::: "memory");
      c.peer = __builtin_amdgcn_raw_buffer_load_b64(c.rw, (c.ml_off & ~63) + (lane & 7) * 8, 0, AUX_SC1);
    }
    const float v = __uint_as_float(c.peer.x);
    const float vx = dpp<XOR1>(v);
    const float m = (lane & 1) ? vx : v, l = (lane & 1) ? v : 0.f;
    float M = fmaxf(m, dpp<XOR2>(m)); M = fmaxf(M, dpp<XOR7>(M));
    const float L = sum8(l * __builtin_amdgcn_exp2f(m - M));
    c.scale = __builtin_amdgcn_exp2f(c.mw - M) / L;
  }
  __builtin_amdgcn_sched_barrier(0);
}
template <int... I> __device__ __forceinline__ void run(Ctx& c, std::integer_sequence<int, I...>) { (step<I>(c), ...); }

__global__ void __launch_bounds__(NTHR, 2)
mha_decode(const __bf16* __restrict__ xA, const __bf16* __restrict__ kA, const __bf16* __restrict__ vA,
           const __bf16* __restrict__ wqA, const __bf16* __restrict__ woA,
           const __bf16* __restrict__ xB, const __bf16* __restrict__ kB, const __bf16* __restrict__ vB,
           const __bf16* __restrict__ wqB, const __bf16* __restrict__ woB,
           __bf16* __restrict__ out, unsigned char* __restrict__ ws) {
  __shared__ __attribute__((aligned(16))) float lds[L_END];
  Ctx c;
  for (int i = 0; i < 10; ++i) c.stv[i] = 0;
  ST(0);
  const int bid = blockIdx.x;
  const int rk = bid >> 3, bh = (bid & 7) * 4 + (rk >> 3), br = bh >> 4, head = bh & 15, b = (rk >> 2) & 1, split = rk & 3;
  const int grp = br * 2 + b, prod = head * 4 + split;
  const int tid = threadIdx.x, lane = tid & 63, w = __builtin_amdgcn_readfirstlane(tid >> 6);
  const __bf16* x = br ? xB : xA;
  const __bf16* kc = br ? kB : kA;
  const __bf16* vc = br ? vB : vA;
  const __bf16* wq = br ? wqB : wqA;
  const __bf16* wo = br ? woB : woA;
  const size_t kvoff = ((size_t)(b * NH + head) * SK + split * 1024) * DH;
  c.rq = __builtin_amdgcn_make_buffer_rsrc((void*)(wq + head * DH), 0, (DM - 1) * 8192 + 512, RSRC_W3);
  c.rk = __builtin_amdgcn_make_buffer_rsrc((void*)(kc + kvoff), 0, 1024 * 512, RSRC_W3);
  c.rv = __builtin_amdgcn_make_buffer_rsrc((void*)(vc + kvoff), 0, 1024 * 512, RSRC_W3);
  c.ro = __builtin_amdgcn_make_buffer_rsrc((void*)(wo + head * DH * DM), 0, DH * DM * 2, RSRC_W3);
  c.rw = __builtin_amdgcn_make_buffer_rsrc((void*)ws, 0, WS_END, RSRC_W3);
  c.rout = __builtin_amdgcn_make_buffer_rsrc((void*)out, 0, 4 * DM * 2, RSRC_W3);
  const int hl = lane >> 5, l31 = lane & 31;
  c.w = w; c.lane = lane; c.lds = lds;
  c.vo_q = hl * 8192 + l31 * 16;
  c.vo_k = (lane >> 3) * 512 + (lane & 7) * 16;
  c.vo_v = lane * 16;
  c.vo_o0 = hl * (DM * 2) + l31 * 16;
  c.vo_o1 = l31 < 24 ? c.vo_o0 + 512 : OOB;
  c.ml_off = WS_ML + (grp * 64 + prod) * 16;
  c.ep_off = grp * 256;
  c.rx = __builtin_amdgcn_make_buffer_rsrc((void*)(x + b * DM), 0, DM * 2, RSRC_W3);
  c.rot = ROT ? ((rk & 7) * 7) >> 1 : 0;

  run(c, std::make_integer_sequence<int, S.n>{});

  ST(5);
  *(f32x4*)&lds[L_RED + w * RED_STR + c.dim4] = c.acc * c.scale;
  __syncthreads();
  ST(6);
  {
    f32x4 t = *(const f32x4*)&lds[L_RED + (lane & 7) * RED_STR + 32 * w + 4 * (lane >> 3)];
    t.x = sum8(t.x); t.y = sum8(t.y); t.z = sum8(t.z); t.w = sum8(t.w);
    const unsigned o0 = cvtpk(t.x, t.y), o1 = cvtpk(t.z, t.w);
    f32x4 y0 = {0.f, 0.f, 0.f, 0.f}, y1 = {0.f, 0.f, 0.f, 0.f};
#pragma unroll
    for (int t2 = 0; t2 < 16; ++t2) {
      const unsigned pr = __builtin_amdgcn_readlane((t2 & 1) ? o1 : o0, 8 * (t2 >> 1));
      const v4u a = c.diag(pr);
      y0 = mfma(a, c.ob[2 * t2], y0);
      y1 = mfma(a, c.ob[2 * t2 + 1], y1);
    }
    *(f32x4*)&lds[L_Y + w * Y_STR + c.dim4] = y0;
    *(f32x4*)&lds[L_Y + w * Y_STR + 256 + c.dim4] = y1;
  }
  __syncthreads();
  ST(7);
  const int pk = WS_PKT + grp * 64 * PKT_BYTES;
  if (w < 4) {
    f32x2 s = {0.f, 0.f};
#pragma unroll
    for (int i = 0; i < NW; ++i) s += *(const f32x2*)&lds[L_Y + i * Y_STR + 2 * tid];
    const v4u g = {__float_as_uint(s.x), c.tag, __float_as_uint(s.y), c.tag};
    __builtin_amdgcn_raw_buffer_store_b128(g, c.rw, tid < DM / 2 ? pk + prod * PKT_BYTES + tid * 16 : OOB, 0, AUX_SC1);
    ST(8);
  } else if (w == NW - 1 && prod < DM / 8) {
    const int off = pk + (lane >> 2) * PKT_BYTES + prod * 64 + (lane & 3) * 16;
    v4u g[4];
    bool bad = false;
    for (unsigned spins = 0;; ++spins) {
      bool ok = true;
      asm volatile("" ::: "memory");
#pragma unroll
      for (int i = 0; i < 4; ++i) g[i] = __builtin_amdgcn_raw_buffer_load_b128(c.rw, off, i * 16 * PKT_BYTES, AUX_SC1);
#pragma unroll
      for (int i = 0; i < 4; ++i) ok &= (g[i].y == c.tag) & (g[i].w == c.tag);
      if (__all(ok)) break;
      if (spins > (1u << 16)) { bad = true; break; }
      __builtin_amdgcn_s_sleep(1);
    }
    float r0 = (__uint_as_float(g[0].x) + __uint_as_float(g[1].x)) + (__uint_as_float(g[2].x) + __uint_as_float(g[3].x));
    float r1 = (__uint_as_float(g[0].z) + __uint_as_float(g[1].z)) + (__uint_as_float(g[2].z) + __uint_as_float(g[3].z));
    r0 += dpp<ROR4>(r0); r1 += dpp<ROR4>(r1);
    r0 += dpp<ROR8>(r0); r1 += dpp<ROR8>(r1);
    r0 = xrow_sum(r0); r1 = xrow_sum(r1);
    const unsigned o = bad ? 0x7fc07fc0u : cvtpk(r0, r1);
    __builtin_amdgcn_raw_buffer_store_b32(o, c.rout, lane < 4 ? (b * (2 * DM) + br * DM + prod * 8 + lane * 2) * 2 : OOB, 0, 0);
    __builtin_amdgcn_raw_buffer_store_b32(c.tag, c.rw, (prod == 0 && lane == 0) ? grp * 256 : OOB, 0, AUX_SC1);
    ST(9);
  }
  if (w == NW - 1 && prod < DM / 8 && (PROBE_WG < 0 || bid == PROBE_WG)) {
    const unsigned long long d = (c.stv[PROBE] - c.stv[0]) * PROBE_K, t0 = __builtin_amdgcn_s_memrealtime();
    while (__builtin_amdgcn_s_memrealtime() - t0 < d) __builtin_amdgcn_s_sleep(2);
  }
}

}

extern "C" void kernel_launch(void* const* d_in, const int* in_sizes, int n_in, void* d_out, int out_size, void* d_ws,
                              size_t ws_size, hipStream_t stream) {
  (void)in_sizes; (void)n_in; (void)out_size; (void)ws_size;
  mha_decode<<<256, NTHR, 0, stream>>>((const __bf16*)d_in[0], (const __bf16*)d_in[1], (const __bf16*)d_in[2],
                                       (const __bf16*)d_in[3], (const __bf16*)d_in[4], (const __bf16*)d_in[5],
                                       (const __bf16*)d_in[6], (const __bf16*)d_in[7], (const __bf16*)d_in[8],
                                       (const __bf16*)d_in[9], (__bf16*)d_out, (unsigned char*)d_ws);
}
```

```cpp
#define STAMPS
#define PROBE 9
#define PROBE_K 0
#define PROBE_WG -1
#include <hip/hip_runtime.h>
#include <hip/hip_bf16.h>
#include <stdint.h>
#include <utility>

#define W1 4
#define W2 3
#define RO 3
#define BURST 2
#define W3 W2
#define N3 0
#define ROT 1
#define Q_AUX 0
#define KV_AUX 2
#define W_AUX 0

#define STAMP_OFF (8u<<20)
#define STAMP_NW 8
#define STAMP_N 10
#define ST(k) do { __builtin_amdgcn_sched_barrier(0); asm volatile("s_memrealtime %0\n\ts_waitcnt lgkmcnt(0)" : "=s"(c.stv[k]) :: "memory"); __builtin_amdgcn_sched_barrier(0); } while (0)

namespace {

typedef unsigned v4u __attribute__((ext_vector_type(4)));
typedef unsigned v2u __attribute__((ext_vector_type(2)));
typedef float f32x4 __attribute__((ext_vector_type(4)));
typedef float f32x2 __attribute__((ext_vector_type(2)));
typedef __bf16 bf16x8 __attribute__((ext_vector_type(8)));
typedef __bf16 bf16x2 __attribute__((ext_vector_type(2)));
typedef __amdgpu_buffer_rsrc_t rsrc_t;

constexpr int DM = 448, NH = 16, SK = 4096, DH = 256;
constexpr int NW = 8, NTHR = NW * 64;
constexpr int N_Q = 28, N_K = 64, N_V = 64, N_O = 32;
constexpr int RSRC_W3 = 0x00020000, AUX_SC1 = 16, OOB = 0x40000000;
constexpr int WS_ML = 4096, WS_PKT = 8192, PKT_BYTES = DM * 8, WS_END = WS_PKT + 256 * PKT_BYTES;
constexpr int RED_STR = 260, Y_STR = 512;
constexpr int L_RED = 0, L_Y = L_RED + NW * RED_STR, L_Q = L_Y + NW * Y_STR, L_P = L_Q + 128, L_ML = L_P + NW * 64,
              L_END = L_ML + NW * 2;

enum Op : int { IQ, IK, IV, IO, WAITV, CQ, QRED, CK, SOFTMAX, CV, MLX, PEERLD, PEERCHK, SETUP };
struct Sched { int n; int op[640]; int a[640]; };
constexpr Sched make_sched() {
  Sched s{};
  int kind[256] = {}, idx[256] = {}, n = 0;
  auto add = [&](int k, int i) { kind[n] = k; idx[n] = i; ++n; };
  add(IK, 0); add(IQ, 0); add(IV, 0); add(IO, 0);
  for (int j = 1; j < N_Q; ++j) { add(IK, j); add(IQ, j); }
  const int n1 = n;
  int o = 1, cnt = 0;
  for (int j = N_Q; j < N_K + N_V; ++j) {
    if (j == N_K) continue;
    if (j < N_K) add(IK, j); else add(IV, j - N_K);
    if (++cnt >= RO + (j < N_Q + 8 ? 100 : 0) && o < N_O) { add(IO, o++); cnt = 0; }
  }
  while (o < N_O) add(IO, o++);
  int posK[N_K] = {}, posV[N_V] = {}, posQ[N_Q] = {};
  for (int i = 0; i < n; ++i) { if (kind[i] == IK) posK[idx[i]] = i; if (kind[i] == IV) posV[idx[i]] = i; if (kind[i] == IQ) posQ[idx[i]] = i; }
  auto emit = [&](int op, int a) { s.op[s.n] = op; s.a[s.n] = a; ++s.n; };
  int nq = 0, nk = 0, nv = 0, i = 0;
  bool qr = false, pr = false;
  while (i < n) {
    const int W = i < n1 + W1 ? W1 : (i >= n - N3 ? W3 : W2);
    if (i >= W && i >= 4) emit(WAITV, W - 1);
    emit(kind[i], idx[i]);
    if (i == 3) emit(SETUP, 0);
    const int landed = i - W;
    ++i;
    while (nq < N_Q && posQ[nq] <= landed) emit(CQ, nq++);
    if (nq == N_Q && !qr) { for (int t = 0; t < BURST && i < n; ++t, ++i) emit(kind[i], idx[i]); emit(QRED, 0); qr = true; continue; }
    if (qr) { for (int t = 0; t < 3 && nk < N_K && posK[nk] <= landed; ++t) emit(CK, nk++); }
    if (nk == N_K && !pr) { for (int t = 0; t < BURST && i < n; ++t, ++i) emit(kind[i], idx[i]); emit(SOFTMAX, 0); pr = true; continue; }
    if (pr) { for (int t = 0; t < 3 && nv < N_V && posV[nv] <= landed; ++t) {
      emit(CV, nv++);
      if (nv == 8) emit(MLX, 0);
      if (nv == N_V - 12) emit(PEERLD, 0);
      if (nv == N_V - 5) emit(PEERCHK, 0); } }
  }
  while (nv < N_V) { emit(CV, nv++); if (nv == N_V - 5) emit(PEERCHK, 0); }
  return s;
}
constexpr Sched S = make_sched();

__device__ __forceinline__ float dot2(unsigned a, unsigned b, float c) {
  return __builtin_amdgcn_fdot2_f32_bf16(__builtin_bit_cast(bf16x2, a), __builtin_bit_cast(bf16x2, b), c, false);
}
__device__ __forceinline__ unsigned cvtpk(float lo, float hi) {
  bf16x2 r = {(__bf16)lo, (__bf16)hi};
  return __builtin_bit_cast(unsigned, r);
}
template <int CTRL> __device__ __forceinline__ float dpp(float x) {
  return __builtin_bit_cast(float, __builtin_amdgcn_mov_dpp(__builtin_bit_cast(int, x), CTRL, 0xf, 0xf, true));
}
constexpr int XOR1 = 0xB1, XOR2 = 0x4E, XOR7 = 0x141, XOR15 = 0x140, ROR4 = 0x124, ROR8 = 0x128;
__device__ __forceinline__ float sum8(float x) { x += dpp<XOR1>(x); x += dpp<XOR2>(x); x += dpp<XOR7>(x); return x; }
__device__ __forceinline__ float max8(float x) { x = fmaxf(x, dpp<XOR1>(x)); x = fmaxf(x, dpp<XOR2>(x)); x = fmaxf(x, dpp<XOR7>(x)); return x; }
__device__ __forceinline__ float xrow_sum(float x) {
  auto s = __builtin_amdgcn_permlane16_swap(__float_as_uint(x), __float_as_uint(x), false, false);
  x = __uint_as_float(s[0]) + __uint_as_float(s[1]);
  auto t = __builtin_amdgcn_permlane32_swap(__float_as_uint(x), __float_as_uint(x), false, false);
  return __uint_as_float(t[0]) + __uint_as_float(t[1]);
}
__device__ __forceinline__ float xrow_max(float x) {
  auto s = __builtin_amdgcn_permlane16_swap(__float_as_uint(x), __float_as_uint(x), false, false);
  x = fmaxf(__uint_as_float(s[0]), __uint_as_float(s[1]));
  auto t = __builtin_amdgcn_permlane32_swap(__float_as_uint(x), __float_as_uint(x), false, false);
  return fmaxf(__uint_as_float(t[0]), __uint_as_float(t[1]));
}
__device__ __forceinline__ f32x4 mfma(v4u a, v4u b, f32x4 c) {
  return __builtin_amdgcn_mfma_f32_16x16x32_bf16(__builtin_bit_cast(bf16x8, a), __builtin_bit_cast(bf16x8, b), c, 0, 0, 0);
}
__device__ __forceinline__ unsigned next_epoch(unsigned e) { return e * 0x9E3779B1u + 0x7F4A7C15u; }

struct Ctx {
  rsrc_t rq, rk, rv, ro, rw, rout;
  rsrc_t rx;
  unsigned xv;
  int rot;
  unsigned ep0, touch;
  int ep_off;
  int w, lane, vo_q, vo_k, vo_v, vo_o0, vo_o1, dim4, ml_off;
  unsigned sel[4], tag;
  float* lds;
  v4u qb[N_Q], kb[N_K], vb[N_V], ob[N_O], qr[4], pp;
  f32x4 acc;
  float d0, d1, xs[8], s0, mw, Mwg, scale;
  v2u peer;
  unsigned long long stv[10];
  __device__ __forceinline__ v4u diag(unsigned pair) const {
    v4u a;
    a.x = __builtin_amdgcn_perm(0u, pair, sel[0]); a.y = __builtin_amdgcn_perm(0u, pair, sel[1]);
    a.z = __builtin_amdgcn_perm(0u, pair, sel[2]); a.w = __builtin_amdgcn_perm(0u, pair, sel[3]);
    return a;
  }
  __device__ __forceinline__ float treduce() {
    const bool b4 = lane & 4, b2 = lane & 2, b1 = lane & 1;
    float t[4], u[2];
#pragma unroll
    for (int h = 0; h < 4; ++h) { const float lo = xs[h] + dpp<XOR7>(xs[h]), hi = xs[h + 4] + dpp<XOR7>(xs[h + 4]); t[h] = b4 ? hi : lo; }
#pragma unroll
    for (int h = 0; h < 2; ++h) { const float lo = t[h] + dpp<XOR2>(t[h]), hi = t[h + 2] + dpp<XOR2>(t[h + 2]); u[h] = b2 ? hi : lo; }
    const float lo = u[0] + dpp<XOR1>(u[0]), hi = u[1] + dpp<XOR1>(u[1]);
    return b1 ? hi : lo;
  }
};

__device__ __forceinline__ bool peer_ok(const Ctx& c) { return __all(c.peer.y == c.tag); }

template <int I> __device__ __forceinline__ void step(Ctx& c) {
  constexpr int op = S.op[I], a = S.a[I];
  float* lds = c.lds;
  const int w = c.w, lane = c.lane;
  if constexpr (op == IQ) {
    int t = a + c.rot; t = t >= N_Q ? t - N_Q : t;
    c.qb[a] = __builtin_amdgcn_raw_buffer_load_b128(c.rq, c.vo_q, (N_Q * w + t) * 16384, Q_AUX);
  }
  else if constexpr (op == IK) c.kb[a] = __builtin_amdgcn_raw_buffer_load_b128(c.rk, c.vo_k + (a & 3) * 128, w * 4096 + (a >> 2) * 32768, KV_AUX);
  else if constexpr (op == IV) c.vb[a] = __builtin_amdgcn_raw_buffer_load_b128(c.rv, c.vo_v + (a & 3) * 1024, w * 4096 + (a >> 2) * 32768, KV_AUX);
  else if constexpr (op == IO) c.ob[a] = __builtin_amdgcn_raw_buffer_load_b128(c.ro, (a & 1) ? c.vo_o1 : c.vo_o0, (32 * w + 2 * (a >> 1)) * (DM * 2), W_AUX);
  else if constexpr (op == WAITV) __builtin_amdgcn_s_waitcnt((a & 15) | ((a >> 4) << 14) | 0x0F70);
  else if constexpr (op == SETUP) {
    c.ep0 = __builtin_amdgcn_raw_buffer_load_b32(c.rw, c.ep_off, 0, AUX_SC1);
    c.touch = __builtin_amdgcn_raw_buffer_load_b32(c.rout, 0, 0, 0);
    { int t = lane + c.rot; t = t >= N_Q ? t - N_Q : t;
      c.xv = __builtin_amdgcn_raw_buffer_load_b32(c.rx, (N_Q * w + t) * 4, 0, 0); }
    const int row = lane & 15, j = row & 7, hl = lane >> 5;
    const bool act = ((lane >> 4) & 1) == (row >> 3);
    const unsigned two = hl ? 0x0302u : 0x0100u;
    const unsigned s = (j & 1) ? ((two << 16) | 0x0c0cu) : (0x0c0c0000u | two);
#pragma unroll
    for (int i = 0; i < 4; ++i) c.sel[i] = (act && (j >> 1) == i) ? s : 0x0c0c0c0cu;
    c.dim4 = 128 * hl + 8 * (lane & 15) + 4 * ((lane >> 4) & 1);
    c.acc = f32x4{0.f, 0.f, 0.f, 0.f};
  } else if constexpr (op == CQ) c.acc = mfma(c.diag(__builtin_amdgcn_readlane(c.xv, a)), c.qb[a], c.acc);
  else if constexpr (op == QRED) {
    ST(2);
    *(f32x4*)&lds[L_RED + w * RED_STR + c.dim4] = c.acc;
    __syncthreads();
    f32x4 t = *(const f32x4*)&lds[L_RED + (lane & 7) * RED_STR + 32 * w + 4 * (lane >> 3)];
    t.x = sum8(t.x); t.y = sum8(t.y); t.z = sum8(t.z); t.w = sum8(t.w);
    const v2u o = {cvtpk(t.x, t.y), cvtpk(t.z, t.w)};
    *(v2u*)&lds[L_Q + 16 * w + 2 * (lane >> 3)] = o;
    __syncthreads();
#pragma unroll
    for (int i = 0; i < 4; ++i) c.qr[i] = *(const v4u*)&lds[L_Q + 32 * i + 4 * (lane & 7)];
    c.acc = f32x4{0.f, 0.f, 0.f, 0.f};
    c.tag = next_epoch(c.ep0);
    asm volatile("" :: "v"(c.touch));
    ST(3);
  } else if constexpr (op == CK) {
    constexpr int i = a & 3;
    if constexpr (i == 0) { c.d0 = 0.f; c.d1 = 0.f; }
    const v4u k = c.kb[a], q = c.qr[i];
    c.d0 = dot2(k.x, q.x, c.d0); c.d1 = dot2(k.y, q.y, c.d1); c.d0 = dot2(k.z, q.z, c.d0); c.d1 = dot2(k.w, q.w, c.d1);
    if constexpr (i == 3) c.xs[(a >> 2) & 7] = c.d0 + c.d1;
    if constexpr (a == 31) c.s0 = c.treduce();
  } else if constexpr (op == SOFTMAX) {
    ST(4);
    constexpr float C2 = 0.0625f * 1.4426950408889634f;
    const float s0 = c.s0 * C2, s1 = c.treduce() * C2;
    float m = max8(fmaxf(s0, s1)); m = fmaxf(m, dpp<XOR15>(m)); m = xrow_max(m);
    const float p0 = __builtin_amdgcn_exp2f(s0 - m), p1 = __builtin_amdgcn_exp2f(s1 - m);
    __bf16* pw = (__bf16*)&lds[L_P + w * 64] + (lane & 7) * 8 + (lane >> 3);
    pw[0] = (__bf16)p0; pw[64] = (__bf16)p1;
    float l = sum8(p0 + p1); l += dpp<XOR15>(l); l = xrow_sum(l);
    lds[L_ML + 2 * w + (lane & 1)] = (lane & 1) ? l : m;
    c.mw = m;
    c.pp = *(const v4u*)&lds[L_P + w * 64];
  } else if constexpr (op == CV) {
    constexpr int i = a & 3;
    c.acc = mfma(c.diag(c.pp[i]), c.vb[a], c.acc);
    if constexpr (i == 3 && a + 1 < N_V) c.pp = *(const v4u*)&lds[L_P + w * 64 + (a + 1)];
  } else if constexpr (op == MLX) {
    __syncthreads();
    const f32x2 ml = *(const f32x2*)&lds[L_ML + 2 * (lane & 7)];
    c.Mwg = max8(ml.x);
    const float L = sum8(ml.y * __builtin_amdgcn_exp2f(ml.x - c.Mwg));
    const v4u g = {__float_as_uint(c.Mwg), c.tag, __float_as_uint(L), c.tag};
    __builtin_amdgcn_raw_buffer_store_b128(g, c.rw, (w == 0 && lane == 0) ? c.ml_off : OOB, 0, AUX_SC1);
  } else if constexpr (op == PEERLD) {
    c.peer = __builtin_amdgcn_raw_buffer_load_b64(c.rw, (c.ml_off & ~63) + (lane & 7) * 8, 0, AUX_SC1);
  } else if constexpr (op == PEERCHK) {
    for (unsigned spins = 0; !peer_ok(c); ++spins) {
      if (spins > (1u << 16)) { c.peer.x = 0x7fc00000u; break; }
      __builtin_amdgcn_s_sleep(1);
      asm volatile("" ::: "memory");
      c.peer = __builtin_amdgcn_raw_buffer_load_b64(c.rw, (c.ml_off & ~63) + (lane & 7) * 8, 0, AUX_SC1);
    }
    const float v = __uint_as_float(c.peer.x);
    const float vx = dpp<XOR1>(v);
    const float m = (lane & 1) ? vx : v, l = (lane & 1) ? v : 0.f;
    float M = fmaxf(m, dpp<XOR2>(m)); M = fmaxf(M, dpp<XOR7>(M));
    const float L = sum8(l * __builtin_amdgcn_exp2f(m - M));
    c.scale = __builtin_amdgcn_exp2f(c.mw - M) / L;
  }
  __builtin_amdgcn_sched_barrier(0);
}
template <int... I> __device__ __forceinline__ void run(Ctx& c, std::integer_sequence<int, I...>) { (step<I>(c), ...); }

__global__ void __launch_bounds__(NTHR, 2)
mha_decode(const __bf16* __restrict__ xA, const __bf16* __restrict__ kA, const __bf16* __restrict__ vA,
           const __bf16* __restrict__ wqA, const __bf16* __restrict__ woA,
           const __bf16* __restrict__ xB, const __bf16* __restrict__ kB, const __bf16* __restrict__ vB,
           const __bf16* __restrict__ wqB, const __bf16* __restrict__ woB,
           __bf16* __restrict__ out, unsigned char* __restrict__ ws) {
  __shared__ __attribute__((aligned(16))) float lds[L_END];
  Ctx c;
  for (int i = 0; i < 10; ++i) c.stv[i] = 0;
  ST(0);
  const int bid = blockIdx.x;
  const int rk = bid >> 3, bh = (bid & 7) * 4 + (rk >> 3), br = bh >> 4, head = bh & 15, b = (rk >> 2) & 1, split = rk & 3;
  const int grp = br * 2 + b, prod = head * 4 + split;
  const int tid = threadIdx.x, lane = tid & 63, w = __builtin_amdgcn_readfirstlane(tid >> 6);
  const __bf16* x = br ? xB : xA;
  const __bf16* kc = br ? kB : kA;
  const __bf16* vc = br ? vB : vA;
  const __bf16* wq = br ? wqB : wqA;
  const __bf16* wo = br ? woB : woA;
  const size_t kvoff = ((size_t)(b * NH + head) * SK + split * 1024) * DH;
  c.rq = __builtin_amdgcn_make_buffer_rsrc((void*)(wq + head * DH), 0, (DM - 1) * 8192 + 512, RSRC_W3);
  c.rk = __builtin_amdgcn_make_buffer_rsrc((void*)(kc + kvoff), 0, 1024 * 512, RSRC_W3);
  c.rv = __builtin_amdgcn_make_buffer_rsrc((void*)(vc + kvoff), 0, 1024 * 512, RSRC_W3);
  c.ro = __builtin_amdgcn_make_buffer_rsrc((void*)(wo + head * DH * DM), 0, DH * DM * 2, RSRC_W3);
  c.rw = __builtin_amdgcn_make_buffer_rsrc((void*)ws, 0, WS_END, RSRC_W3);
  c.rout = __builtin_amdgcn_make_buffer_rsrc((void*)out, 0, 4 * DM * 2, RSRC_W3);
  const int hl = lane >> 5, l31 = lane & 31;
  c.w = w; c.lane = lane; c.lds = lds;
  c.vo_q = hl * 8192 + l31 * 16;
  c.vo_k = (lane >> 3) * 512 + (lane & 7) * 16;
  c.vo_v = lane * 16;
  c.vo_o0 = hl * (DM * 2) + l31 * 16;
  c.vo_o1 = l31 < 24 ? c.vo_o0 + 512 : OOB;
  c.ml_off = WS_ML + (grp * 64 + prod) * 16;
  c.ep_off = grp * 256;
  c.rx = __builtin_amdgcn_make_buffer_rsrc((void*)(x + b * DM), 0, DM * 2, RSRC_W3);
  c.rot = ROT ? ((rk & 7) * 7) >> 1 : 0;

  run(c, std::make_integer_sequence<int, S.n>{});

  ST(5);
  *(f32x4*)&lds[L_RED + w * RED_STR + c.dim4] = c.acc * c.scale;
  __syncthreads();
  ST(6);
  {
    f32x4 t = *(const f32x4*)&lds[L_RED + (lane & 7) * RED_STR + 32 * w + 4 * (lane >> 3)];
    t.x = sum8(t.x); t.y = sum8(t.y); t.z = sum8(t.z); t.w = sum8(t.w);
    const unsigned o0 = cvtpk(t.x, t.y), o1 = cvtpk(t.z, t.w);
    f32x4 y0 = {0.f, 0.f, 0.f, 0.f}, y1 = {0.f, 0.f, 0.f, 0.f};
#pragma unroll
    for (int t2 = 0; t2 < 16; ++t2) {
      const unsigned pr = __builtin_amdgcn_readlane((t2 & 1) ? o1 : o0, 8 * (t2 >> 1));
      const v4u a = c.diag(pr);
      y0 = mfma(a, c.ob[2 * t2], y0);
      y1 = mfma(a, c.ob[2 * t2 + 1], y1);
    }
    *(f32x4*)&lds[L_Y + w * Y_STR + c.dim4] = y0;
    *(f32x4*)&lds[L_Y + w * Y_STR + 256 + c.dim4] = y1;
  }
  __syncthreads();
  ST(7);
  const int pk = WS_PKT + grp * 64 * PKT_BYTES;
  if (w < 4) {
    f32x2 s = {0.f, 0.f};
#pragma unroll
    for (int i = 0; i < NW; ++i) s += *(const f32x2*)&lds[L_Y + i * Y_STR + 2 * tid];
    const v4u g = {__float_as_uint(s.x), c.tag, __float_as_uint(s.y), c.tag};
    __builtin_amdgcn_raw_buffer_store_b128(g, c.rw, tid < DM / 2 ? pk + prod * PKT_BYTES + tid * 16 : OOB, 0, AUX_SC1);
    ST(8);
  } else if (w == NW - 1 && prod < DM / 8) {
    const int off = pk + (lane >> 2) * PKT_BYTES + prod * 64 + (lane & 3) * 16;
    v4u g[4];
    bool bad = false;
    for (unsigned spins = 0;; ++spins) {
      bool ok = true;
      asm volatile("" ::: "memory");
#pragma unroll
      for (int i = 0; i < 4; ++i) g[i] = __builtin_amdgcn_raw_buffer_load_b128(c.rw, off, i * 16 * PKT_BYTES, AUX_SC1);
#pragma unroll
      for (int i = 0; i < 4; ++i) ok &= (g[i].y == c.tag) & (g[i].w == c.tag);
      if (__all(ok)) break;
      if (spins > (1u << 16)) { bad = true; break; }
      __builtin_amdgcn_s_sleep(1);
    }
    float r0 = (__uint_as_float(g[0].x) + __uint_as_float(g[1].x)) + (__uint_as_float(g[2].x) + __uint_as_float(g[3].x));
    float r1 = (__uint_as_float(g[0].z) + __uint_as_float(g[1].z)) + (__uint_as_float(g[2].z) + __uint_as_float(g[3].z));
    r0 += dpp<ROR4>(r0); r1 += dpp<ROR4>(r1);
    r0 += dpp<ROR8>(r0); r1 += dpp<ROR8>(r1);
    r0 = xrow_sum(r0); r1 = xrow_sum(r1);
    const unsigned o = bad ? 0x7fc07fc0u : cvtpk(r0, r1);
    __builtin_amdgcn_raw_buffer_store_b32(o, c.rout, lane < 4 ? (b * (2 * DM) + br * DM + prod * 8 + lane * 2) * 2 : OOB, 0, 0);
    __builtin_amdgcn_raw_buffer_store_b32(c.tag, c.rw, (prod == 0 && lane == 0) ? grp * 256 : OOB, 0, AUX_SC1);
    ST(9);
  }
  if (w == NW - 1 && prod < DM / 8 && (PROBE_WG < 0 || bid == PROBE_WG)) {
    const unsigned long long d = (c.stv[PROBE] - c.stv[0]) * PROBE_K, t0 = __builtin_amdgcn_s_memrealtime();
    while (__builtin_amdgcn_s_memrealtime() - t0 < d) __builtin_amdgcn_s_sleep(2);
  }
}

__global__ void tiny_first(unsigned char* ws) { if (threadIdx.x == 0 && blockIdx.x == 0) *(unsigned long long*)(ws + (4u << 20)) = __builtin_amdgcn_s_memrealtime(); }
__global__ void tiny_last(unsigned char* ws, int k) {
  const unsigned long long t0 = __builtin_amdgcn_s_memrealtime();
  const unsigned long long d = (t0 - *(unsigned long long*)(ws + (4u << 20) + 64)) * k;
  while (__builtin_amdgcn_s_memrealtime() - t0 < d) __builtin_amdgcn_s_sleep(2);
}
}

extern "C" void kernel_launch(void* const* d_in, const int* in_sizes, int n_in, void* d_out, int out_size, void* d_ws,
                              size_t ws_size, hipStream_t stream) {
  (void)in_sizes; (void)n_in; (void)out_size; (void)ws_size;
  mha_decode<<<256, NTHR, 0, stream>>>((const __bf16*)d_in[0], (const __bf16*)d_in[1], (const __bf16*)d_in[2],
                                       (const __bf16*)d_in[3], (const __bf16*)d_in[4], (const __bf16*)d_in[5],
                                       (const __bf16*)d_in[6], (const __bf16*)d_in[7], (const __bf16*)d_in[8],
                                       (const __bf16*)d_in[9], (__bf16*)d_out, (unsigned char*)d_ws);
}
```

```cpp
#include <hip/hip_runtime.h>
#include <hip/hip_bf16.h>
#include <stdint.h>
#include <utility>

#define WIN2 6
#define RO 2
#define O_START 24
#define TAU 0
#define TOFF 0
#define PACE_FROM 12
#define QPUB_AT 3
#define QLD_AT 10
#define QCHK_AT 14
#define ROT 1
#define OROT 1
#define KV_AUX 2
#define W_AUX 0
#define Q_AUX 2

#define ST(k)

namespace {

typedef unsigned v4u __attribute__((ext_vector_type(4)));
typedef unsigned v2u __attribute__((ext_vector_type(2)));
typedef float f32x4 __attribute__((ext_vector_type(4)));
typedef float f32x2 __attribute__((ext_vector_type(2)));
typedef __bf16 bf16x8 __attribute__((ext_vector_type(8)));
typedef __bf16 bf16x2 __attribute__((ext_vector_type(2)));
typedef __amdgpu_buffer_rsrc_t rsrc_t;

constexpr int DM = 448, NH = 16, SK = 4096, DH = 256;
constexpr int NW = 8, NTHR = NW * 64;
constexpr int N_Q = 4, N_K = 64, N_V = 64, N_O = 32;
constexpr int RSRC_W3 = 0x00020000, AUX_SC1 = 16, OOB = 0x40000000;
constexpr int WS_ML = 4096, WS_PKT = 8192, PKT_BYTES = DM * 8, WS_Q = WS_PKT + 256 * PKT_BYTES, WS_END = WS_Q + 32 * 16384;
constexpr int RED_STR = 260, Y_STR = 512;
constexpr int L_RED = 0, L_Y = L_RED + NW * RED_STR, L_Q = L_Y + NW * Y_STR, L_P = L_Q + 128, L_ML = L_P + NW * 64,
              L_END = L_ML + NW * 2;

enum Op : int { IQ, IK, IV, IO, WAITV, PACE, QPUB, QLD, QCHK, CK, SOFTMAX, CV, MLX, PEERLD, PEERCHK, SETUP };
struct Sched { int n; int op[800]; int a[800]; };
constexpr Sched make_sched() {
  Sched s{};
  int kind[256] = {}, idx[256] = {}, n = 0;
  auto add = [&](int k, int i) { kind[n] = k; idx[n] = i; ++n; };
  for (int j = 0; j < N_Q; ++j) add(IQ, j);
  add(IK, 0); add(IV, 0); add(IO, 0);
  const int n0 = n;
  int o = 1, cnt = 0;
  for (int j = 1; j < N_K + N_V; ++j) {
    if (j == N_K) continue;
    if (j < N_K) add(IK, j); else add(IV, j - N_K);
    if (j >= O_START && ++cnt >= RO && o < N_O) { add(IO, o++); cnt = 0; }
  }
  while (o < N_O) add(IO, o++);
  int posK[N_K] = {}, posV[N_V] = {};
  for (int i = 0; i < n; ++i) { if (kind[i] == IK) posK[idx[i]] = i; if (kind[i] == IV) posV[idx[i]] = i; }
  auto emit = [&](int op, int a) { s.op[s.n] = op; s.a[s.n] = a; ++s.n; };
  int nk = 0, nv = 0, cost = 0, landed = -1, nkv = 0;
  bool qr = false, pr = false;
  for (int i = 0; i < n; ++i) {
    if (i >= n0 && kind[i] != IO) {
      if (TAU && nkv >= PACE_FROM) emit(PACE, TOFF + cost * TAU / 128);
      const int win = (WIN2 + (nkv & 1)) / 2;
      int p = i, seen = 0;
      while (p > 0 && seen < win) { --p; if (kind[p] != IO) ++seen; }
      emit(WAITV, i - 1 - p);
      landed = p;
      ++nkv;
    }
    emit(kind[i], idx[i]);
    cost += kind[i] == IO ? (idx[i] < 4 ? 7 : 0) : (kind[i] == IQ ? 7 : 8);
    if (i == 0) emit(SETUP, 0);
    if (kind[i] == IK && idx[i] == QPUB_AT) emit(QPUB, 0);
    if (kind[i] == IK && idx[i] == QLD_AT) emit(QLD, 0);
    if (kind[i] == IK && idx[i] == QCHK_AT) { emit(QCHK, 0); qr = true; continue; }
    if (qr) { for (int t = 0; t < 3 && nk < N_K && posK[nk] <= landed; ++t) emit(CK, nk++); }
    if (nk == N_K && !pr) { emit(SOFTMAX, 0); pr = true; continue; }
    if (pr) { for (int t = 0; t < 3 && nv < N_V && posV[nv] <= landed; ++t) {
      emit(CV, nv++);
      if (nv == 8) emit(MLX, 0);
      if (nv == N_V - 16) emit(PEERLD, 0);
      if (nv == N_V - 8) emit(PEERCHK, 0); } }
  }
  while (nv < N_V) { emit(CV, nv++); if (nv == N_V - 8) emit(PEERCHK, 0); }
  return s;
}
constexpr Sched S = make_sched();

__device__ __forceinline__ float dot2(unsigned a, unsigned b, float c) {
  return __builtin_amdgcn_fdot2_f32_bf16(__builtin_bit_cast(bf16x2, a), __builtin_bit_cast(bf16x2, b), c, false);
}
__device__ __forceinline__ unsigned cvtpk(float lo, float hi) {
  bf16x2 r = {(__bf16)lo, (__bf16)hi};
  return __builtin_bit_cast(unsigned, r);
}
template <int CTRL> __device__ __forceinline__ float dpp(float x) {
  return __builtin_bit_cast(float, __builtin_amdgcn_mov_dpp(__builtin_bit_cast(int, x), CTRL, 0xf, 0xf, true));
}
constexpr int XOR1 = 0xB1, XOR2 = 0x4E, XOR7 = 0x141, XOR15 = 0x140, ROR4 = 0x124, ROR8 = 0x128;
__device__ __forceinline__ float sum8(float x) { x += dpp<XOR1>(x); x += dpp<XOR2>(x); x += dpp<XOR7>(x); return x; }
__device__ __forceinline__ float max8(float x) { x = fmaxf(x, dpp<XOR1>(x)); x = fmaxf(x, dpp<XOR2>(x)); x = fmaxf(x, dpp<XOR7>(x)); return x; }
__device__ __forceinline__ float xrow_sum(float x) {
  auto s = __builtin_amdgcn_permlane16_swap(__float_as_uint(x), __float_as_uint(x), false, false);
  x = __uint_as_float(s[0]) + __uint_as_float(s[1]);
  auto t = __builtin_amdgcn_permlane32_swap(__float_as_uint(x), __float_as_uint(x), false, false);
  return __uint_as_float(t[0]) + __uint_as_float(t[1]);
}
__device__ __forceinline__ float xrow_max(float x) {
  auto s = __builtin_amdgcn_permlane16_swap(__float_as_uint(x), __float_as_uint(x), false, false);
  x = fmaxf(__uint_as_float(s[0]), __uint_as_float(s[1]));
  auto t = __builtin_amdgcn_permlane32_swap(__float_as_uint(x), __float_as_uint(x), false, false);
  return fmaxf(__uint_as_float(t[0]), __uint_as_float(t[1]));
}
__device__ __forceinline__ f32x4 mfma(v4u a, v4u b, f32x4 c) {
  return __builtin_amdgcn_mfma_f32_16x16x32_bf16(__builtin_bit_cast(bf16x8, a), __builtin_bit_cast(bf16x8, b), c, 0, 0, 0);
}
__device__ __forceinline__ unsigned next_epoch(unsigned e) { return e * 0x9E3779B1u + 0x7F4A7C15u; }

struct Ctx {
  rsrc_t rq, rk, rv, ro, rw, rout;
  rsrc_t rx;
  v2u xv;
  v4u qg;
  unsigned t0;
  int q_st, q_ld, vo_x, so_kv, orot;
  unsigned ep0, touch;
  int ep_off;
  int w, lane, vo_q, vo_k, vo_v, vo_o0, vo_o1, dim4, ml_off;
  unsigned sel[4], tag;
  float* lds;
  v4u qb[N_Q], kb[N_K], vb[N_V], ob[N_O], qr[4], pp;
  f32x4 acc;
  float d0, d1, xs[8], s0, mw, Mwg, scale;
  v2u peer;
  __device__ __forceinline__ v4u diag(unsigned pair) const {
    v4u a;
    a.x = __builtin_amdgcn_perm(0u, pair, sel[0]); a.y = __builtin_amdgcn_perm(0u, pair, sel[1]);
    a.z = __builtin_amdgcn_perm(0u, pair, sel[2]); a.w = __builtin_amdgcn_perm(0u, pair, sel[3]);
    return a;
  }
  __device__ __forceinline__ float treduce() {
    const bool b4 = lane & 4, b2 = lane & 2, b1 = lane & 1;
    float t[4], u[2];
#pragma unroll
    for (int h = 0; h < 4; ++h) { const float lo = xs[h] + dpp<XOR7>(xs[h]), hi = xs[h + 4] + dpp<XOR7>(xs[h + 4]); t[h] = b4 ? hi : lo; }
#pragma unroll
    for (int h = 0; h < 2; ++h) { const float lo = t[h] + dpp<XOR2>(t[h]), hi = t[h + 2] + dpp<XOR2>(t[h + 2]); u[h] = b2 ? hi : lo; }
    const float lo = u[0] + dpp<XOR1>(u[0]), hi = u[1] + dpp<XOR1>(u[1]);
    return b1 ? hi : lo;
  }
};

__device__ __forceinline__ bool peer_ok(const Ctx& c) { return __all(c.peer.y == c.tag); }

template <int I> __device__ __forceinline__ void step(Ctx& c) {
  constexpr int op = S.op[I], a = S.a[I];
  float* lds = c.lds;
  const int w = c.w, lane = c.lane;
  if constexpr (op == IQ) c.qb[a] = __builtin_amdgcn_raw_buffer_load_b128(c.rq, c.vo_q, (16 * w + a) * 8192, Q_AUX);
  else if constexpr (op == IK) c.kb[a] = __builtin_amdgcn_raw_buffer_load_b128(c.rk, c.vo_k + (a & 3) * 128, (c.so_kv + (a >> 2) * 32768) & 0x7ffff, KV_AUX);
  else if constexpr (op == IV) c.vb[a] = __builtin_amdgcn_raw_buffer_load_b128(c.rv, c.vo_v + (a & 3) * 1024, (c.so_kv + (a >> 2) * 32768) & 0x7ffff, KV_AUX);
  else if constexpr (op == IO) c.ob[a] = __builtin_amdgcn_raw_buffer_load_b128(c.ro, (a & 1) ? c.vo_o1 : c.vo_o0, (32 * w + (((a & ~1) + c.orot) & 31)) * (DM * 2), W_AUX);
  else if constexpr (op == WAITV) __builtin_amdgcn_s_waitcnt((a & 15) | ((a >> 4) << 14) | 0x0F70);
  else if constexpr (op == SETUP) {
    c.ep0 = __builtin_amdgcn_raw_buffer_load_b32(c.rw, c.ep_off, 0, AUX_SC1);
    c.touch = __builtin_amdgcn_raw_buffer_load_b32(c.rout, 0, 0, 0);
    c.xv = __builtin_amdgcn_raw_buffer_load_b64(c.rx, c.vo_x, 0, 0);
    const int row = lane & 15, j = row & 7, hl = lane >> 5;
    const bool act = ((lane >> 4) & 1) == (row >> 3);
    const unsigned two = hl ? 0x0302u : 0x0100u;
    const unsigned s = (j & 1) ? ((two << 16) | 0x0c0cu) : (0x0c0c0000u | two);
#pragma unroll
    for (int i = 0; i < 4; ++i) c.sel[i] = (act && (j >> 1) == i) ? s : 0x0c0c0c0cu;
    c.dim4 = 128 * hl + 8 * (lane & 15) + 4 * ((lane >> 4) & 1);
    c.acc = f32x4{0.f, 0.f, 0.f, 0.f};
  } else if constexpr (op == PACE) {
    const unsigned tgt = c.t0 + a;
    asm volatile("1:\n\ts_memrealtime vcc\n\ts_waitcnt lgkmcnt(0)\n\ts_sub_i32 vcc_lo, vcc_lo, %0\n\ts_cmp_lt_i32 vcc_lo, 0\n\ts_cbranch_scc1 1b"
                 :: "s"(tgt) : "scc", "vcc", "memory");
  } else if constexpr (op == QPUB) {
    const int j = lane & 7;
    f32x4 acc = {0.f, 0.f, 0.f, 0.f};
#pragma unroll
    for (int t = 0; t < N_Q; ++t) {
      const unsigned xw = (t & 1) ? (c.xv[t >> 1] >> 16) : (c.xv[t >> 1] & 0xffffu);
      const unsigned xs = (j & 1) ? (xw << 16) : xw;
      v4u am;
#pragma unroll
      for (int i = 0; i < 4; ++i) am[i] = (j >> 1) == i ? xs : 0u;
      acc = mfma(am, c.qb[t], acc);
    }
    ST(1);
    c.tag = next_epoch(c.ep0);
    asm volatile("" : "+v"(c.tag) : "v"(c.touch));
    *(f32x4*)&lds[L_RED + w * RED_STR + c.dim4] = acc;
    __syncthreads();
    f32x4 t = *(const f32x4*)&lds[L_RED + (lane & 7) * RED_STR + 32 * w + 4 * (lane >> 3)];
    t.x = sum8(t.x); t.y = sum8(t.y); t.z = sum8(t.z); t.w = sum8(t.w);
    const v4u g = {__float_as_uint((lane & 1) ? t.z : t.x), c.tag, __float_as_uint((lane & 1) ? t.w : t.y), c.tag};
    __builtin_amdgcn_raw_buffer_store_b128(g, c.rw, c.q_st, 0, AUX_SC1);
    ST(2);
  } else if constexpr (op == QLD) {
    c.qg = __builtin_amdgcn_raw_buffer_load_b128(c.rw, c.q_ld, 0, AUX_SC1);
  } else if constexpr (op == QCHK) {
    for (unsigned spins = 0; !__all((c.qg.y == c.tag) & (c.qg.w == c.tag)); ++spins) {
      if (spins > (1u << 16)) { c.qg.x = 0x7fc00000u; break; }
      asm volatile("" ::: "memory");
      c.qg = __builtin_amdgcn_raw_buffer_load_b128(c.rw, c.q_ld, 0, AUX_SC1);
    }
    const float q0 = xrow_sum(__uint_as_float(c.qg.x)), q1 = xrow_sum(__uint_as_float(c.qg.z));
    ((unsigned*)lds)[L_Q + 16 * w + (lane & 15)] = cvtpk(q0, q1);
    __syncthreads();
#pragma unroll
    for (int i = 0; i < 4; ++i) c.qr[i] = *(const v4u*)&lds[L_Q + 32 * i + 4 * (lane & 7)];
    ST(3);
  } else if constexpr (op == CK) {
    constexpr int i = a & 3;
    if constexpr (i == 0) { c.d0 = 0.f; c.d1 = 0.f; }
    const v4u k = c.kb[a], q = c.qr[i];
    c.d0 = dot2(k.x, q.x, c.d0); c.d1 = dot2(k.y, q.y, c.d1); c.d0 = dot2(k.z, q.z, c.d0); c.d1 = dot2(k.w, q.w, c.d1);
    if constexpr (i == 3) c.xs[(a >> 2) & 7] = c.d0 + c.d1;
    if constexpr (a == 31) c.s0 = c.treduce();
  } else if constexpr (op == SOFTMAX) {
    ST(4);
    constexpr float C2 = 0.0625f * 1.4426950408889634f;
    const float s0 = c.s0 * C2, s1 = c.treduce() * C2;
    float m = max8(fmaxf(s0, s1)); m = fmaxf(m, dpp<XOR15>(m)); m = xrow_max(m);
    const float p0 = __builtin_amdgcn_exp2f(s0 - m), p1 = __builtin_amdgcn_exp2f(s1 - m);
    __bf16* pw = (__bf16*)&lds[L_P + w * 64] + (lane & 7) * 8 + (lane >> 3);
    pw[0] = (__bf16)p0; pw[64] = (__bf16)p1;
    float l = sum8(p0 + p1); l += dpp<XOR15>(l); l = xrow_sum(l);
    lds[L_ML + 2 * w + (lane & 1)] = (lane & 1) ? l : m;
    c.mw = m;
    c.pp = *(const v4u*)&lds[L_P + w * 64];
  } else if constexpr (op == CV) {
    constexpr int i = a & 3;
    c.acc = mfma(c.diag(c.pp[i]), c.vb[a], c.acc);
    if constexpr (i == 3 && a + 1 < N_V) c.pp = *(const v4u*)&lds[L_P + w * 64 + (a + 1)];
  } else if constexpr (op == MLX) {
    __syncthreads();
    const f32x2 ml = *(const f32x2*)&lds[L_ML + 2 * (lane & 7)];
    c.Mwg = max8(ml.x);
    const float L = sum8(ml.y * __builtin_amdgcn_exp2f(ml.x - c.Mwg));
    const v4u g = {__float_as_uint(c.Mwg), c.tag, __float_as_uint(L), c.tag};
    __builtin_amdgcn_raw_buffer_store_b128(g, c.rw, (w == 0 && lane == 0) ? c.ml_off : OOB, 0, AUX_SC1);
  } else if constexpr (op == PEERLD) {
    c.peer = __builtin_amdgcn_raw_buffer_load_b64(c.rw, (c.ml_off & ~63) + (lane & 7) * 8, 0, AUX_SC1);
  } else if constexpr (op == PEERCHK) {
    for (unsigned spins = 0; !peer_ok(c); ++spins) {
      if (spins > (1u << 16)) { c.peer.x = 0x7fc00000u; break; }
      __builtin_amdgcn_s_sleep(1);
      asm volatile("" ::: "memory");
      c.peer = __builtin_amdgcn_raw_buffer_load_b64(c.rw, (c.ml_off & ~63) + (lane & 7) * 8, 0, AUX_SC1);
    }
    const float v = __uint_as_float(c.peer.x);
    const float vx = dpp<XOR1>(v);
    const float m = (lane & 1) ? vx : v, l = (lane & 1) ? v : 0.f;
    float M = fmaxf(m, dpp<XOR2>(m)); M = fmaxf(M, dpp<XOR7>(M));
    const float L = sum8(l * __builtin_amdgcn_exp2f(m - M));
    c.scale = __builtin_amdgcn_exp2f(c.mw - M) / L;
  }
  __builtin_amdgcn_sched_barrier(0);
}
template <int... I> __device__ __forceinline__ void run(Ctx& c, std::integer_sequence<int, I...>) { (step<I>(c), ...); }

__global__ void __launch_bounds__(NTHR, 2)
mha_decode(const __bf16* __restrict__ xA, const __bf16* __restrict__ kA, const __bf16* __restrict__ vA,
           const __bf16* __restrict__ wqA, const __bf16* __restrict__ woA,
           const __bf16* __restrict__ xB, const __bf16* __restrict__ kB, const __bf16* __restrict__ vB,
           const __bf16* __restrict__ wqB, const __bf16* __restrict__ woB,
           __bf16* __restrict__ out, unsigned char* __restrict__ ws) {
  __shared__ __attribute__((aligned(16))) float lds[L_END];
  Ctx c;
  ST(0);
  const int bid = blockIdx.x;
  const int rk = bid >> 3, bh = (bid & 7) * 4 + (rk >> 3), br = bh >> 4, head = bh & 15, b = (rk >> 2) & 1, split = rk & 3;
  const int grp = br * 2 + b, prod = head * 4 + split;
  const int tid = threadIdx.x, lane = tid & 63, w = __builtin_amdgcn_readfirstlane(tid >> 6);
  const __bf16* x = br ? xB : xA;
  const __bf16* kc = br ? kB : kA;
  const __bf16* vc = br ? vB : vA;
  const __bf16* wq = br ? wqB : wqA;
  const __bf16* wo = br ? woB : woA;
  const size_t kvoff = ((size_t)(b * NH + head) * SK + split * 1024) * DH;
  c.t0 = (unsigned)__builtin_amdgcn_s_memrealtime();
  c.rq = __builtin_amdgcn_make_buffer_rsrc((void*)(wq + (size_t)split * 112 * (NH * DH) + head * DH + b * 128), 0, 111 * 8192 + 256, RSRC_W3);
  c.rk = __builtin_amdgcn_make_buffer_rsrc((void*)(kc + kvoff), 0, 1024 * 512, RSRC_W3);
  c.rv = __builtin_amdgcn_make_buffer_rsrc((void*)(vc + kvoff), 0, 1024 * 512, RSRC_W3);
  c.ro = __builtin_amdgcn_make_buffer_rsrc((void*)(wo + head * DH * DM), 0, DH * DM * 2, RSRC_W3);
  c.rw = __builtin_amdgcn_make_buffer_rsrc((void*)ws, 0, WS_END, RSRC_W3);
  c.rout = __builtin_amdgcn_make_buffer_rsrc((void*)out, 0, 4 * DM * 2, RSRC_W3);
  const int hl = lane >> 5, l31 = lane & 31;
  c.w = w; c.lane = lane; c.lds = lds;
  c.vo_q = (lane >> 4) * (4 * 8192) + (lane & 15) * 16;
  c.vo_k = (lane >> 3) * 512 + (lane & 7) * 16;
  c.vo_v = lane * 16;
  c.orot = OROT ? 4 * (rk & 7) : 0;
  c.so_kv = w * 4096 + (ROT ? (head + 8 * br) & 15 : 0) * 32768;
  c.vo_o0 = hl * (DM * 2) + l31 * 16;
  c.vo_o1 = l31 < 24 ? c.vo_o0 + 512 : OOB;
  c.ml_off = WS_ML + (grp * 64 + prod) * 16;
  c.ep_off = br * 256;
  c.rx = __builtin_amdgcn_make_buffer_rsrc((void*)x, 0, 2 * DM * 2, RSRC_W3);
  c.vo_x = w < 7 ? (((lane >> 3) & 1) * DM + split * 112 + 16 * w + 4 * (lane >> 4)) * 2 : OOB;
  c.q_st = (lane & 7) < 2 ? WS_Q + bh * 16384 + (w >> 2) * 8192 + split * 2048 + (128 * b + 32 * (w & 3) + 4 * (lane >> 3) + 2 * (lane & 1)) * 8 : OOB;
  c.q_ld = WS_Q + bh * 16384 + b * 8192 + (lane >> 4) * 2048 + (32 * w + 2 * (lane & 15)) * 8;

  run(c, std::make_integer_sequence<int, S.n>{});

  ST(5);
  *(f32x4*)&lds[L_RED + w * RED_STR + c.dim4] = c.acc * c.scale;
  __syncthreads();
  ST(6);
  {
    f32x4 t = *(const f32x4*)&lds[L_RED + (lane & 7) * RED_STR + 32 * w + ((4 * (lane >> 3) + c.orot) & 31)];
    t.x = sum8(t.x); t.y = sum8(t.y); t.z = sum8(t.z); t.w = sum8(t.w);
    const unsigned o0 = cvtpk(t.x, t.y), o1 = cvtpk(t.z, t.w);
    f32x4 y0 = {0.f, 0.f, 0.f, 0.f}, y1 = {0.f, 0.f, 0.f, 0.f};
#pragma unroll
    for (int t2 = 0; t2 < 16; ++t2) {
      const unsigned pr = __builtin_amdgcn_readlane((t2 & 1) ? o1 : o0, 8 * (t2 >> 1));
      const v4u a = c.diag(pr);
      y0 = mfma(a, c.ob[2 * t2], y0);
      y1 = mfma(a, c.ob[2 * t2 + 1], y1);
    }
    *(f32x4*)&lds[L_Y + w * Y_STR + c.dim4] = y0;
    *(f32x4*)&lds[L_Y + w * Y_STR + 256 + c.dim4] = y1;
  }
  __syncthreads();
  ST(7);
  const int pk = WS_PKT + grp * 64 * PKT_BYTES;
  if (w < 4) {
    f32x2 s = {0.f, 0.f};
#pragma unroll
    for (int i = 0; i < NW; ++i) s += *(const f32x2*)&lds[L_Y + i * Y_STR + 2 * tid];
    const v4u g = {__float_as_uint(s.x), c.tag, __float_as_uint(s.y), c.tag};
    __builtin_amdgcn_raw_buffer_store_b128(g, c.rw, tid < DM / 2 ? pk + prod * PKT_BYTES + tid * 16 : OOB, 0, AUX_SC1);
    ST(8);
  } else if (w == NW - 1 && prod < DM / 8) {
    const int off = pk + (lane >> 2) * PKT_BYTES + prod * 64 + (lane & 3) * 16;
    v4u g[4];
    bool bad = false;
    for (unsigned spins = 0;; ++spins) {
      bool ok = true;
      asm volatile("" ::: "memory");
#pragma unroll
      for (int i = 0; i < 4; ++i) g[i] = __builtin_amdgcn_raw_buffer_load_b128(c.rw, off, i * 16 * PKT_BYTES, AUX_SC1);
#pragma unroll
      for (int i = 0; i < 4; ++i) ok &= (g[i].y == c.tag) & (g[i].w == c.tag);
      if (__all(ok)) break;
      if (spins > (1u << 16)) { bad = true; break; }
      __builtin_amdgcn_s_sleep(1);
    }
    float r0 = (__uint_as_float(g[0].x) + __uint_as_float(g[1].x)) + (__uint_as_float(g[2].x) + __uint_as_float(g[3].x));
    float r1 = (__uint_as_float(g[0].z) + __uint_as_float(g[1].z)) + (__uint_as_float(g[2].z) + __uint_as_float(g[3].z));
    r0 += dpp<ROR4>(r0); r1 += dpp<ROR4>(r1);
    r0 += dpp<ROR8>(r0); r1 += dpp<ROR8>(r1);
    r0 = xrow_sum(r0); r1 = xrow_sum(r1);
    const unsigned o = bad ? 0x7fc07fc0u : cvtpk(r0, r1);
    __builtin_amdgcn_raw_buffer_store_b32(o, c.rout, lane < 4 ? (b * (2 * DM) + br * DM + prod * 8 + lane * 2) * 2 : OOB, 0, 0);
    __builtin_amdgcn_raw_buffer_store_b32(c.tag, c.rw, (prod == 0 && b == 0 && lane == 0) ? c.ep_off : OOB, 0, AUX_SC1);
    ST(9);
  }
}

}

extern "C" void kernel_launch(void* const* d_in, const int* in_sizes, int n_in, void* d_out, int out_size, void* d_ws,
                              size_t ws_size, hipStream_t stream) {
  (void)in_sizes; (void)n_in; (void)out_size; (void)ws_size;
  mha_decode<<<256, NTHR, 0, stream>>>((const __bf16*)d_in[0], (const __bf16*)d_in[1], (const __bf16*)d_in[2],
                                       (const __bf16*)d_in[3], (const __bf16*)d_in[4], (const __bf16*)d_in[5],
                                       (const __bf16*)d_in[6], (const __bf16*)d_in[7], (const __bf16*)d_in[8],
                                       (const __bf16*)d_in[9], (__bf16*)d_out, (unsigned char*)d_ws);
}
```

```cpp
#define STAMPS
#define GP 0
#define GS 2
#define GK 0
#include <hip/hip_runtime.h>
#include <hip/hip_bf16.h>
#include <stdint.h>
#include <utility>

#define WIN2 6
#define RO 2
#define O_START 24
#define TAU 0
#define TOFF 0
#define PACE_FROM 12
#define QPUB_AT 3
#define QLD_AT 10
#define QCHK_AT 14
#define ROT 1
#define OROT 1
#define KV_AUX 2
#define W_AUX 0
#define Q_AUX 2

#define STAMP_OFF (8u<<20)
#define STAMP_NW 8
#define STAMP_N 10
#define ST(k) do { __builtin_amdgcn_sched_barrier(0); asm volatile("s_memrealtime %0\n\ts_waitcnt lgkmcnt(0)" : "=s"(c.stv[k]) :: "memory"); __builtin_amdgcn_sched_barrier(0); } while (0)

namespace {

typedef unsigned v4u __attribute__((ext_vector_type(4)));
typedef unsigned v2u __attribute__((ext_vector_type(2)));
typedef float f32x4 __attribute__((ext_vector_type(4)));
typedef float f32x2 __attribute__((ext_vector_type(2)));
typedef __bf16 bf16x8 __attribute__((ext_vector_type(8)));
typedef __bf16 bf16x2 __attribute__((ext_vector_type(2)));
typedef __amdgpu_buffer_rsrc_t rsrc_t;

constexpr int DM = 448, NH = 16, SK = 4096, DH = 256;
constexpr int NW = 8, NTHR = NW * 64;
constexpr int N_Q = 4, N_K = 64, N_V = 64, N_O = 32;
constexpr int RSRC_W3 = 0x00020000, AUX_SC1 = 16, OOB = 0x40000000;
constexpr int WS_ML = 4096, WS_PKT = 8192, PKT_BYTES = DM * 8, WS_Q = WS_PKT + 256 * PKT_BYTES, WS_END = WS_Q + 32 * 16384;
constexpr int RED_STR = 260, Y_STR = 512;
constexpr int L_RED = 0, L_Y = L_RED + NW * RED_STR, L_Q = L_Y + NW * Y_STR, L_P = L_Q + 128, L_ML = L_P + NW * 64,
              L_END = L_ML + NW * 2;

enum Op : int { IQ, IK, IV, IO, WAITV, PACE, QPUB, QLD, QCHK, CK, SOFTMAX, CV, MLX, PEERLD, PEERCHK, SETUP };
struct Sched { int n; int op[800]; int a[800]; };
constexpr Sched make_sched() {
  Sched s{};
  int kind[256] = {}, idx[256] = {}, n = 0;
  auto add = [&](int k, int i) { kind[n] = k; idx[n] = i; ++n; };
  for (int j = 0; j < N_Q; ++j) add(IQ, j);
  add(IK, 0); add(IV, 0); add(IO, 0);
  const int n0 = n;
  int o = 1, cnt = 0;
  for (int j = 1; j < N_K + N_V; ++j) {
    if (j == N_K) continue;
    if (j < N_K) add(IK, j); else add(IV, j - N_K);
    if (j >= O_START && ++cnt >= RO && o < N_O) { add(IO, o++); cnt = 0; }
  }
  while (o < N_O) add(IO, o++);
  int posK[N_K] = {}, posV[N_V] = {};
  for (int i = 0; i < n; ++i) { if (kind[i] == IK) posK[idx[i]] = i; if (kind[i] == IV) posV[idx[i]] = i; }
  auto emit = [&](int op, int a) { s.op[s.n] = op; s.a[s.n] = a; ++s.n; };
  int nk = 0, nv = 0, cost = 0, landed = -1, nkv = 0;
  bool qr = false, pr = false;
  for (int i = 0; i < n; ++i) {
    if (i >= n0 && kind[i] != IO) {
      if (TAU && nkv >= PACE_FROM) emit(PACE, TOFF + cost * TAU / 128);
      const int win = (WIN2 + (nkv & 1)) / 2;
      int p = i, seen = 0;
      while (p > 0 && seen < win) { --p; if (kind[p] != IO) ++seen; }
      emit(WAITV, i - 1 - p);
      landed = p;
      ++nkv;
    }
    emit(kind[i], idx[i]);
    cost += kind[i] == IO ? (idx[i] < 4 ? 7 : 0) : (kind[i] == IQ ? 7 : 8);
    if (i == 0) emit(SETUP, 0);
    if (kind[i] == IK && idx[i] == QPUB_AT) emit(QPUB, 0);
    if (kind[i] == IK && idx[i] == QLD_AT) emit(QLD, 0);
    if (kind[i] == IK && idx[i] == QCHK_AT) { emit(QCHK, 0); qr = true; continue; }
    if (qr) { for (int t = 0; t < 3 && nk < N_K && posK[nk] <= landed; ++t) emit(CK, nk++); }
    if (nk == N_K && !pr) { emit(SOFTMAX, 0); pr = true; continue; }
    if (pr) { for (int t = 0; t < 3 && nv < N_V && posV[nv] <= landed; ++t) {
      emit(CV, nv++);
      if (nv == 8) emit(MLX, 0);
      if (nv == N_V - 16) emit(PEERLD, 0);
      if (nv == N_V - 8) emit(PEERCHK, 0); } }
  }
  while (nv < N_V) { emit(CV, nv++); if (nv == N_V - 8) emit(PEERCHK, 0); }
  return s;
}
constexpr Sched S = make_sched();

__device__ __forceinline__ float dot2(unsigned a, unsigned b, float c) {
  return __builtin_amdgcn_fdot2_f32_bf16(__builtin_bit_cast(bf16x2, a), __builtin_bit_cast(bf16x2, b), c, false);
}
__device__ __forceinline__ unsigned cvtpk(float lo, float hi) {
  bf16x2 r = {(__bf16)lo, (__bf16)hi};
  return __builtin_bit_cast(unsigned, r);
}
template <int CTRL> __device__ __forceinline__ float dpp(float x) {
  return __builtin_bit_cast(float, __builtin_amdgcn_mov_dpp(__builtin_bit_cast(int, x), CTRL, 0xf, 0xf, true));
}
constexpr int XOR1 = 0xB1, XOR2 = 0x4E, XOR7 = 0x141, XOR15 = 0x140, ROR4 = 0x124, ROR8 = 0x128;
__device__ __forceinline__ float sum8(float x) { x += dpp<XOR1>(x); x += dpp<XOR2>(x); x += dpp<XOR7>(x); return x; }
__device__ __forceinline__ float max8(float x) { x = fmaxf(x, dpp<XOR1>(x)); x = fmaxf(x, dpp<XOR2>(x)); x = fmaxf(x, dpp<XOR7>(x)); return x; }
__device__ __forceinline__ float xrow_sum(float x) {
  auto s = __builtin_amdgcn_permlane16_swap(__float_as_uint(x), __float_as_uint(x), false, false);
  x = __uint_as_float(s[0]) + __uint_as_float(s[1]);
  auto t = __builtin_amdgcn_permlane32_swap(__float_as_uint(x), __float_as_uint(x), false, false);
  return __uint_as_float(t[0]) + __uint_as_float(t[1]);
}
__device__ __forceinline__ float xrow_max(float x) {
  auto s = __builtin_amdgcn_permlane16_swap(__float_as_uint(x), __float_as_uint(x), false, false);
  x = fmaxf(__uint_as_float(s[0]), __uint_as_float(s[1]));
  auto t = __builtin_amdgcn_permlane32_swap(__float_as_uint(x), __float_as_uint(x), false, false);
  return fmaxf(__uint_as_float(t[0]), __uint_as_float(t[1]));
}
__device__ __forceinline__ f32x4 mfma(v4u a, v4u b, f32x4 c) {
  return __builtin_amdgcn_mfma_f32_16x16x32_bf16(__builtin_bit_cast(bf16x8, a), __builtin_bit_cast(bf16x8, b), c, 0, 0, 0);
}
__device__ __forceinline__ unsigned next_epoch(unsigned e) { return e * 0x9E3779B1u + 0x7F4A7C15u; }

struct Ctx {
  rsrc_t rq, rk, rv, ro, rw, rout;
  rsrc_t rx;
  v2u xv;
  v4u qg;
  unsigned t0;
  int q_st, q_ld, vo_x, so_kv, orot;
  unsigned ep0, touch;
  int ep_off;
  int w, lane, vo_q, vo_k, vo_v, vo_o0, vo_o1, dim4, ml_off;
  unsigned sel[4], tag;
  float* lds;
  v4u qb[N_Q], kb[N_K], vb[N_V], ob[N_O], qr[4], pp;
  f32x4 acc;
  float d0, d1, xs[8], s0, mw, Mwg, scale;
  v2u peer;
  unsigned long long stv[10];
  __device__ __forceinline__ v4u diag(unsigned pair) const {
    v4u a;
    a.x = __builtin_amdgcn_perm(0u, pair, sel[0]); a.y = __builtin_amdgcn_perm(0u, pair, sel[1]);
    a.z = __builtin_amdgcn_perm(0u, pair, sel[2]); a.w = __builtin_amdgcn_perm(0u, pair, sel[3]);
    return a;
  }
  __device__ __forceinline__ float treduce() {
    const bool b4 = lane & 4, b2 = lane & 2, b1 = lane & 1;
    float t[4], u[2];
#pragma unroll
    for (int h = 0; h < 4; ++h) { const float lo = xs[h] + dpp<XOR7>(xs[h]), hi = xs[h + 4] + dpp<XOR7>(xs[h + 4]); t[h] = b4 ? hi : lo; }
#pragma unroll
    for (int h = 0; h < 2; ++h) { const float lo = t[h] + dpp<XOR2>(t[h]), hi = t[h + 2] + dpp<XOR2>(t[h + 2]); u[h] = b2 ? hi : lo; }
    const float lo = u[0] + dpp<XOR1>(u[0]), hi = u[1] + dpp<XOR1>(u[1]);
    return b1 ? hi : lo;
  }
};

__device__ __forceinline__ bool peer_ok(const Ctx& c) { return __all(c.peer.y == c.tag); }

template <int I> __device__ __forceinline__ void step(Ctx& c) {
  constexpr int op = S.op[I], a = S.a[I];
  float* lds = c.lds;
  const int w = c.w, lane = c.lane;
  if constexpr (op == IQ) c.qb[a] = __builtin_amdgcn_raw_buffer_load_b128(c.rq, c.vo_q, (16 * w + a) * 8192, Q_AUX);
  else if constexpr (op == IK) c.kb[a] = __builtin_amdgcn_raw_buffer_load_b128(c.rk, c.vo_k + (a & 3) * 128, (c.so_kv + (a >> 2) * 32768) & 0x7ffff, KV_AUX);
  else if constexpr (op == IV) c.vb[a] = __builtin_amdgcn_raw_buffer_load_b128(c.rv, c.vo_v + (a & 3) * 1024, (c.so_kv + (a >> 2) * 32768) & 0x7ffff, KV_AUX);
  else if constexpr (op == IO) c.ob[a] = __builtin_amdgcn_raw_buffer_load_b128(c.ro, (a & 1) ? c.vo_o1 : c.vo_o0, (32 * w + (((a & ~1) + c.orot) & 31)) * (DM * 2), W_AUX);
  else if constexpr (op == WAITV) __builtin_amdgcn_s_waitcnt((a & 15) | ((a >> 4) << 14) | 0x0F70);
  else if constexpr (op == SETUP) {
    c.ep0 = __builtin_amdgcn_raw_buffer_load_b32(c.rw, c.ep_off, 0, AUX_SC1);
    c.touch = __builtin_amdgcn_raw_buffer_load_b32(c.rout, 0, 0, 0);
    c.xv = __builtin_amdgcn_raw_buffer_load_b64(c.rx, c.vo_x, 0, 0);
    const int row = lane & 15, j = row & 7, hl = lane >> 5;
    const bool act = ((lane >> 4) & 1) == (row >> 3);
    const unsigned two = hl ? 0x0302u : 0x0100u;
    const unsigned s = (j & 1) ? ((two << 16) | 0x0c0cu) : (0x0c0c0000u | two);
#pragma unroll
    for (int i = 0; i < 4; ++i) c.sel[i] = (act && (j >> 1) == i) ? s : 0x0c0c0c0cu;
    c.dim4 = 128 * hl + 8 * (lane & 15) + 4 * ((lane >> 4) & 1);
    c.acc = f32x4{0.f, 0.f, 0.f, 0.f};
  } else if constexpr (op == PACE) {
    const unsigned tgt = c.t0 + a;
    asm volatile("1:\n\ts_memrealtime vcc\n\ts_waitcnt lgkmcnt(0)\n\ts_sub_i32 vcc_lo, vcc_lo, %0\n\ts_cmp_lt_i32 vcc_lo, 0\n\ts_cbranch_scc1 1b"
                 :: "s"(tgt) : "scc", "vcc", "memory");
  } else if constexpr (op == QPUB) {
    const int j = lane & 7;
    f32x4 acc = {0.f, 0.f, 0.f, 0.f};
#pragma unroll
    for (int t = 0; t < N_Q; ++t) {
      const unsigned xw = (t & 1) ? (c.xv[t >> 1] >> 16) : (c.xv[t >> 1] & 0xffffu);
      const unsigned xs = (j & 1) ? (xw << 16) : xw;
      v4u am;
#pragma unroll
      for (int i = 0; i < 4; ++i) am[i] = (j >> 1) == i ? xs : 0u;
      acc = mfma(am, c.qb[t], acc);
    }
    ST(1);
    c.tag = next_epoch(c.ep0);
    asm volatile("" : "+v"(c.tag) : "v"(c.touch));
    *(f32x4*)&lds[L_RED + w * RED_STR + c.dim4] = acc;
    __syncthreads();
    f32x4 t = *(const f32x4*)&lds[L_RED + (lane & 7) * RED_STR + 32 * w + 4 * (lane >> 3)];
    t.x = sum8(t.x); t.y = sum8(t.y); t.z = sum8(t.z); t.w = sum8(t.w);
    const v4u g = {__float_as_uint((lane & 1) ? t.z : t.x), c.tag, __float_as_uint((lane & 1) ? t.w : t.y), c.tag};
    __builtin_amdgcn_raw_buffer_store_b128(g, c.rw, c.q_st, 0, AUX_SC1);
    ST(2);
  } else if constexpr (op == QLD) {
    c.qg = __builtin_amdgcn_raw_buffer_load_b128(c.rw, c.q_ld, 0, AUX_SC1);
  } else if constexpr (op == QCHK) {
    for (unsigned spins = 0; !__all((c.qg.y == c.tag) & (c.qg.w == c.tag)); ++spins) {
      if (spins > (1u << 16)) { c.qg.x = 0x7fc00000u; break; }
      asm volatile("" ::: "memory");
      c.qg = __builtin_amdgcn_raw_buffer_load_b128(c.rw, c.q_ld, 0, AUX_SC1);
    }
    const float q0 = xrow_sum(__uint_as_float(c.qg.x)), q1 = xrow_sum(__uint_as_float(c.qg.z));
    ((unsigned*)lds)[L_Q + 16 * w + (lane & 15)] = cvtpk(q0, q1);
    __syncthreads();
#pragma unroll
    for (int i = 0; i < 4; ++i) c.qr[i] = *(const v4u*)&lds[L_Q + 32 * i + 4 * (lane & 7)];
    ST(3);
  } else if constexpr (op == CK) {
    constexpr int i = a & 3;
    if constexpr (i == 0) { c.d0 = 0.f; c.d1 = 0.f; }
    const v4u k = c.kb[a], q = c.qr[i];
    c.d0 = dot2(k.x, q.x, c.d0); c.d1 = dot2(k.y, q.y, c.d1); c.d0 = dot2(k.z, q.z, c.d0); c.d1 = dot2(k.w, q.w, c.d1);
    if constexpr (i == 3) c.xs[(a >> 2) & 7] = c.d0 + c.d1;
    if constexpr (a == 31) c.s0 = c.treduce();
  } else if constexpr (op == SOFTMAX) {
    ST(4);
    constexpr float C2 = 0.0625f * 1.4426950408889634f;
    const float s0 = c.s0 * C2, s1 = c.treduce() * C2;
    float m = max8(fmaxf(s0, s1)); m = fmaxf(m, dpp<XOR15>(m)); m = xrow_max(m);
    const float p0 = __builtin_amdgcn_exp2f(s0 - m), p1 = __builtin_amdgcn_exp2f(s1 - m);
    __bf16* pw = (__bf16*)&lds[L_P + w * 64] + (lane & 7) * 8 + (lane >> 3);
    pw[0] = (__bf16)p0; pw[64] = (__bf16)p1;
    float l = sum8(p0 + p1); l += dpp<XOR15>(l); l = xrow_sum(l);
    lds[L_ML + 2 * w + (lane & 1)] = (lane & 1) ? l : m;
    c.mw = m;
    c.pp = *(const v4u*)&lds[L_P + w * 64];
  } else if constexpr (op == CV) {
    constexpr int i = a & 3;
    c.acc = mfma(c.diag(c.pp[i]), c.vb[a], c.acc);
    if constexpr (i == 3 && a + 1 < N_V) c.pp = *(const v4u*)&lds[L_P + w * 64 + (a + 1)];
  } else if constexpr (op == MLX) {
    __syncthreads();
    const f32x2 ml = *(const f32x2*)&lds[L_ML + 2 * (lane & 7)];
    c.Mwg = max8(ml.x);
    const float L = sum8(ml.y * __builtin_amdgcn_exp2f(ml.x - c.Mwg));
    const v4u g = {__float_as_uint(c.Mwg), c.tag, __float_as_uint(L), c.tag};
    __builtin_amdgcn_raw_buffer_store_b128(g, c.rw, (w == 0 && lane == 0) ? c.ml_off : OOB, 0, AUX_SC1);
  } else if constexpr (op == PEERLD) {
    c.peer = __builtin_amdgcn_raw_buffer_load_b64(c.rw, (c.ml_off & ~63) + (lane & 7) * 8, 0, AUX_SC1);
  } else if constexpr (op == PEERCHK) {
    for (unsigned spins = 0; !peer_ok(c); ++spins) {
      if (spins > (1u << 16)) { c.peer.x = 0x7fc00000u; break; }
      __builtin_amdgcn_s_sleep(1);
      asm volatile("" ::: "memory");
      c.peer = __builtin_amdgcn_raw_buffer_load_b64(c.rw, (c.ml_off & ~63) + (lane & 7) * 8, 0, AUX_SC1);
    }
    const float v = __uint_as_float(c.peer.x);
    const float vx = dpp<XOR1>(v);
    const float m = (lane & 1) ? vx : v, l = (lane & 1) ? v : 0.f;
    float M = fmaxf(m, dpp<XOR2>(m)); M = fmaxf(M, dpp<XOR7>(M));
    const float L = sum8(l * __builtin_amdgcn_exp2f(m - M));
    c.scale = __builtin_amdgcn_exp2f(c.mw - M) / L;
  }
  __builtin_amdgcn_sched_barrier(0);
}
template <int... I> __device__ __forceinline__ void run(Ctx& c, std::integer_sequence<int, I...>) { (step<I>(c), ...); }

__global__ void __launch_bounds__(NTHR, 2)
mha_decode(const __bf16* __restrict__ xA, const __bf16* __restrict__ kA, const __bf16* __restrict__ vA,
           const __bf16* __restrict__ wqA, const __bf16* __restrict__ woA,
           const __bf16* __restrict__ xB, const __bf16* __restrict__ kB, const __bf16* __restrict__ vB,
           const __bf16* __restrict__ wqB, const __bf16* __restrict__ woB,
           __bf16* __restrict__ out, unsigned char* __restrict__ ws) {
  __shared__ __attribute__((aligned(16))) float lds[L_END];
  Ctx c;
  for (int i = 0; i < 10; ++i) c.stv[i] = 0;
  ST(0);
  const int bid = blockIdx.x;
  const int rk = bid >> 3, bh = (bid & 7) * 4 + (rk >> 3), br = bh >> 4, head = bh & 15, b = (rk >> 2) & 1, split = rk & 3;
  const int grp = br * 2 + b, prod = head * 4 + split;
  const int tid = threadIdx.x, lane = tid & 63, w = __builtin_amdgcn_readfirstlane(tid >> 6);
  const __bf16* x = br ? xB : xA;
  const __bf16* kc = br ? kB : kA;
  const __bf16* vc = br ? vB : vA;
  const __bf16* wq = br ? wqB : wqA;
  const __bf16* wo = br ? woB : woA;
  const size_t kvoff = ((size_t)(b * NH + head) * SK + split * 1024) * DH;
  c.t0 = (unsigned)__builtin_amdgcn_s_memrealtime();
  c.rq = __builtin_amdgcn_make_buffer_rsrc((void*)(wq + (size_t)split * 112 * (NH * DH) + head * DH + b * 128), 0, 111 * 8192 + 256, RSRC_W3);
  c.rk = __builtin_amdgcn_make_buffer_rsrc((void*)(kc + kvoff), 0, 1024 * 512, RSRC_W3);
  c.rv = __builtin_amdgcn_make_buffer_rsrc((void*)(vc + kvoff), 0, 1024 * 512, RSRC_W3);
  c.ro = __builtin_amdgcn_make_buffer_rsrc((void*)(wo + head * DH * DM), 0, DH * DM * 2, RSRC_W3);
  c.rw = __builtin_amdgcn_make_buffer_rsrc((void*)ws, 0, WS_END, RSRC_W3);
  c.rout = __builtin_amdgcn_make_buffer_rsrc((void*)out, 0, 4 * DM * 2, RSRC_W3);
  const int hl = lane >> 5, l31 = lane & 31;
  c.w = w; c.lane = lane; c.lds = lds;
  c.vo_q = (lane >> 4) * (4 * 8192) + (lane & 15) * 16;
  c.vo_k = (lane >> 3) * 512 + (lane & 7) * 16;
  c.vo_v = lane * 16;
  c.orot = OROT ? 4 * (rk & 7) : 0;
  c.so_kv = w * 4096 + (ROT ? (head + 8 * br) & 15 : 0) * 32768;
  c.vo_o0 = hl * (DM * 2) + l31 * 16;
  c.vo_o1 = l31 < 24 ? c.vo_o0 + 512 : OOB;
  c.ml_off = WS_ML + (grp * 64 + prod) * 16;
  c.ep_off = br * 256;
  c.rx = __builtin_amdgcn_make_buffer_rsrc((void*)x, 0, 2 * DM * 2, RSRC_W3);
  c.vo_x = w < 7 ? (((lane >> 3) & 1) * DM + split * 112 + 16 * w + 4 * (lane >> 4)) * 2 : OOB;
  c.q_st = (lane & 7) < 2 ? WS_Q + bh * 16384 + (w >> 2) * 8192 + split * 2048 + (128 * b + 32 * (w & 3) + 4 * (lane >> 3) + 2 * (lane & 1)) * 8 : OOB;
  c.q_ld = WS_Q + bh * 16384 + b * 8192 + (lane >> 4) * 2048 + (32 * w + 2 * (lane & 15)) * 8;

  run(c, std::make_integer_sequence<int, S.n>{});

  ST(5);
  *(f32x4*)&lds[L_RED + w * RED_STR + c.dim4] = c.acc * c.scale;
  __syncthreads();
  ST(6);
  {
    f32x4 t = *(const f32x4*)&lds[L_RED + (lane & 7) * RED_STR + 32 * w + ((4 * (lane >> 3) + c.orot) & 31)];
    t.x = sum8(t.x); t.y = sum8(t.y); t.z = sum8(t.z); t.w = sum8(t.w);
    const unsigned o0 = cvtpk(t.x, t.y), o1 = cvtpk(t.z, t.w);
    f32x4 y0 = {0.f, 0.f, 0.f, 0.f}, y1 = {0.f, 0.f, 0.f, 0.f};
#pragma unroll
    for (int t2 = 0; t2 < 16; ++t2) {
      const unsigned pr = __builtin_amdgcn_readlane((t2 & 1) ? o1 : o0, 8 * (t2 >> 1));
      const v4u a = c.diag(pr);
      y0 = mfma(a, c.ob[2 * t2], y0);
      y1 = mfma(a, c.ob[2 * t2 + 1], y1);
    }
    *(f32x4*)&lds[L_Y + w * Y_STR + c.dim4] = y0;
    *(f32x4*)&lds[L_Y + w * Y_STR + 256 + c.dim4] = y1;
  }
  __syncthreads();
  ST(7);
  const int pk = WS_PKT + grp * 64 * PKT_BYTES;
  if (w < 4) {
    f32x2 s = {0.f, 0.f};
#pragma unroll
    for (int i = 0; i < NW; ++i) s += *(const f32x2*)&lds[L_Y + i * Y_STR + 2 * tid];
    const v4u g = {__float_as_uint(s.x), c.tag, __float_as_uint(s.y), c.tag};
    __builtin_amdgcn_raw_buffer_store_b128(g, c.rw, tid < DM / 2 ? pk + prod * PKT_BYTES + tid * 16 : OOB, 0, AUX_SC1);
    ST(8);
  } else if (w == NW - 1 && prod < DM / 8) {
    const int off = pk + (lane >> 2) * PKT_BYTES + prod * 64 + (lane & 3) * 16;
    v4u g[4];
    bool bad = false;
    for (unsigned spins = 0;; ++spins) {
      bool ok = true;
      asm volatile("" ::: "memory");
#pragma unroll
      for (int i = 0; i < 4; ++i) g[i] = __builtin_amdgcn_raw_buffer_load_b128(c.rw, off, i * 16 * PKT_BYTES, AUX_SC1);
#pragma unroll
      for (int i = 0; i < 4; ++i) ok &= (g[i].y == c.tag) & (g[i].w == c.tag);
      if (__all(ok)) break;
      if (spins > (1u << 16)) { bad = true; break; }
      __builtin_amdgcn_s_sleep(1);
    }
    float r0 = (__uint_as_float(g[0].x) + __uint_as_float(g[1].x)) + (__uint_as_float(g[2].x) + __uint_as_float(g[3].x));
    float r1 = (__uint_as_float(g[0].z) + __uint_as_float(g[1].z)) + (__uint_as_float(g[2].z) + __uint_as_float(g[3].z));
    r0 += dpp<ROR4>(r0); r1 += dpp<ROR4>(r1);
    r0 += dpp<ROR8>(r0); r1 += dpp<ROR8>(r1);
    r0 = xrow_sum(r0); r1 = xrow_sum(r1);
    const unsigned o = bad ? 0x7fc07fc0u : cvtpk(r0, r1);
    __builtin_amdgcn_raw_buffer_store_b32(o, c.rout, lane < 4 ? (b * (2 * DM) + br * DM + prod * 8 + lane * 2) * 2 : OOB, 0, 0);
    __builtin_amdgcn_raw_buffer_store_b32(c.tag, c.rw, (prod == 0 && b == 0 && lane == 0) ? c.ep_off : OOB, 0, AUX_SC1);
    ST(9);
  }
  if (w == NW - 1) {
    unsigned* sp = (unsigned*)(ws + (6u << 20));
    if (lane == 0) { __hip_atomic_store(sp + bid, (unsigned)c.stv[0], __ATOMIC_RELAXED, __HIP_MEMORY_SCOPE_AGENT);
                     __hip_atomic_store(sp + 256 + bid, (unsigned)c.stv[GP], __ATOMIC_RELAXED, __HIP_MEMORY_SCOPE_AGENT); }
    if (bid == 0) {
      const unsigned long long t1 = __builtin_amdgcn_s_memrealtime();
      while (__builtin_amdgcn_s_memrealtime() - t1 < 1000) __builtin_amdgcn_s_sleep(2);
      const unsigned ref = (unsigned)c.stv[0] - 100000;
      unsigned mn0 = ~0u, mn = ~0u, mx = 0, sum = 0, cnt = 0;
      for (int i = 0; i < 256; ++i) {
        const unsigned a = __hip_atomic_load(sp + i, __ATOMIC_RELAXED, __HIP_MEMORY_SCOPE_AGENT) - ref;
        const unsigned braw = __hip_atomic_load(sp + 256 + i, __ATOMIC_RELAXED, __HIP_MEMORY_SCOPE_AGENT);
        mn0 = a < mn0 ? a : mn0;
        if (braw) { const unsigned bb = braw - ref; mn = bb < mn ? bb : mn; mx = bb > mx ? bb : mx; sum += bb; ++cnt; }
      }
      const unsigned st = (GS == 0 ? mn : GS == 1 ? sum / cnt : mx) - mn0;
      const unsigned long long d = (unsigned long long)__builtin_amdgcn_readfirstlane(st) * GK + 30000;
      while (__builtin_amdgcn_s_memrealtime() - t1 < d) __builtin_amdgcn_s_sleep(2);
    }
  }
}

}

extern "C" void kernel_launch(void* const* d_in, const int* in_sizes, int n_in, void* d_out, int out_size, void* d_ws,
                              size_t ws_size, hipStream_t stream) {
  (void)in_sizes; (void)n_in; (void)out_size; (void)ws_size;
  mha_decode<<<256, NTHR, 0, stream>>>((const __bf16*)d_in[0], (const __bf16*)d_in[1], (const __bf16*)d_in[2],
                                       (const __bf16*)d_in[3], (const __bf16*)d_in[4], (const __bf16*)d_in[5],
                                       (const __bf16*)d_in[6], (const __bf16*)d_in[7], (const __bf16*)d_in[8],
                                       (const __bf16*)d_in[9], (__bf16*)d_out, (unsigned char*)d_ws);
}
```

```cpp
#include <hip/hip_runtime.h>
#include <hip/hip_bf16.h>
#include <stdint.h>
#include <utility>

#define WIN2 6
#define RO 2
#define O_START 28
#define TAU 0
#define TOFF 0
#define PACE_FROM 12
#define BLIND 0
#define NAP 9
#define STAG 1
#define N3 8
#define WIN3 8
#define QMM_AT 3
#define QPUB_AT 6
#define QLD_AT 14
#define QCHK_AT 18
#define QGET_AT 21
#define ROT 1
#define OROT 1
#define KV_AUX 2
#define W_AUX 0
#define Q_AUX 2

#define ST(k)

namespace {

typedef unsigned v4u __attribute__((ext_vector_type(4)));
typedef unsigned v2u __attribute__((ext_vector_type(2)));
typedef float f32x4 __attribute__((ext_vector_type(4)));
typedef float f32x2 __attribute__((ext_vector_type(2)));
typedef __bf16 bf16x8 __attribute__((ext_vector_type(8)));
typedef __bf16 bf16x2 __attribute__((ext_vector_type(2)));
typedef __amdgpu_buffer_rsrc_t rsrc_t;

constexpr int DM = 448, NH = 16, SK = 4096, DH = 256;
constexpr int NW = 8, NTHR = NW * 64;
constexpr int N_Q = 4, N_K = 64, N_V = 64, N_O = 32;
constexpr unsigned SPIN_CAP = 1u << 16;
constexpr int RSRC_W3 = 0x00020000, AUX_SC1 = 16, OOB = 0x40000000;
constexpr int WS_PKT = 4096, PKT_BYTES = DM * 8, WS_Q = WS_PKT + 256 * PKT_BYTES, WS_ML = WS_Q + 32 * 16384, ML_STR = 128,
              WS_END = WS_ML + 2048 * ML_STR;
constexpr int RED_STR = 260, Y_STR = 512;
constexpr int L_RED = 0, L_Y = L_RED + NW * RED_STR, L_Q = L_Y + NW * Y_STR, L_P = L_Q + 128, L_F = L_P + NW * 64,
              L_END = L_F + NW * 2;

enum Op : int { IQ, IK, IV, IO, QPUB, QLD, MLPUB, PEERLD, WAITV, PACE, QMM, QCHK, QGET, CK, SM1, SM2, CV, PEERCHK, SETUP, BAR0, SLEEP };
struct Sched { int n; int op[800]; int a[800]; };
constexpr Sched make_sched() {
  Sched s{};
  int kind[256] = {}, idx[256] = {}, n = 0;
  auto add = [&](int k, int i) { kind[n] = k; idx[n] = i; ++n; };
  for (int j = 0; j < N_Q; ++j) add(IQ, j);
  add(IK, 0);
  const int n0 = n;
  int o = 0, cnt = RO - 1;
  for (int j = 1; j < N_K + N_V; ++j) {
    if (j < N_K) add(IK, j); else add(IV, j - N_K);
    if (j >= O_START && ++cnt >= RO && o < N_O) { add(IO, o++); cnt = 0; }
  }
  while (o < N_O) add(IO, o++);
  int vmk[320] = {}, vmi[320] = {}, nvm = 0;
  int nkv = 0, st_vm = -1, st_kv = 0;
  auto emit = [&](int op, int a) { s.op[s.n] = op; s.a[s.n] = a; ++s.n;
    if (op == QPUB || op == MLPUB) { st_vm = nvm; st_kv = nkv; }
    if (op <= PEERLD) { vmk[nvm] = op; vmi[nvm] = a; ++nvm; }
    if (op == SETUP) for (int t = 0; t < 2; ++t) { vmk[nvm] = QLD; ++nvm; } };
  int nk = 0, nv = 0, cost = 0, lk = 0, lv = 0, sm = 0;
  bool qr = false;
  for (int i = 0; i < n; ++i) {
    if (i >= n0 && kind[i] != IO) {
      if (TAU && nkv >= PACE_FROM) emit(PACE, TOFF + cost * TAU / 128);
      const int win = ((nkv >= 127 - N3 ? WIN3 : WIN2) + (nkv & 1)) / 2;
      int p = nvm, seen = 0;
      while (p > 0 && seen < win) { --p; if (vmk[p] <= IV) ++seen; }
      if (st_vm >= 0 && st_vm < p && nkv - st_kv < BLIND) { if (!TAU) emit(SLEEP, NAP); }
      else {
        emit(WAITV, nvm - 1 - p);
        if (vmk[p] == IK) lk = vmi[p] + 1;
        if (vmk[p] == IV) { lk = N_K; lv = vmi[p] + 1; }
      }
      ++nkv;
    }
    emit(kind[i], idx[i]);
    cost += kind[i] == IO ? (idx[i] < 4 ? 7 : 0) : (kind[i] == IQ ? 7 : 8);
    if (i == n0 - 1) { emit(SETUP, 0); emit(BAR0, 0); }
    if (kind[i] == IO) continue;
    if (i + 1 < n && kind[i + 1] == IO) { ++i; emit(IO, idx[i]); cost += idx[i] < 4 ? 7 : 0; --i; }
    const bool skip = i + 1 < n && kind[i + 1] == IO;
    if (kind[i] == IK && idx[i] == QMM_AT) emit(QMM, 0);
    if (kind[i] == IK && idx[i] == QPUB_AT) emit(QPUB, 0);
    if (kind[i] == IK && idx[i] == QLD_AT) emit(QLD, 0);
    if (kind[i] == IK && idx[i] == QCHK_AT) emit(QCHK, 0);
    if (kind[i] == IK && idx[i] == QGET_AT) { emit(QGET, 0); qr = true; }
    else if (qr && nk < N_K) { for (int t = 0; t < 3 && nk < lk; ++t) emit(CK, nk++); }
    else if (nk == N_K && sm < 2) { emit(sm == 0 ? SM2 : MLPUB, 0); ++sm; }
    else if (sm == 2) { for (int t = 0; t < 3 && nv < lv; ++t) {
      emit(CV, nv++);
      if (nv == N_V - 20) emit(PEERLD, 0);
      if (nv == N_V - 10) emit(PEERCHK, 0); } }
    if (skip) ++i;
  }
  while (nv < N_V) emit(CV, nv++);
  return s;
}
constexpr Sched S = make_sched();

__device__ __forceinline__ float dot2(unsigned a, unsigned b, float c) {
  return __builtin_amdgcn_fdot2_f32_bf16(__builtin_bit_cast(bf16x2, a), __builtin_bit_cast(bf16x2, b), c, false);
}
__device__ __forceinline__ unsigned cvtpk(float lo, float hi) {
  bf16x2 r = {(__bf16)lo, (__bf16)hi};
  return __builtin_bit_cast(unsigned, r);
}
template <int CTRL> __device__ __forceinline__ float dpp(float x) {
  return __builtin_bit_cast(float, __builtin_amdgcn_mov_dpp(__builtin_bit_cast(int, x), CTRL, 0xf, 0xf, true));
}
constexpr int XOR1 = 0xB1, XOR2 = 0x4E, XOR7 = 0x141, XOR15 = 0x140, ROR4 = 0x124, ROR8 = 0x128;
__device__ __forceinline__ float sum8(float x) { x += dpp<XOR1>(x); x += dpp<XOR2>(x); x += dpp<XOR7>(x); return x; }
__device__ __forceinline__ float max8(float x) { x = fmaxf(x, dpp<XOR1>(x)); x = fmaxf(x, dpp<XOR2>(x)); x = fmaxf(x, dpp<XOR7>(x)); return x; }
template <int CTRL> __device__ __forceinline__ float tstep(float lo, float hi, bool sel) {
  const float a = lo + dpp<CTRL>(lo), b = hi + dpp<CTRL>(hi);
  return sel ? b : a;
}
__device__ __forceinline__ float xrow_sum(float x) {
  auto s = __builtin_amdgcn_permlane16_swap(__float_as_uint(x), __float_as_uint(x), false, false);
  x = __uint_as_float(s[0]) + __uint_as_float(s[1]);
  auto t = __builtin_amdgcn_permlane32_swap(__float_as_uint(x), __float_as_uint(x), false, false);
  return __uint_as_float(t[0]) + __uint_as_float(t[1]);
}
__device__ __forceinline__ float xrow_max(float x) {
  auto s = __builtin_amdgcn_permlane16_swap(__float_as_uint(x), __float_as_uint(x), false, false);
  x = fmaxf(__uint_as_float(s[0]), __uint_as_float(s[1]));
  auto t = __builtin_amdgcn_permlane32_swap(__float_as_uint(x), __float_as_uint(x), false, false);
  return fmaxf(__uint_as_float(t[0]), __uint_as_float(t[1]));
}
__device__ __forceinline__ f32x4 mfma(v4u a, v4u b, f32x4 c) {
  return __builtin_amdgcn_mfma_f32_16x16x32_bf16(__builtin_bit_cast(bf16x8, a), __builtin_bit_cast(bf16x8, b), c, 0, 0, 0);
}
__device__ __forceinline__ unsigned next_epoch(unsigned e) { return e * 0x9E3779B1u + 0x7F4A7C15u; }

struct Ctx {
  rsrc_t rq, rk, rv, ro, rw, rout;
  rsrc_t rx;
  v2u xv;
  v4u qg;
  unsigned t0;
  int q_st, q_ld, vo_x, so_kv, orot;
  unsigned ep0, touch;
  int ep_off;
  int w, lane, vo_q, vo_v, vo_o0, vo_o1, dim4, ml_off;
  unsigned sel[4], tag;
  float* lds;
  v4u qb[N_Q], kb[N_K], vb[N_V], ob[N_O], qr, pp;
  f32x4 acc;
  float xs[5], s0, s1, mw, scale;
  v4u peer;
  int split;
  __device__ __forceinline__ v4u diag(unsigned pair) const {
    v4u a;
    a.x = __builtin_amdgcn_perm(0u, pair, sel[0]); a.y = __builtin_amdgcn_perm(0u, pair, sel[1]);
    a.z = __builtin_amdgcn_perm(0u, pair, sel[2]); a.w = __builtin_amdgcn_perm(0u, pair, sel[3]);
    return a;
  }
};

__device__ __forceinline__ void await_tags(v4u& g, rsrc_t r, int off, unsigned tag) {
  if (__builtin_expect(!__all((g.y == tag) & (g.w == tag)), 0)) {
    unsigned spins = 0;
    bool ok;
#pragma unroll 1
    do {
      asm volatile("" ::: "memory");
      g = __builtin_amdgcn_raw_buffer_load_b128(r, off, 0, AUX_SC1);
      ok = __all((g.y == tag) & (g.w == tag));
    } while (!ok && ++spins < SPIN_CAP);
    if (!ok) g.x = 0x7fc00000u;
  }
}
typedef __attribute__((address_space(3))) volatile unsigned lds_flag;
__device__ __forceinline__ void flag_set(float* lds, int k, int w) {
  asm volatile("s_waitcnt lgkmcnt(0)" ::: "memory");
  ((lds_flag*)lds)[L_F + k * NW + w] = 1u;
}
__device__ __forceinline__ void flag_wait(float* lds, int k, int lane) {
  while (!__all(((lds_flag*)lds)[L_F + k * NW + (lane & 7)] != 0u)) {}
  asm volatile("" ::: "memory");
}

template <int I> __device__ __forceinline__ void step(Ctx& c) {
  constexpr int op = S.op[I], a = S.a[I];
  float* lds = c.lds;
  const int w = c.w, lane = c.lane;
  if constexpr (op == IQ) c.qb[a] = __builtin_amdgcn_raw_buffer_load_b128(c.rq, c.vo_q, (16 * w + a) * 8192, Q_AUX);
  else if constexpr (op == IK) c.kb[a] = __builtin_amdgcn_raw_buffer_load_b128(c.rk, c.vo_v + (a & 3) * 1024, (c.so_kv + (a >> 2) * 32768) & 0x7ffff, KV_AUX);
  else if constexpr (op == IV) c.vb[a] = __builtin_amdgcn_raw_buffer_load_b128(c.rv, c.vo_v + (a & 3) * 1024, (c.so_kv + (a >> 2) * 32768) & 0x7ffff, KV_AUX);
  else if constexpr (op == IO) c.ob[a] = __builtin_amdgcn_raw_buffer_load_b128(c.ro, (a & 1) ? c.vo_o1 : c.vo_o0, (32 * w + (((a & ~1) + c.orot) & 31)) * (DM * 2), W_AUX);
  else if constexpr (op == WAITV) __builtin_amdgcn_s_waitcnt((a & 15) | ((a >> 4) << 14) | 0x0F70);
  else if constexpr (op == SETUP) {
    const int row = lane & 15, j = row & 7, hl = lane >> 5;
    const bool act = ((lane >> 4) & 1) == (row >> 3);
    const unsigned two = hl ? 0x0302u : 0x0100u;
    const unsigned s = (j & 1) ? ((two << 16) | 0x0c0cu) : (0x0c0c0000u | two);
#pragma unroll
    for (int i = 0; i < 4; ++i) c.sel[i] = (act && (j >> 1) == i) ? s : 0x0c0c0c0cu;
    c.dim4 = 128 * hl + 8 * (lane & 15) + 4 * ((lane >> 4) & 1);
    c.acc = f32x4{0.f, 0.f, 0.f, 0.f};
    ((unsigned*)lds)[L_F + (lane & 1) * NW + w] = 0u;
  } else if constexpr (op == PACE) {
    const unsigned tgt = c.t0 + a;
    asm volatile("1:\n\ts_memrealtime vcc\n\ts_waitcnt lgkmcnt(0)\n\ts_sub_i32 vcc_lo, vcc_lo, %0\n\ts_cmp_lt_i32 vcc_lo, 0\n\ts_cbranch_scc1 1b"
                 :: "s"(tgt) : "scc", "vcc", "memory");
  } else if constexpr (op == SLEEP) {
    __builtin_amdgcn_s_sleep(a);
  } else if constexpr (op == BAR0) {
    __syncthreads();
  } else if constexpr (op == QMM) {
    const int j = lane & 7;
    f32x4 acc = {0.f, 0.f, 0.f, 0.f};
#pragma unroll
    for (int t = 0; t < N_Q; ++t) {
      const unsigned xw = (t & 1) ? (c.xv[t >> 1] >> 16) : (c.xv[t >> 1] & 0xffffu);
      const unsigned xs = (j & 1) ? (xw << 16) : xw;
      v4u am;
#pragma unroll
      for (int i = 0; i < 4; ++i) am[i] = (j >> 1) == i ? xs : 0u;
      acc = mfma(am, c.qb[t], acc);
    }
    c.tag = next_epoch(c.ep0);
    asm volatile("" : "+v"(c.tag));
    *(f32x4*)&lds[L_RED + w * RED_STR + c.dim4] = acc;
    flag_set(lds, 0, w);
    ST(1);
  } else if constexpr (op == QPUB) {
    flag_wait(lds, 0, lane);
    f32x4 t = *(const f32x4*)&lds[L_RED + (lane & 7) * RED_STR + 32 * w + 4 * (lane >> 3)];
    t.x = sum8(t.x); t.y = sum8(t.y); t.z = sum8(t.z); t.w = sum8(t.w);
    const v4u g = {__float_as_uint((lane & 1) ? t.z : t.x), c.tag, __float_as_uint((lane & 1) ? t.w : t.y), c.tag};
    __builtin_amdgcn_raw_buffer_store_b128(g, c.rw, c.q_st, 0, AUX_SC1);
    ST(2);
  } else if constexpr (op == QLD) {
    c.qg = __builtin_amdgcn_raw_buffer_load_b128(c.rw, c.q_ld, 0, AUX_SC1);
  } else if constexpr (op == QCHK) {
    await_tags(c.qg, c.rw, c.q_ld, c.tag);
    const float q0 = xrow_sum(__uint_as_float(c.qg.x)), q1 = xrow_sum(__uint_as_float(c.qg.z));
    ((unsigned*)lds)[L_Q + 16 * w + (lane & 15)] = cvtpk(q0, q1);
    flag_set(lds, 1, w);
  } else if constexpr (op == QGET) {
    flag_wait(lds, 1, lane);
    c.qr = *(const v4u*)&lds[L_Q + 4 * (lane & 31)];
    ST(3);
  } else if constexpr (op == CK) {
    const v4u k = c.kb[a], q = c.qr;
    float x = dot2(k.x, q.x, dot2(k.z, q.z, 0.f)) + dot2(k.y, q.y, dot2(k.w, q.w, 0.f));
    if constexpr ((a & 1) == 0) c.xs[0] = x;
    else {
      const auto sw = __builtin_amdgcn_permlane16_swap(__float_as_uint(c.xs[0]), __float_as_uint(x), false, false);
      x = __uint_as_float(sw[0]) + __uint_as_float(sw[1]);
      if constexpr ((a & 2) == 0) c.xs[1] = x;
      else {
        x = tstep<ROR8>(c.xs[1], x, lane & 8);
        if constexpr ((a & 4) == 0) c.xs[2] = x;
        else {
          x = tstep<XOR7>(c.xs[2], x, lane & 4);
          if constexpr ((a & 8) == 0) c.xs[3] = x;
          else {
            x = tstep<XOR2>(c.xs[3], x, lane & 2);
            if constexpr ((a & 16) == 0) c.xs[4] = x;
            else {
              x = tstep<XOR1>(c.xs[4], x, lane & 1) * (0.0625f * 1.4426950408889634f);
              if constexpr (a == 31) c.s0 = x; else c.s1 = x;
            }
          }
        }
      }
    }
  } else if constexpr (op == SM2) {
    ST(4);
    float m = max8(fmaxf(c.s0, c.s1)); m = fmaxf(m, dpp<XOR15>(m)); m = xrow_max(m);
    const float p0 = __builtin_amdgcn_exp2f(c.s0 - m), p1 = __builtin_amdgcn_exp2f(c.s1 - m);
    __bf16* pw = (__bf16*)&lds[L_P + w * 64] + (__builtin_bitreverse32(lane) >> 26);
    pw[0] = (__bf16)p0; pw[64] = (__bf16)p1;
    c.mw = m; c.s0 = p0 + p1;
    c.pp = *(const v4u*)&lds[L_P + w * 64];
  } else if constexpr (op == MLPUB) {
    float l = sum8(c.s0); l += dpp<XOR15>(l); l = xrow_sum(l);
    const v4u g = {__float_as_uint(c.mw), c.tag, __float_as_uint(l), c.tag};
    __builtin_amdgcn_raw_buffer_store_b128(g, c.rw, lane < ML_STR / 16 ? c.ml_off + (c.split * NW + w) * ML_STR + lane * 16 : OOB, 0, AUX_SC1);
  } else if constexpr (op == CV) {
    constexpr int i = a & 3;
    c.acc = mfma(c.diag(c.pp[i]), c.vb[a], c.acc);
    if constexpr (i == 3 && a + 1 < N_V) c.pp = *(const v4u*)&lds[L_P + w * 64 + (a + 1)];
  } else if constexpr (op == PEERLD) {
    c.peer = __builtin_amdgcn_raw_buffer_load_b128(c.rw, c.ml_off + (lane & 31) * ML_STR, 0, AUX_SC1);
  } else if constexpr (op == PEERCHK) {
    await_tags(c.peer, c.rw, c.ml_off + (lane & 31) * ML_STR, c.tag);
    const float m = __uint_as_float(c.peer.x);
    float M = max8(m); M = fmaxf(M, dpp<XOR15>(M)); M = xrow_max(M);
    float L = sum8(__uint_as_float(c.peer.z) * __builtin_amdgcn_exp2f(m - M)); L += dpp<XOR15>(L); L = xrow_sum(L);
    c.scale = 2.f * __builtin_amdgcn_exp2f(c.mw - M) / L;
  }
  __builtin_amdgcn_sched_barrier(0);
}
template <int... I> __device__ __forceinline__ void run(Ctx& c, std::integer_sequence<int, I...>) { (step<I>(c), ...); }
template <int B, int... I> __device__ __forceinline__ void run_from(Ctx& c, std::integer_sequence<int, I...>) { (step<B + I>(c), ...); }
constexpr int N_FIRST = N_Q + 1;

__global__ void __launch_bounds__(NTHR, 2)
mha_decode(const __bf16* __restrict__ wqA, const __bf16* __restrict__ wqB, const __bf16* __restrict__ kA,
           const __bf16* __restrict__ kB, const __bf16* __restrict__ xA, const __bf16* __restrict__ xB, unsigned char* ws,
           const __bf16* __restrict__ vA, const __bf16* __restrict__ vB, const __bf16* __restrict__ woA,
           const __bf16* __restrict__ woB, __bf16* out) {
  __shared__ __attribute__((aligned(16))) float lds[L_END];
  Ctx c;
  ST(0);
  const int bid = blockIdx.x;
  const int rk = bid >> 3, bh = (bid & 7) * 4 + (rk >> 3), br = bh >> 4, head = bh & 15, b = (rk >> 2) & 1, split = rk & 3;
  const int grp = br * 2 + b, prod = head * 4 + split;
  const int tid = threadIdx.x, lane = tid & 63, w = __builtin_amdgcn_readfirstlane(tid >> 6);
  c.t0 = TAU ? (unsigned)__builtin_amdgcn_s_memrealtime() + STAG * w * TAU / 128 : 0u;
  const size_t kvoff = ((size_t)(b * NH + head) * SK + split * 1024) * DH;
  c.rq = __builtin_amdgcn_make_buffer_rsrc((void*)((br ? wqB : wqA) + (size_t)split * 112 * (NH * DH) + head * DH + b * 128), 0, 111 * 8192 + 256, RSRC_W3);
  c.rk = __builtin_amdgcn_make_buffer_rsrc((void*)((br ? kB : kA) + kvoff), 0, 1024 * 512, RSRC_W3);
  c.w = w; c.lane = lane; c.lds = lds;
  c.vo_q = (lane >> 4) * (4 * 8192) + (lane & 15) * 16;
  c.vo_v = lane * 16;
  c.so_kv = w * 4096 + (ROT == 1 ? (head + 8 * br) & 15 : ROT == 2 ? split * 4 : 0) * 32768;
  run(c, std::make_integer_sequence<int, N_FIRST>{});
  c.rx = __builtin_amdgcn_make_buffer_rsrc((void*)(br ? xB : xA), 0, 2 * DM * 2, RSRC_W3);
  c.rw = __builtin_amdgcn_make_buffer_rsrc((void*)ws, 0, WS_END, RSRC_W3);
  c.vo_x = ((((lane >> 3) & 1) * DM + split * 112 + 16 * w + 4 * (lane >> 4)) * 2) | (w < 7 ? 0 : OOB);
  c.ep_off = br * 256;
  c.xv = __builtin_amdgcn_raw_buffer_load_b64(c.rx, c.vo_x, 0, 0);
  c.ep0 = __builtin_amdgcn_raw_buffer_load_b32(c.rw, c.ep_off, 0, AUX_SC1);
  __builtin_amdgcn_sched_barrier(0);
  int br2 = br;
  asm volatile("" : "+s"(br2));
  c.rv = __builtin_amdgcn_make_buffer_rsrc((void*)((br2 ? vB : vA) + kvoff), 0, 1024 * 512, RSRC_W3);
  c.ro = __builtin_amdgcn_make_buffer_rsrc((void*)((br2 ? woB : woA) + head * DH * DM), 0, DH * DM * 2, RSRC_W3);
  c.rout = __builtin_amdgcn_make_buffer_rsrc((void*)out, 0, 4 * DM * 2, RSRC_W3);
  const int hl = lane >> 5, l31 = lane & 31;
  c.orot = OROT ? 4 * (rk & 7) : 0;
  c.vo_o0 = hl * (DM * 2) + l31 * 16;
  c.vo_o1 = l31 < 24 ? c.vo_o0 + 512 : OOB;
  c.ml_off = WS_ML + (grp * 16 + head) * 32 * ML_STR;
  c.split = split;
  c.q_st = (WS_Q + bh * 16384 + (w >> 2) * 8192 + split * 2048 + (128 * b + 32 * (w & 3) + 4 * (lane >> 3) + 2 * (lane & 1)) * 8) | ((lane & 6) << 28);
  c.q_ld = WS_Q + bh * 16384 + b * 8192 + (lane >> 4) * 2048 + (32 * w + 2 * (lane & 15)) * 8;
  run_from<N_FIRST>(c, std::make_integer_sequence<int, S.n - N_FIRST>{});

  ST(5);
  *(f32x4*)&lds[L_RED + w * RED_STR + c.dim4] = c.acc * c.scale;
  __syncthreads();
  ST(6);
  {
    f32x4 t = *(const f32x4*)&lds[L_RED + (lane & 7) * RED_STR + 32 * w + ((4 * (lane >> 3) + c.orot) & 31)];
    t.x = sum8(t.x); t.y = sum8(t.y); t.z = sum8(t.z); t.w = sum8(t.w);
    const unsigned o0 = cvtpk(t.x, t.y), o1 = cvtpk(t.z, t.w);
    f32x4 y0 = {0.f, 0.f, 0.f, 0.f}, y1 = {0.f, 0.f, 0.f, 0.f};
#pragma unroll
    for (int t2 = 0; t2 < 16; ++t2) {
      const unsigned pr = __builtin_amdgcn_readlane((t2 & 1) ? o1 : o0, 8 * (t2 >> 1));
      const v4u a = c.diag(pr);
      y0 = mfma(a, c.ob[2 * t2], y0);
      y1 = mfma(a, c.ob[2 * t2 + 1], y1);
    }
    *(f32x4*)&lds[L_Y + w * Y_STR + c.dim4] = y0;
    *(f32x4*)&lds[L_Y + w * Y_STR + 256 + c.dim4] = y1;
  }
  __syncthreads();
  ST(7);
  const int pk = WS_PKT + grp * 64 * PKT_BYTES;
  if (w < 4) {
    f32x2 s = {0.f, 0.f};
#pragma unroll
    for (int i = 0; i < NW; ++i) s += *(const f32x2*)&lds[L_Y + i * Y_STR + 2 * tid];
    const v4u g = {__float_as_uint(s.x), c.tag, __float_as_uint(s.y), c.tag};
    __builtin_amdgcn_raw_buffer_store_b128(g, c.rw, tid < DM / 2 ? pk + prod * PKT_BYTES + tid * 16 : OOB, 0, AUX_SC1);
    ST(8);
  } else if (w == NW - 1 && prod < DM / 8) {
    const int off = pk + (lane >> 2) * PKT_BYTES + prod * 64 + (lane & 3) * 16;
    v4u g[4];
    bool bad = false;
#pragma unroll 1
    for (unsigned spins = 0;; ++spins) {
      bool ok = true;
      asm volatile("" ::: "memory");
#pragma unroll
      for (int i = 0; i < 4; ++i) g[i] = __builtin_amdgcn_raw_buffer_load_b128(c.rw, off, i * 16 * PKT_BYTES, AUX_SC1);
#pragma unroll
      for (int i = 0; i < 4; ++i) ok &= (g[i].y == c.tag) & (g[i].w == c.tag);
      if (__all(ok)) break;
      if (spins > (1u << 16)) { bad = true; break; }
      __builtin_amdgcn_s_sleep(1);
    }
    float r0 = (__uint_as_float(g[0].x) + __uint_as_float(g[1].x)) + (__uint_as_float(g[2].x) + __uint_as_float(g[3].x));
    float r1 = (__uint_as_float(g[0].z) + __uint_as_float(g[1].z)) + (__uint_as_float(g[2].z) + __uint_as_float(g[3].z));
    r0 += dpp<ROR4>(r0); r1 += dpp<ROR4>(r1);
    r0 += dpp<ROR8>(r0); r1 += dpp<ROR8>(r1);
    r0 = xrow_sum(r0); r1 = xrow_sum(r1);
    const unsigned o = bad ? 0x7fc07fc0u : cvtpk(r0, r1);
    __builtin_amdgcn_raw_buffer_store_b32(o, c.rout, lane < 4 ? (b * (2 * DM) + br * DM + prod * 8 + lane * 2) * 2 : OOB, 0, 0);
    __builtin_amdgcn_raw_buffer_store_b32(c.tag, c.rw, (prod == 0 && b == 0 && lane == 0) ? c.ep_off : OOB, 0, AUX_SC1);
    ST(9);
  }
}

}

extern "C" void kernel_launch(void* const* d_in, const int* in_sizes, int n_in, void* d_out, int out_size, void* d_ws,
                              size_t ws_size, hipStream_t stream) {
  (void)in_sizes; (void)n_in; (void)out_size; (void)ws_size;
  mha_decode<<<256, NTHR, 0, stream>>>((const __bf16*)d_in[3], (const __bf16*)d_in[8], (const __bf16*)d_in[1],
                                       (const __bf16*)d_in[6], (const __bf16*)d_in[0], (const __bf16*)d_in[5],
                                       (unsigned char*)d_ws, (const __bf16*)d_in[2], (const __bf16*)d_in[7],
                                       (const __bf16*)d_in[4], (const __bf16*)d_in[9], (__bf16*)d_out);
}
```

```cpp
#include <hip/hip_runtime.h>
#include <hip/hip_bf16.h>
#include <stdint.h>
#include <utility>

#define WIN2 6
#define RO 2
#define O_START 40
#define TAU 0
#define TOFF 0
#define PACE_FROM 12
#define BLIND 0
#define NAP 9
#define STAG 1
#define N3 8
#define WIN3 8
#define YST_AUX 16
#define YLD_AUX 16
#define POLLD 1
#define POLLS 1
#define QSP 0
#define NF 5
#define BOOST_AT 6
#define BOOST_MAX 40
#define BOOST_T 340
#define BOOST_G 3
#define QMM_AT 3
#define QPUB_AT 6
#define QLD_AT 14
#define QCHK_AT 18
#define QGET_AT 21
#define ROT 2
#define OROT 1
#define KV_AUX 2
#define W_AUX 0
#define Q_AUX 2

#define ST(k)

namespace {

typedef unsigned v4u __attribute__((ext_vector_type(4)));
typedef unsigned v2u __attribute__((ext_vector_type(2)));
typedef float f32x4 __attribute__((ext_vector_type(4)));
typedef float f32x2 __attribute__((ext_vector_type(2)));
typedef __bf16 bf16x8 __attribute__((ext_vector_type(8)));
typedef __bf16 bf16x2 __attribute__((ext_vector_type(2)));
typedef __bf16 bf16x16 __attribute__((ext_vector_type(16)));
typedef short i16x4 __attribute__((ext_vector_type(4)));
typedef unsigned v8u __attribute__((ext_vector_type(8)));
typedef __amdgpu_buffer_rsrc_t rsrc_t;

constexpr int DM = 448, NH = 16, SK = 4096, DH = 256;
constexpr int NW = 8, NTHR = NW * 64;
constexpr int N_Q = 4, N_K = 64, N_V = 64, N_O = 32;
constexpr unsigned SPIN_CAP = 1u << 16;
constexpr int RSRC_W3 = 0x00020000, AUX_SC1 = 16, OOB = 0x40000000;
constexpr int WS_PKT = 4096, PKT_BYTES = DM * 4, WS_Q = WS_PKT + 256 * PKT_BYTES, WS_ML = WS_Q + 64 * 1024,
              WS_END = WS_ML + 64 * 128;
constexpr int RED_STR = 260, A_STR = 68;
constexpr int L_RED = 0, L_T = L_RED + NW * RED_STR, L_ML = L_T + NW * 1024, L_P = L_ML + 2 * NW, L_F = L_P + NW * 64,
              L_END = L_F + NW * 2;

constexpr int N_FIRST = NF;
enum Op : int { IQ, IK, IV, IO, QPUB, QLD, MLPUB, PEERLD, WAITV, PACE, QMM, QCHK, QTR, CK, SM3, SM2, CV, PEERCHK, SETUP, BAR0, SLEEP, TRW, TRR, WAITB, LAG };
struct Sched { int n; int op[800]; int a[800]; };
constexpr Sched make_sched() {
  Sched s{};
  int kind[256] = {}, idx[256] = {}, n = 0;
  auto add = [&](int k, int i) { kind[n] = k; idx[n] = i; ++n; };
  const int n0 = N_FIRST;
  int o = 0, cnt = RO - 1, nq = 0;
  for (int j = 0; j < N_K + N_V; ++j) {
    while (nq < N_Q && nq * QSP <= j) add(IQ, nq++);
    if (j < N_K) add(IK, j); else add(IV, j - N_K);
    if (j >= O_START && ++cnt >= RO && o < N_O) { add(IO, o++); cnt = 0; }
  }
  while (o < N_O) add(IO, o++);
  int vmk[320] = {}, vmi[320] = {}, nvm = 0;
  int nkv = 0, st_vm = -1, st_kv = 0;
  auto emit = [&](int op, int a) { s.op[s.n] = op; s.a[s.n] = a; ++s.n;
    if (op == QPUB || op == MLPUB) { st_vm = nvm; st_kv = nkv; }
    if (op <= PEERLD) { vmk[nvm] = op; vmi[nvm] = a; ++nvm; }
    if (op == SETUP) for (int t = 0; t < 2; ++t) { vmk[nvm] = QLD; ++nvm; } };
  int nk = 0, nv = 0, cost = 0, lk = 0, lv = 0, lo = 0, nw = 0, nr = 0, sm = 0;
  bool qr = false;
  for (int i = 0; i < n; ++i) {
    if (i >= n0 && kind[i] != IO) {
      if (TAU && nkv >= PACE_FROM) emit(PACE, TOFF + cost * TAU / 128);
      const int win = ((nkv >= 127 - N3 ? WIN3 : WIN2) + (nkv & 1)) / 2;
      int p = nvm, seen = 0;
      while (p > 0 && seen < win) { --p; if (vmk[p] <= IV) ++seen; }
      if (st_vm >= 0 && st_vm < p && nkv - st_kv < BLIND) { if (!TAU) emit(SLEEP, NAP); }
      else {
        if (BOOST_AT >= 0 && nkv > BOOST_AT && nkv <= BOOST_AT + BOOST_MAX) {
          int p2 = p; do --p2; while (vmk[p2] > IV);
          emit(WAITV, nvm - 1 - p2);
          emit(WAITB, (nvm - 1 - p) | ((nkv - BOOST_AT) << 8));
          p = p2;
        } else emit(WAITV, nvm - 1 - p);
        if (nkv == BOOST_AT) emit(LAG, 0);
        if (vmk[p] == IK) lk = vmi[p] + 1;
        if (vmk[p] == IV) { lk = N_K; lv = vmi[p] + 1; }
        for (int t = 0; t < p; ++t) if (vmk[t] == IO) lo = vmi[t] + 1;
      }
      ++nkv;
    }
    emit(kind[i], idx[i]);
    cost += kind[i] == IO ? (idx[i] < 4 ? 7 : 0) : (kind[i] == IQ ? 7 : 8);
    if (i == n0 - 1) { emit(SETUP, 0); emit(BAR0, 0); }
    if (kind[i] == IO) continue;
    if (i + 1 < n && kind[i + 1] == IO) { ++i; emit(IO, idx[i]); cost += idx[i] < 4 ? 7 : 0; --i; }
    const bool skip = i + 1 < n && kind[i + 1] == IO;
    if (kind[i] == IK && idx[i] == QMM_AT - 1) emit(QTR, 0);
    if (kind[i] == IK && idx[i] == QMM_AT) emit(QMM, 0);
    if (kind[i] == IK && idx[i] == QPUB_AT) emit(QPUB, 0);
    if (kind[i] == IK && idx[i] == QLD_AT) emit(QLD, 0);
    if (kind[i] == IK && idx[i] == QCHK_AT) { emit(QCHK, 0); qr = true; }
    else if (qr && nk < N_K) { for (int t = 0; t < 3 && nk < lk; ++t) emit(CK, nk++); }
    else if (nk == N_K && sm < 2) { emit(sm == 0 ? SM2 : SM3, 0); ++sm; }
    else if (sm == 2) { for (int t = 0; t < 3 && nv < lv; ++t) {
      emit(CV, nv++);
      if (nv == 12) emit(MLPUB, 0);
      if (nv == N_V - 20) emit(PEERLD, 0);
      if (nv == N_V - 10) emit(PEERCHK, 0); } }
    if (nr < nw) emit(TRR, nr++);
    else if (4 * nw + 4 <= lo) emit(TRW, nw++);
    if (skip) ++i;
  }
  while (nr < N_O / 4) { if (nr == nw) emit(TRW, nw++); emit(TRR, nr++); }
  while (nv < N_V) emit(CV, nv++);
  return s;
}
constexpr Sched S = make_sched();

__device__ __forceinline__ float dot2(unsigned a, unsigned b, float c) {
  return __builtin_amdgcn_fdot2_f32_bf16(__builtin_bit_cast(bf16x2, a), __builtin_bit_cast(bf16x2, b), c, false);
}
__device__ __forceinline__ unsigned cvtpk(float lo, float hi) {
  bf16x2 r = {(__bf16)lo, (__bf16)hi};
  return __builtin_bit_cast(unsigned, r);
}
template <int CTRL> __device__ __forceinline__ float dpp(float x) {
  return __builtin_bit_cast(float, __builtin_amdgcn_mov_dpp(__builtin_bit_cast(int, x), CTRL, 0xf, 0xf, true));
}
constexpr int XOR1 = 0xB1, XOR2 = 0x4E, XOR7 = 0x141, XOR15 = 0x140, ROR4 = 0x124, ROR8 = 0x128;
__device__ __forceinline__ float sum8(float x) { x += dpp<XOR1>(x); x += dpp<XOR2>(x); x += dpp<XOR7>(x); return x; }
__device__ __forceinline__ float max8(float x) { x = fmaxf(x, dpp<XOR1>(x)); x = fmaxf(x, dpp<XOR2>(x)); x = fmaxf(x, dpp<XOR7>(x)); return x; }
template <int CTRL> __device__ __forceinline__ float tstep(float lo, float hi, bool sel) {
  const float a = lo + dpp<CTRL>(lo), b = hi + dpp<CTRL>(hi);
  return sel ? b : a;
}
__device__ __forceinline__ float xrow_sum(float x) {
  auto s = __builtin_amdgcn_permlane16_swap(__float_as_uint(x), __float_as_uint(x), false, false);
  x = __uint_as_float(s[0]) + __uint_as_float(s[1]);
  auto t = __builtin_amdgcn_permlane32_swap(__float_as_uint(x), __float_as_uint(x), false, false);
  return __uint_as_float(t[0]) + __uint_as_float(t[1]);
}
__device__ __forceinline__ float xrow_max(float x) {
  auto s = __builtin_amdgcn_permlane16_swap(__float_as_uint(x), __float_as_uint(x), false, false);
  x = fmaxf(__uint_as_float(s[0]), __uint_as_float(s[1]));
  auto t = __builtin_amdgcn_permlane32_swap(__float_as_uint(x), __float_as_uint(x), false, false);
  return fmaxf(__uint_as_float(t[0]), __uint_as_float(t[1]));
}
__device__ __forceinline__ f32x4 mfma(v4u a, v4u b, f32x4 c) {
  return __builtin_amdgcn_mfma_f32_16x16x32_bf16(__builtin_bit_cast(bf16x8, a), __builtin_bit_cast(bf16x8, b), c, 0, 0, 0);
}
__device__ __forceinline__ unsigned next_epoch(unsigned e) {
  const unsigned t = e * 0x9E3779B1u + 0x7F4A7C15u;
  return t ? t : 0x7F4A7C15u;
}

struct Ctx {
  rsrc_t rq, rk, rv, ro, rw, rout;
  rsrc_t rx;
  v4u xv[2];
  v2u qf[2][2][2];
  v4u qg;
  unsigned t0;
  int q_st, q_ld, vo_x, so_kv, orot;
  unsigned ep0, touch;
  int ep_off;
  int w, lane, vo_q, vo_v, vo_o, dim4, ml_off;
  unsigned sel[4], tag;
  float* lds;
  v4u qb[N_Q], kb[N_K], vb[N_V], ob[N_O], qr, pp;
  v2u of[8][4][2];
  f32x4 acc;
  float xs[5], s0, s1, mw, scale;
  v4u peer;
  int ml_st, boost;
  __device__ __forceinline__ v4u diag(unsigned pair) const {
    v4u a;
    a.x = __builtin_amdgcn_perm(0u, pair, sel[0]); a.y = __builtin_amdgcn_perm(0u, pair, sel[1]);
    a.z = __builtin_amdgcn_perm(0u, pair, sel[2]); a.w = __builtin_amdgcn_perm(0u, pair, sel[3]);
    return a;
  }
};

__device__ __forceinline__ void await_tags(v4u& g, rsrc_t r, int off, unsigned tag) {
  if (__builtin_expect(!__all((g.y == tag) & (g.w == tag)), 0)) {
    unsigned spins = 0;
    bool ok;
#pragma unroll 1
    do {
      asm volatile("" ::: "memory");
      g = __builtin_amdgcn_raw_buffer_load_b128(r, off, 0, AUX_SC1);
      ok = __all((g.y == tag) & (g.w == tag));
    } while (!ok && ++spins < SPIN_CAP);
    if (!ok) g.x = 0x7fc00000u;
  }
}
typedef __attribute__((address_space(3))) volatile unsigned lds_flag;
__device__ __forceinline__ void flag_set(float* lds, int k, int w) {
  asm volatile("s_waitcnt lgkmcnt(0)" ::: "memory");
  ((lds_flag*)lds)[L_F + k * NW + w] = 1u;
}
__device__ __forceinline__ void flag_wait(float* lds, int k, int lane) {
  while (!__all(((lds_flag*)lds)[L_F + k * NW + (lane & 7)] != 0u)) {}
  asm volatile("" ::: "memory");
}

template <int I> __device__ __forceinline__ void step(Ctx& c) {
  constexpr int op = S.op[I], a = S.a[I];
  float* lds = c.lds;
  const int w = c.w, lane = c.lane;
  if constexpr (op == IQ) c.qb[a] = __builtin_amdgcn_raw_buffer_load_b128(c.rq, c.vo_q, (64 * w + 16 * a) * 8192, Q_AUX);
  else if constexpr (op == IK) c.kb[a] = __builtin_amdgcn_raw_buffer_load_b128(c.rk, c.vo_v + (a & 3) * 1024, (c.so_kv + (a >> 2) * 32768) & 0x7ffff, KV_AUX);
  else if constexpr (op == IV) c.vb[a] = __builtin_amdgcn_raw_buffer_load_b128(c.rv, c.vo_v + (a & 3) * 1024, (c.so_kv + (a >> 2) * 32768) & 0x7ffff, KV_AUX);
  else if constexpr (op == IO) c.ob[a] = __builtin_amdgcn_raw_buffer_load_b128(c.ro, c.vo_o, ((8 * a + 32 * c.orot) & 255) * (DM * 2), W_AUX);
  else if constexpr (op == WAITV) __builtin_amdgcn_s_waitcnt((a & 15) | ((a >> 4) << 14) | 0x0F70);
  else if constexpr (op == SETUP) {
    const int row = lane & 15, j = row & 7, hl = lane >> 5;
    const bool act = ((lane >> 4) & 1) == (row >> 3);
    const unsigned two = hl ? 0x0302u : 0x0100u;
    const unsigned s = (j & 1) ? ((two << 16) | 0x0c0cu) : (0x0c0c0000u | two);
#pragma unroll
    for (int i = 0; i < 4; ++i) c.sel[i] = (act && (j >> 1) == i) ? s : 0x0c0c0c0cu;
    c.dim4 = 128 * hl + 8 * (lane & 15) + 4 * ((lane >> 4) & 1);
    c.acc = f32x4{0.f, 0.f, 0.f, 0.f};
    ((unsigned*)lds)[L_F + (lane & 1) * NW + w] = 0u;
  } else if constexpr (op == PACE) {
    const unsigned tgt = c.t0 + a;
    asm volatile("1:\n\ts_memrealtime vcc\n\ts_waitcnt lgkmcnt(0)\n\ts_sub_i32 vcc_lo, vcc_lo, %0\n\ts_cmp_lt_i32 vcc_lo, 0\n\ts_cbranch_scc1 1b"
                 :: "s"(tgt) : "scc", "vcc", "memory");
  } else if constexpr (op == TRW) {
#pragma unroll
    for (int i = 0; i < 4; ++i) *(v4u*)&lds[L_T + w * 1024 + i * 256 + lane * 4] = c.ob[4 * a + i];
  } else if constexpr (op == TRR) {
    typedef __attribute__((address_space(3))) i16x4 lds_i16x4;
    const float* base = &lds[L_T + w * 1024 + (lane >> 4) * 256 + ((lane >> 2) & 3) * 32 + (lane & 3) * 2];
#pragma unroll
    for (int ct = 0; ct < 4; ++ct)
#pragma unroll
      for (int h = 0; h < 2; ++h)
        c.of[a][ct][h] = __builtin_bit_cast(v2u, __builtin_amdgcn_ds_read_tr16_b64_v4i16((lds_i16x4*)(base + h * 128 + ct * 8)));
  } else if constexpr (op == LAG) {
    const int late = (int)((unsigned)__builtin_amdgcn_s_memrealtime() - c.t0) - BOOST_T;
    c.boost = late * BOOST_G >> 4;
  } else if constexpr (op == WAITB) {
    asm volatile("s_cmp_ge_i32 %0, %1\n\ts_cbranch_scc1 1f\n\ts_waitcnt vmcnt(%2)\n1:" :: "s"(c.boost), "i"(a >> 8), "i"(a & 255) : "scc", "memory");
  } else if constexpr (op == SLEEP) {
    __builtin_amdgcn_s_sleep(a);
  } else if constexpr (op == BAR0) {
    __syncthreads();
  } else if constexpr (op == QTR) {
    typedef __attribute__((address_space(3))) i16x4 lds_i16x4;
#pragma unroll
    for (int i = 0; i < 4; ++i) *(v4u*)&lds[L_T + w * 1024 + i * 256 + lane * 4] = c.qb[i];
    const float* base = &lds[L_T + w * 1024 + (lane >> 4) * 128 + ((lane >> 2) & 3) * 16 + (lane & 3) * 2];
#pragma unroll
    for (int ks = 0; ks < 2; ++ks)
#pragma unroll
      for (int ct = 0; ct < 2; ++ct)
#pragma unroll
        for (int h = 0; h < 2; ++h)
          c.qf[ks][ct][h] = __builtin_bit_cast(v2u, __builtin_amdgcn_ds_read_tr16_b64_v4i16((lds_i16x4*)(base + ks * 512 + h * 64 + ct * 8)));
  } else if constexpr (op == QMM) {
    f32x4 d[2];
#pragma unroll
    for (int ct = 0; ct < 2; ++ct) {
      d[ct] = f32x4{0.f, 0.f, 0.f, 0.f};
#pragma unroll
      for (int ks = 0; ks < 2; ++ks) {
        const v4u bm = {c.qf[ks][ct][0].x, c.qf[ks][ct][0].y, c.qf[ks][ct][1].x, c.qf[ks][ct][1].y};
        d[ct] = mfma(c.xv[ks], bm, d[ct]);
      }
    }
    c.tag = next_epoch(c.ep0);
    asm volatile("" : "+s"(c.tag));
    const float lo = (lane & 16) ? d[1].x : d[0].x, hi = (lane & 16) ? d[1].y : d[0].y;
    lds[L_RED + w * 64 + lane] = (lane & 32) ? hi : lo;
    flag_set(lds, 0, w);
    ST(1);
  } else if constexpr (op == QPUB) {
    flag_wait(lds, 0, lane);
    float t = 0.f;
#pragma unroll
    for (int i = 0; i < 7; ++i) t += lds[L_RED + i * 64 + lane];
    const unsigned pr = cvtpk(t, dpp<XOR1>(t));
    const v4u g = {pr, c.tag, __float_as_uint(dpp<XOR2>(__uint_as_float(pr))), c.tag};
    __builtin_amdgcn_raw_buffer_store_b128(g, c.rw, c.q_st, 0, AUX_SC1);
    ST(2);
  } else if constexpr (op == QLD) {
    c.qg = __builtin_amdgcn_raw_buffer_load_b128(c.rw, c.q_ld, 0, AUX_SC1);
  } else if constexpr (op == QCHK) {
    await_tags(c.qg, c.rw, c.q_ld, c.tag);
    *(v2u*)&lds[L_T + w * 1024 + lane * 2] = v2u{c.qg.x, c.qg.z};
    c.qr = *(const v4u*)&lds[L_T + w * 1024 + (lane & 31) * 4];
    ST(3);
  } else if constexpr (op == CK) {
    const v4u k = c.kb[a], q = c.qr;
    float x = dot2(k.x, q.x, dot2(k.z, q.z, 0.f)) + dot2(k.y, q.y, dot2(k.w, q.w, 0.f));
    if constexpr ((a & 1) == 0) c.xs[0] = x;
    else {
      const auto sw = __builtin_amdgcn_permlane16_swap(__float_as_uint(c.xs[0]), __float_as_uint(x), false, false);
      x = __uint_as_float(sw[0]) + __uint_as_float(sw[1]);
      if constexpr ((a & 2) == 0) c.xs[1] = x;
      else {
        x = tstep<ROR8>(c.xs[1], x, lane & 8);
        if constexpr ((a & 4) == 0) c.xs[2] = x;
        else {
          x = tstep<XOR7>(c.xs[2], x, lane & 4);
          if constexpr ((a & 8) == 0) c.xs[3] = x;
          else {
            x = tstep<XOR2>(c.xs[3], x, lane & 2);
            if constexpr ((a & 16) == 0) c.xs[4] = x;
            else {
              x = tstep<XOR1>(c.xs[4], x, lane & 1) * (0.0625f * 1.4426950408889634f);
              if constexpr (a == 31) c.s0 = x; else c.s1 = x;
            }
          }
        }
      }
    }
  } else if constexpr (op == SM2) {
    ST(4);
    float m = max8(fmaxf(c.s0, c.s1)); m = fmaxf(m, dpp<XOR15>(m)); m = xrow_max(m);
    const float p0 = __builtin_amdgcn_exp2f(c.s0 - m), p1 = __builtin_amdgcn_exp2f(c.s1 - m);
    __bf16* pw = (__bf16*)&lds[L_P + w * 64] + (__builtin_bitreverse32(lane) >> 26);
    pw[0] = (__bf16)p0; pw[64] = (__bf16)p1;
    c.mw = m; c.s0 = p0 + p1;
    c.pp = *(const v4u*)&lds[L_P + w * 64];
  } else if constexpr (op == SM3) {
    float l = sum8(c.s0); l += dpp<XOR15>(l); l = xrow_sum(l);
    lds[L_ML + 2 * w + (lane & 1)] = (lane & 1) ? l : c.mw;
    flag_set(lds, 1, w);
  } else if constexpr (op == MLPUB) {
    flag_wait(lds, 1, lane);
    const f32x2 ml = *(const f32x2*)&lds[L_ML + 2 * (lane & 7)];
    const float M = max8(ml.x), L = sum8(ml.y * __builtin_amdgcn_exp2f(ml.x - M));
    const v4u g = {__float_as_uint(M), c.tag, __float_as_uint(L), c.tag};
    __builtin_amdgcn_raw_buffer_store_b128(g, c.rw, c.ml_st, 0, AUX_SC1);
  } else if constexpr (op == CV) {
    constexpr int i = a & 3;
    c.acc = mfma(c.diag(c.pp[i]), c.vb[a], c.acc);
    if constexpr (i == 3 && a + 1 < N_V) c.pp = *(const v4u*)&lds[L_P + w * 64 + (a + 1)];
  } else if constexpr (op == PEERLD) {
    c.peer = __builtin_amdgcn_raw_buffer_load_b128(c.rw, c.ml_off + (lane & 3) * 16, 0, AUX_SC1);
  } else if constexpr (op == PEERCHK) {
    await_tags(c.peer, c.rw, c.ml_off + (lane & 3) * 16, c.tag);
    const float m = __uint_as_float(c.peer.x);
    float M = fmaxf(m, dpp<XOR1>(m)); M = fmaxf(M, dpp<XOR2>(M));
    float L = __uint_as_float(c.peer.z) * __builtin_amdgcn_exp2f(m - M); L += dpp<XOR1>(L); L += dpp<XOR2>(L);
    c.scale = __builtin_amdgcn_exp2f(c.mw - M) / L;
  }
  __builtin_amdgcn_sched_barrier(0);
}
template <int... I> __device__ __forceinline__ void run(Ctx& c, std::integer_sequence<int, I...>) { (step<I>(c), ...); }
template <int B, int... I> __device__ __forceinline__ void run_from(Ctx& c, std::integer_sequence<int, I...>) { (step<B + I>(c), ...); }
constexpr int N_LAST = 3;

__global__ void __launch_bounds__(NTHR, 2)
mha_decode(const __bf16* __restrict__ wqA, const __bf16* __restrict__ wqB, const __bf16* __restrict__ kA,
           const __bf16* __restrict__ kB, const __bf16* __restrict__ xA, const __bf16* __restrict__ xB, unsigned char* ws,
           const __bf16* __restrict__ vA, const __bf16* __restrict__ vB, const __bf16* __restrict__ woA,
           const __bf16* __restrict__ woB, __bf16* out) {
  __shared__ __attribute__((aligned(16))) float lds[L_END];
  Ctx c;
  ST(0);
  const int bid = blockIdx.x;
  const int rk = bid >> 3, bh = (bid & 7) * 4 + (rk >> 3), br = bh >> 4, head = bh & 15, b = (rk >> 2) & 1, split = rk & 3;
  const int grp = br * 2 + b, prod = head * 4 + split;
  const int tid = threadIdx.x, lane = tid & 63, w = __builtin_amdgcn_readfirstlane(tid >> 6);
  c.t0 = (unsigned)__builtin_amdgcn_s_memrealtime() + STAG * w * TAU / 128;
  const size_t kvoff = ((size_t)(b * NH + head) * SK + split * 1024) * DH;
  c.rq = __builtin_amdgcn_make_buffer_rsrc((void*)((br ? wqB : wqA) + head * DH + (rk & 7) * 32), 0, (DM - 1) * 8192 + 64, RSRC_W3);
  c.rk = __builtin_amdgcn_make_buffer_rsrc((void*)((br ? kB : kA) + kvoff), 0, 1024 * 512, RSRC_W3);
  c.w = w; c.lane = lane; c.lds = lds;
  c.vo_q = (lane >> 2) * 8192 + (lane & 3) * 16;
  c.vo_v = lane * 16;
  c.so_kv = w * 4096 + (ROT == 1 ? (head + 8 * br) & 15 : ROT == 2 ? split * 4 : 0) * 32768;
  run(c, std::make_integer_sequence<int, N_FIRST>{});
  c.rx = __builtin_amdgcn_make_buffer_rsrc((void*)(br ? xB : xA), 0, 2 * DM * 2, RSRC_W3);
  c.rw = __builtin_amdgcn_make_buffer_rsrc((void*)ws, 0, WS_END, RSRC_W3);
  c.vo_x = (((lane & 1) * DM + 64 * w + 8 * (lane >> 4)) * 2) | (w < 7 ? 0 : OOB);
  c.ep_off = br * 256;
  c.xv[0] = __builtin_amdgcn_raw_buffer_load_b128(c.rx, c.vo_x, 0, 0);
  c.xv[1] = __builtin_amdgcn_raw_buffer_load_b128(c.rx, c.vo_x + 64, 0, 0);
  c.ep0 = *(const __attribute__((address_space(4))) unsigned*)(ws + c.ep_off);
  __builtin_amdgcn_sched_barrier(0);
  int br2 = br;
  asm volatile("" : "+s"(br2));
  c.rv = __builtin_amdgcn_make_buffer_rsrc((void*)((br2 ? vB : vA) + kvoff), 0, 1024 * 512, RSRC_W3);
  c.ro = __builtin_amdgcn_make_buffer_rsrc((void*)((br2 ? woB : woA) + head * DH * DM), 0, DH * DM * 2, RSRC_W3);
  c.rout = __builtin_amdgcn_make_buffer_rsrc((void*)out, 0, 4 * DM * 2, RSRC_W3);
  c.orot = OROT ? rk & 7 : 0;
  c.vo_o = ((lane >> 3) * (DM * 2) + w * 128 + (lane & 7) * 16) | (w < 7 ? 0 : OOB);
  c.ml_off = WS_ML + (grp * 16 + head) * 128;
  c.ml_st = (c.ml_off + split * 16) | (tid == 0 ? 0 : OOB);
  c.q_st = (WS_Q + bh * 2048 + (lane >> 5) * 1024 + (rk & 7) * 128 + (lane & 31) * 4) | ((tid & ~60) == 0 ? 0 : OOB);
  c.q_ld = WS_Q + bh * 2048 + b * 1024 + lane * 16;
  run_from<N_FIRST>(c, std::make_integer_sequence<int, S.n - N_FIRST - N_LAST>{});
  const int pk = WS_PKT + grp * 64 * PKT_BYTES;
  int a_wr = (L_RED + w * A_STR + (((c.dim4 >> 2) - 8 * c.orot) & 63)) * 4, a_rd = (L_RED + (lane & 15) * A_STR + 4 * (lane >> 4)) * 4;
  int idx = (lane & 8) ? 0xEEEE : 0x4444, y_off = (pk + prod * PKT_BYTES + tid * 4) | (w < 7 && !(lane & 1) ? 0 : OOB);
  int p_off = pk + (lane >> 2) * PKT_BYTES + prod * 64 + (lane & 3) * 16;
  int o_off = ((b * (2 * DM) + br * DM + prod * 16 + (lane & 3) * 4 + (lane >> 4)) * 2) | ((lane & 12) << 27);
  int e_off = (prod == 0 && b == 0 && lane == 0) ? c.ep_off : OOB;
  asm volatile("" : "+v"(a_wr), "+v"(a_rd), "+v"(idx), "+v"(y_off), "+v"(p_off), "+v"(o_off), "+v"(e_off));
  f32x4 y[4];
#pragma unroll
  for (int ct = 0; ct < 4; ++ct) { y[ct] = f32x4{0.f, 0.f, 0.f, 0.f}; asm volatile("" : "+v"(y[ct])); }
  run_from<S.n - N_LAST>(c, std::make_integer_sequence<int, N_LAST>{});
  ST(5);
  typedef __attribute__((address_space(3))) unsigned char lds_byte;
  lds_byte* const lb = (lds_byte*)lds;
  *(__attribute__((address_space(3))) unsigned*)(lb + a_wr) = cvtpk(c.acc.x * c.scale, c.acc.y * c.scale);
  *(__attribute__((address_space(3))) unsigned*)(lb + a_wr + 8 * A_STR * 4) = cvtpk(c.acc.z * c.scale, c.acc.w * c.scale);
  __syncthreads();
  ST(6);
  {
    v4u am[4];
#pragma unroll
    for (int ks = 0; ks < 4; ++ks) am[ks] = *(__attribute__((address_space(3))) v4u*)(lb + a_rd + 64 * ks);
    __builtin_amdgcn_sched_barrier(0);
#pragma unroll
    for (int ks = 0; ks < 4; ++ks)
#pragma unroll
      for (int ct = 0; ct < 4; ++ct) {
        const v2u b0 = c.of[2 * ks][ct][0], b1 = c.of[2 * ks][ct][1], b2 = c.of[2 * ks + 1][ct][0], b3 = c.of[2 * ks + 1][ct][1];
        const v8u bm = {b0.x, b0.y, b1.x, b1.y, b2.x, b2.y, b3.x, b3.y};
        y[ct] = __builtin_amdgcn_smfmac_f32_16x16x64_bf16(__builtin_bit_cast(bf16x8, am[ks]), __builtin_bit_cast(bf16x16, bm), y[ct], idx, 0, 0);
      }
    float sm[4];
#pragma unroll
    for (int ct = 0; ct < 4; ++ct) sm[ct] = (y[ct].x + y[ct].y) + (y[ct].z + y[ct].w);
    const auto u0 = __builtin_amdgcn_permlane16_swap(__float_as_uint(sm[0]), __float_as_uint(sm[1]), false, false);
    const auto u1 = __builtin_amdgcn_permlane16_swap(__float_as_uint(sm[2]), __float_as_uint(sm[3]), false, false);
    const float t0 = __uint_as_float(u0[0]) + __uint_as_float(u0[1]), t1 = __uint_as_float(u1[0]) + __uint_as_float(u1[1]);
    const auto u2 = __builtin_amdgcn_permlane32_swap(__float_as_uint(t0), __float_as_uint(t1), false, false);
    const float yv = __uint_as_float(u2[0]) + __uint_as_float(u2[1]);
    const v2u g = {(__float_as_uint(yv) & ~255u) | (c.tag & 255u), (__float_as_uint(dpp<XOR1>(yv)) & ~255u) | ((c.tag >> 8) & 255u)};
    __builtin_amdgcn_raw_buffer_store_b64(g, c.rw, y_off, 0, AUX_SC1);
    ST(8);
  }
  if (w == NW - 1 && prod < DM / 16) {
    v4u v[4], u[4];
    const unsigned t0 = c.tag & 255u, t1 = (c.tag >> 8) & 255u;
    bool got[4] = {false, false, false, false}, done = false;
#pragma unroll 1
    for (unsigned spins = 0; !done && spins < SPIN_CAP; ++spins) {
      asm volatile("" ::: "memory");
#pragma unroll
      for (int i = 0; i < 4; ++i)
        if (!got[i]) v[i] = __builtin_amdgcn_raw_buffer_load_b128(c.rw, p_off, i * 16 * PKT_BYTES, AUX_SC1);
#pragma unroll
      for (int i = 0; i < 4; ++i) {
        u[i] = v[i] ^ v4u{t0, t1, t0, t1};
        got[i] = __all(((u[i].x | u[i].y | u[i].z | u[i].w) & 255u) == 0u);
      }
      done = got[0] & got[1] & got[2] & got[3];
    }
    const f32x4 r = (__builtin_bit_cast(f32x4, u[0]) + __builtin_bit_cast(f32x4, u[1])) + (__builtin_bit_cast(f32x4, u[2]) + __builtin_bit_cast(f32x4, u[3]));
    const auto s0 = __builtin_amdgcn_permlane32_swap(__float_as_uint(r[0]), __float_as_uint(r[2]), false, false);
    const auto s1 = __builtin_amdgcn_permlane32_swap(__float_as_uint(r[1]), __float_as_uint(r[3]), false, false);
    float e0 = __uint_as_float(s0[0]) + __uint_as_float(s0[1]), e1 = __uint_as_float(s1[0]) + __uint_as_float(s1[1]);
    const auto s2 = __builtin_amdgcn_permlane16_swap(__float_as_uint(e0), __float_as_uint(e1), false, false);
    float z = __uint_as_float(s2[0]) + __uint_as_float(s2[1]);
    z += dpp<ROR4>(z);
    z += dpp<ROR8>(z);
    const __bf16 zb = (__bf16)z;
    __builtin_amdgcn_raw_buffer_store_b16(done ? __builtin_bit_cast(unsigned short, zb) : (unsigned short)0x7fc0u, c.rout, o_off, 0, 0);
    __builtin_amdgcn_raw_buffer_store_b32(c.tag, c.rw, e_off, 0, AUX_SC1);
    ST(9);
  }
}

}

extern "C" void kernel_launch(void* const* d_in, const int* in_sizes, int n_in, void* d_out, int out_size, void* d_ws,
                              size_t ws_size, hipStream_t stream) {
  (void)in_sizes; (void)n_in; (void)out_size; (void)ws_size;
  mha_decode<<<256, NTHR, 0, stream>>>((const __bf16*)d_in[3], (const __bf16*)d_in[8], (const __bf16*)d_in[1],
                                       (const __bf16*)d_in[6], (const __bf16*)d_in[0], (const __bf16*)d_in[5],
                                       (unsigned char*)d_ws, (const __bf16*)d_in[2], (const __bf16*)d_in[7],
                                       (const __bf16*)d_in[4], (const __bf16*)d_in[9], (__bf16*)d_out);
}
```

```cpp
#include <hip/hip_runtime.h>
#include <hip/hip_bf16.h>
#include <stdint.h>
#include <utility>

#define WIN2 6
#define RO 2
#define O_START 40
#define TAU 0
#define TOFF 0
#define PACE_FROM 12
#define BLIND 0
#define NAP 9
#define STAG 1
#define N3 8
#define WIN3 8
#define YST_AUX 16
#define YLD_AUX 16
#define POLLD 1
#define POLLS 1
#define QSP 0
#define NF 5
#define BOOST_AT 6
#define BOOST_MAX 40
#define BOOST_T 380
#define BOOST_G 4
#define TOUCH 0
#define XROT 1
#define NWC 0
#define WC0 98
#define WC1 60
#define WCD 4
#define WB_G 3
#define WB_MAX 20
#define WS_G 2
#define WS_MAX 12
#define T0EARLY 1
#define QMM_AT 3
#define QPUB_AT 6
#define QLD_AT 14
#define QCHK_AT 18
#define QGET_AT 21
#define ROT 1
#define OROT 1
#define KV_AUX 2
#define W_AUX 0
#define Q_AUX 2

#define ST(k)

namespace {

typedef unsigned v4u __attribute__((ext_vector_type(4)));
typedef unsigned v2u __attribute__((ext_vector_type(2)));
typedef float f32x4 __attribute__((ext_vector_type(4)));
typedef float f32x2 __attribute__((ext_vector_type(2)));
typedef __bf16 bf16x8 __attribute__((ext_vector_type(8)));
typedef __bf16 bf16x2 __attribute__((ext_vector_type(2)));
typedef __bf16 bf16x16 __attribute__((ext_vector_type(16)));
typedef short i16x4 __attribute__((ext_vector_type(4)));
typedef unsigned v8u __attribute__((ext_vector_type(8)));
typedef __amdgpu_buffer_rsrc_t rsrc_t;

constexpr int DM = 448, NH = 16, SK = 4096, DH = 256;
constexpr int NW = 8, NTHR = NW * 64;
constexpr int N_Q = 4, N_K = 64, N_V = 64, N_O = 32;
constexpr unsigned SPIN_CAP = 1u << 16;
constexpr int RSRC_W3 = 0x00020000, AUX_SC1 = 16, OOB = 0x40000000;
constexpr int WS_PKT = 4096, PKT_BYTES = DM * 4, WS_Q = WS_PKT + 256 * PKT_BYTES, WS_ML = WS_Q + 64 * 1024,
              WS_END = WS_ML + 64 * 128;
constexpr int RED_STR = 260, A_STR = 68;
constexpr int L_RED = 0, L_T = L_RED + NW * RED_STR, L_ML = L_T + NW * 1024, L_P = L_ML + 2 * NW, L_F = L_P + NW * 64,
              L_END = L_F + NW * 4;

constexpr int N_FIRST = NF;
enum Op : int { IQ, IK, IV, IO, QPUB, QLD, MLPUB, PEERLD, WAITV, PACE, QMM, QCHK, QTR, CK, SM3, SM2, CV, PEERCHK, SETUP, BAR0, SLEEP, TRW, TRR, WAITB, LAG, WPUB, WGET, WAITS };
struct Sched { int n; int op[1000]; int a[1000]; };
constexpr Sched make_sched() {
  Sched s{};
  int kind[256] = {}, idx[256] = {}, n = 0;
  auto add = [&](int k, int i) { kind[n] = k; idx[n] = i; ++n; };
  const int n0 = N_FIRST;
  int o = 0, cnt = RO - 1, nq = 0;
  for (int j = 0; j < N_K + N_V; ++j) {
    while (nq < N_Q && nq * QSP <= j) add(IQ, nq++);
    if (j < N_K) add(IK, j); else add(IV, j - N_K);
    if (j >= O_START && ++cnt >= RO && o < N_O) { add(IO, o++); cnt = 0; }
  }
  while (o < N_O) add(IO, o++);
  int vmk[320] = {}, vmi[320] = {}, nvm = 0;
  int nkv = 0, st_vm = -1, st_kv = 0, bs = -1;
  auto emit = [&](int op, int a) { s.op[s.n] = op; s.a[s.n] = a; ++s.n;
    if (op == QPUB || op == MLPUB) { st_vm = nvm; st_kv = nkv; }
    if (op <= PEERLD) { vmk[nvm] = op; vmi[nvm] = a; ++nvm; }
    if (op == SETUP) for (int t = 0; t < 2; ++t) { vmk[nvm] = QLD; ++nvm; } };
  int nk = 0, nv = 0, cost = 0, lk = 0, lv = 0, lo = 0, nw = 0, nr = 0, sm = 0;
  bool qr = false;
  for (int i = 0; i < n; ++i) {
    if (i >= n0 && kind[i] != IO) {
      if (TAU && nkv >= PACE_FROM) emit(PACE, TOFF + cost * TAU / 128);
      const int win = ((nkv >= 127 - N3 ? WIN3 : WIN2) + (nkv & 1)) / 2;
      int p = nvm, seen = 0;
      while (p > 0 && seen < win) { --p; if (vmk[p] <= IV) ++seen; }
      if (st_vm >= 0 && st_vm < p && nkv - st_kv < BLIND) { if (!TAU) emit(SLEEP, NAP); }
      else {
        if (bs >= 0 && nkv <= bs + (bs == BOOST_AT ? BOOST_MAX : WB_MAX) && win == WIN2 / 2) {
          int p2 = p; do --p2; while (vmk[p2] > IV);
          int p0 = p; do ++p0; while (vmk[p0] > IV);
          emit(WAITV, nvm - 1 - p2);
          emit(WAITB, (nvm - 1 - p) | ((nkv - bs) << 8));
          if (WS_G && bs != BOOST_AT && nkv - bs <= WS_MAX) emit(WAITS, (nvm - 1 - p0) | ((nkv - bs) << 8));
          p = p2;
        } else emit(WAITV, nvm - 1 - p);
        if (nkv == BOOST_AT) { emit(LAG, 0); bs = nkv; }
        for (int t = 0; t < NWC; ++t) {
          const int cp = t == 0 ? WC0 : WC1;
          if (nkv == cp) emit(WPUB, t);
          if (nkv == cp + WCD) { emit(WGET, t); bs = nkv; }
        }
        if (vmk[p] == IK) lk = vmi[p] + 1;
        if (vmk[p] == IV) { lk = N_K; lv = vmi[p] + 1; }
        for (int t = 0; t < p; ++t) if (vmk[t] == IO) lo = vmi[t] + 1;
      }
      ++nkv;
    }
    emit(kind[i], idx[i]);
    cost += kind[i] == IO ? (idx[i] < 4 ? 7 : 0) : (kind[i] == IQ ? 7 : 8);
    if (i == n0 - 1) { emit(SETUP, 0); emit(BAR0, 0); }
    if (kind[i] == IO) continue;
    if (i + 1 < n && kind[i + 1] == IO) { ++i; emit(IO, idx[i]); cost += idx[i] < 4 ? 7 : 0; --i; }
    const bool skip = i + 1 < n && kind[i + 1] == IO;
    if (kind[i] == IK && idx[i] == QMM_AT - 1) emit(QTR, 0);
    if (kind[i] == IK && idx[i] == QMM_AT) emit(QMM, 0);
    if (kind[i] == IK && idx[i] == QPUB_AT) emit(QPUB, 0);
    if (kind[i] == IK && idx[i] == QLD_AT) emit(QLD, 0);
    if (kind[i] == IK && idx[i] == QCHK_AT) { emit(QCHK, 0); qr = true; }
    else if (qr && nk < N_K) { for (int t = 0; t < 3 && nk < lk; ++t) emit(CK, nk++); }
    else if (nk == N_K && sm < 2) { emit(sm == 0 ? SM2 : SM3, 0); ++sm; }
    else if (sm == 2) { for (int t = 0; t < 3 && nv < lv; ++t) {
      emit(CV, nv++);
      if (nv == 12) emit(MLPUB, 0);
      if (nv == N_V - 20) emit(PEERLD, 0);
      if (nv == N_V - 10) emit(PEERCHK, 0); } }
    if (nr < nw) emit(TRR, nr++);
    else if (4 * nw + 4 <= lo) emit(TRW, nw++);
    if (skip) ++i;
  }
  while (nr < N_O / 4) { if (nr == nw) emit(TRW, nw++); emit(TRR, nr++); }
  while (nv < N_V) emit(CV, nv++);
  return s;
}
constexpr Sched S = make_sched();

__device__ __forceinline__ float dot2(unsigned a, unsigned b, float c) {
  return __builtin_amdgcn_fdot2_f32_bf16(__builtin_bit_cast(bf16x2, a), __builtin_bit_cast(bf16x2, b), c, false);
}
__device__ __forceinline__ unsigned cvtpk(float lo, float hi) {
  bf16x2 r = {(__bf16)lo, (__bf16)hi};
  return __builtin_bit_cast(unsigned, r);
}
template <int CTRL> __device__ __forceinline__ float dpp(float x) {
  return __builtin_bit_cast(float, __builtin_amdgcn_mov_dpp(__builtin_bit_cast(int, x), CTRL, 0xf, 0xf, true));
}
constexpr int XOR1 = 0xB1, XOR2 = 0x4E, XOR7 = 0x141, XOR15 = 0x140, ROR4 = 0x124, ROR8 = 0x128;
__device__ __forceinline__ float sum8(float x) { x += dpp<XOR1>(x); x += dpp<XOR2>(x); x += dpp<XOR7>(x); return x; }
__device__ __forceinline__ float max8(float x) { x = fmaxf(x, dpp<XOR1>(x)); x = fmaxf(x, dpp<XOR2>(x)); x = fmaxf(x, dpp<XOR7>(x)); return x; }
template <int CTRL> __device__ __forceinline__ float tstep(float lo, float hi, bool sel) {
  const float a = lo + dpp<CTRL>(lo), b = hi + dpp<CTRL>(hi);
  return sel ? b : a;
}
__device__ __forceinline__ float xrow_sum(float x) {
  auto s = __builtin_amdgcn_permlane16_swap(__float_as_uint(x), __float_as_uint(x), false, false);
  x = __uint_as_float(s[0]) + __uint_as_float(s[1]);
  auto t = __builtin_amdgcn_permlane32_swap(__float_as_uint(x), __float_as_uint(x), false, false);
  return __uint_as_float(t[0]) + __uint_as_float(t[1]);
}
__device__ __forceinline__ float xrow_max(float x) {
  auto s = __builtin_amdgcn_permlane16_swap(__float_as_uint(x), __float_as_uint(x), false, false);
  x = fmaxf(__uint_as_float(s[0]), __uint_as_float(s[1]));
  auto t = __builtin_amdgcn_permlane32_swap(__float_as_uint(x), __float_as_uint(x), false, false);
  return fmaxf(__uint_as_float(t[0]), __uint_as_float(t[1]));
}
__device__ __forceinline__ f32x4 mfma(v4u a, v4u b, f32x4 c) {
  return __builtin_amdgcn_mfma_f32_16x16x32_bf16(__builtin_bit_cast(bf16x8, a), __builtin_bit_cast(bf16x8, b), c, 0, 0, 0);
}
__device__ __forceinline__ unsigned next_epoch(unsigned e) {
  const unsigned t = e * 0x9E3779B1u + 0x7F4A7C15u;
  return t ? t : 0x7F4A7C15u;
}

struct Ctx {
  rsrc_t rq, rk, rv, ro, rw, rout;
  rsrc_t rx;
  v4u xv[2];
  v2u qf[2][2][2];
  v4u qg;
  unsigned t0;
  int q_st, q_ld, vo_x, so_kv, orot;
  unsigned ep0, touch;
  int ep_off;
  int w, lane, vo_q, vo_v, vo_o, dim4, ml_off;
  unsigned sel[4], tag;
  float* lds;
  v4u qb[N_Q], kb[N_K], vb[N_V], ob[N_O], qr, pp;
  v2u of[8][4][2];
  f32x4 acc;
  float xs[5], s0, s1, mw, scale;
  v4u peer;
  int ml_st, boost, slow;
  unsigned tc;
  const __bf16 *pv, *po, *pout;
  unsigned th[4];
  const __attribute__((address_space(4))) unsigned* pep;
  __device__ __forceinline__ v4u diag(unsigned pair) const {
    v4u a;
    a.x = __builtin_amdgcn_perm(0u, pair, sel[0]); a.y = __builtin_amdgcn_perm(0u, pair, sel[1]);
    a.z = __builtin_amdgcn_perm(0u, pair, sel[2]); a.w = __builtin_amdgcn_perm(0u, pair, sel[3]);
    return a;
  }
};

__device__ __forceinline__ void await_tags(v4u& g, rsrc_t r, int off, unsigned tag) {
  if (__builtin_expect(!__all((g.y == tag) & (g.w == tag)), 0)) {
    unsigned spins = 0;
    bool ok;
#pragma unroll 1
    do {
      asm volatile("" ::: "memory");
      g = __builtin_amdgcn_raw_buffer_load_b128(r, off, 0, AUX_SC1);
      ok = __all((g.y == tag) & (g.w == tag));
    } while (!ok && ++spins < SPIN_CAP);
    if (!ok) g.x = 0x7fc00000u;
  }
}
typedef __attribute__((address_space(3))) volatile unsigned lds_flag;
__device__ __forceinline__ void flag_set(float* lds, int k, int w) {
  asm volatile("s_waitcnt lgkmcnt(0)" ::: "memory");
  ((lds_flag*)lds)[L_F + k * NW + w] = 1u;
}
__device__ __forceinline__ void flag_wait(float* lds, int k, int lane) {
  while (!__all(((lds_flag*)lds)[L_F + k * NW + (lane & 7)] != 0u)) {}
  asm volatile("" ::: "memory");
}

template <int I> __device__ __forceinline__ void step(Ctx& c) {
  constexpr int op = S.op[I], a = S.a[I];
  float* lds = c.lds;
  const int w = c.w, lane = c.lane;
  if constexpr (op == IQ) c.qb[a] = __builtin_amdgcn_raw_buffer_load_b128(c.rq, c.vo_q, (64 * w + 16 * a) * 8192, Q_AUX);
  else if constexpr (op == IK) c.kb[a] = __builtin_amdgcn_raw_buffer_load_b128(c.rk, c.vo_v + (a & 3) * 1024, XROT ? c.so_kv ^ ((a >> 2) * 32768) : (c.so_kv + (a >> 2) * 32768) & 0x7ffff, KV_AUX);
  else if constexpr (op == IV) c.vb[a] = __builtin_amdgcn_raw_buffer_load_b128(c.rv, c.vo_v + (a & 3) * 1024, XROT ? c.so_kv ^ ((a >> 2) * 32768) : (c.so_kv + (a >> 2) * 32768) & 0x7ffff, KV_AUX);
  else if constexpr (op == IO) c.ob[a] = __builtin_amdgcn_raw_buffer_load_b128(c.ro, c.vo_o, ((8 * a + 32 * c.orot) & 255) * (DM * 2), W_AUX);
  else if constexpr (op == WAITV) __builtin_amdgcn_s_waitcnt((a & 15) | ((a >> 4) << 14) | 0x0F70);
  else if constexpr (op == SETUP) {
    const int row = lane & 15, j = row & 7, hl = lane >> 5;
    const bool act = ((lane >> 4) & 1) == (row >> 3);
    const unsigned two = hl ? 0x0302u : 0x0100u;
    const unsigned s = (j & 1) ? ((two << 16) | 0x0c0cu) : (0x0c0c0000u | two);
#pragma unroll
    for (int i = 0; i < 4; ++i) c.sel[i] = (act && (j >> 1) == i) ? s : 0x0c0c0c0cu;
    c.dim4 = 128 * hl + 8 * (lane & 15) + 4 * ((lane >> 4) & 1);
    c.acc = f32x4{0.f, 0.f, 0.f, 0.f};
    ((unsigned*)lds)[L_F + (lane & 3) * NW + w] = 0u;
  } else if constexpr (op == PACE) {
    const unsigned tgt = c.t0 + a;
    asm volatile("1:\n\ts_memrealtime vcc\n\ts_waitcnt lgkmcnt(0)\n\ts_sub_i32 vcc_lo, vcc_lo, %0\n\ts_cmp_lt_i32 vcc_lo, 0\n\ts_cbranch_scc1 1b"
                 :: "s"(tgt) : "scc", "vcc", "memory");
  } else if constexpr (op == TRW) {
#pragma unroll
    for (int i = 0; i < 4; ++i) *(v4u*)&lds[L_T + w * 1024 + i * 256 + lane * 4] = c.ob[4 * a + i];
  } else if constexpr (op == TRR) {
    typedef __attribute__((address_space(3))) i16x4 lds_i16x4;
    const float* base = &lds[L_T + w * 1024 + (lane >> 4) * 256 + ((lane >> 2) & 3) * 32 + (lane & 3) * 2];
#pragma unroll
    for (int ct = 0; ct < 4; ++ct)
#pragma unroll
      for (int h = 0; h < 2; ++h)
        c.of[a][ct][h] = __builtin_bit_cast(v2u, __builtin_amdgcn_ds_read_tr16_b64_v4i16((lds_i16x4*)(base + h * 128 + ct * 8)));
  } else if constexpr (op == LAG) {
    const int late = (int)((unsigned)__builtin_amdgcn_s_memrealtime() - c.t0) - BOOST_T;
    c.boost = late * BOOST_G >> 4;
  } else if constexpr (op == WPUB) {
    c.tc = (unsigned)__builtin_amdgcn_s_memrealtime() | 1u;
    ((lds_flag*)lds)[L_F + (2 + a) * NW + w] = c.tc;
  } else if constexpr (op == WGET) {
    const unsigned t = ((lds_flag*)lds)[L_F + (2 + a) * NW + (lane & 7)];
    int lv = (int)(sum8(t ? (float)(int)(c.tc - t) : (float)(-28 * WCD)) * 0.125f);
    asm volatile("" : "+v"(lv));
    const int late = __builtin_amdgcn_readfirstlane(lv);
    c.boost = late * WB_G >> 4;
    c.slow = -late * WS_G >> 4;
  } else if constexpr (op == WAITS) {
    asm volatile("s_cmp_lt_i32 %0, %1\n\ts_cbranch_scc1 1f\n\ts_waitcnt vmcnt(%2)\n1:" :: "s"(c.slow), "i"(a >> 8), "i"(a & 255) : "scc", "memory");
  } else if constexpr (op == WAITB) {
    asm volatile("s_cmp_ge_i32 %0, %1\n\ts_cbranch_scc1 1f\n\ts_waitcnt vmcnt(%2)\n1:" :: "s"(c.boost), "i"(a >> 8), "i"(a & 255) : "scc", "memory");
  } else if constexpr (op == SLEEP) {
    __builtin_amdgcn_s_sleep(a);
  } else if constexpr (op == BAR0) {
    __syncthreads();
    c.ep0 = *c.pep;
  } else if constexpr (op == QTR) {
    typedef __attribute__((address_space(3))) i16x4 lds_i16x4;
#pragma unroll
    for (int i = 0; i < 4; ++i) *(v4u*)&lds[L_T + w * 1024 + i * 256 + lane * 4] = c.qb[i];
    const float* base = &lds[L_T + w * 1024 + (lane >> 4) * 128 + ((lane >> 2) & 3) * 16 + (lane & 3) * 2];
#pragma unroll
    for (int ks = 0; ks < 2; ++ks)
#pragma unroll
      for (int ct = 0; ct < 2; ++ct)
#pragma unroll
        for (int h = 0; h < 2; ++h)
          c.qf[ks][ct][h] = __builtin_bit_cast(v2u, __builtin_amdgcn_ds_read_tr16_b64_v4i16((lds_i16x4*)(base + ks * 512 + h * 64 + ct * 8)));
  } else if constexpr (op == QMM) {
    f32x4 d[2];
#pragma unroll
    for (int ct = 0; ct < 2; ++ct) {
      d[ct] = f32x4{0.f, 0.f, 0.f, 0.f};
#pragma unroll
      for (int ks = 0; ks < 2; ++ks) {
        const v4u bm = {c.qf[ks][ct][0].x, c.qf[ks][ct][0].y, c.qf[ks][ct][1].x, c.qf[ks][ct][1].y};
        d[ct] = mfma(c.xv[ks], bm, d[ct]);
      }
    }
    c.tag = next_epoch(c.ep0);
    asm volatile("" : "+s"(c.tag));
    const float lo = (lane & 16) ? d[1].x : d[0].x, hi = (lane & 16) ? d[1].y : d[0].y;
    lds[L_RED + w * 64 + lane] = (lane & 32) ? hi : lo;
    flag_set(lds, 0, w);
    ST(1);
  } else if constexpr (op == QPUB) {
    flag_wait(lds, 0, lane);
    float t = 0.f;
#pragma unroll
    for (int i = 0; i < 7; ++i) t += lds[L_RED + i * 64 + lane];
    const unsigned pr = cvtpk(t, dpp<XOR1>(t));
    const v4u g = {pr, c.tag, __float_as_uint(dpp<XOR2>(__uint_as_float(pr))), c.tag};
    __builtin_amdgcn_raw_buffer_store_b128(g, c.rw, c.q_st, 0, AUX_SC1);
    ST(2);
  } else if constexpr (op == QLD) {
    c.qg = __builtin_amdgcn_raw_buffer_load_b128(c.rw, c.q_ld, 0, AUX_SC1);
  } else if constexpr (op == QCHK) {
    await_tags(c.qg, c.rw, c.q_ld, c.tag);
    *(v2u*)&lds[L_T + w * 1024 + lane * 2] = v2u{c.qg.x, c.qg.z};
    c.qr = *(const v4u*)&lds[L_T + w * 1024 + (lane & 31) * 4];
    ST(3);
  } else if constexpr (op == CK) {
    const v4u k = c.kb[a], q = c.qr;
    float x = dot2(k.x, q.x, dot2(k.z, q.z, 0.f)) + dot2(k.y, q.y, dot2(k.w, q.w, 0.f));
    if constexpr ((a & 1) == 0) c.xs[0] = x;
    else {
      const auto sw = __builtin_amdgcn_permlane16_swap(__float_as_uint(c.xs[0]), __float_as_uint(x), false, false);
      x = __uint_as_float(sw[0]) + __uint_as_float(sw[1]);
      if constexpr ((a & 2) == 0) c.xs[1] = x;
      else {
        x = tstep<ROR8>(c.xs[1], x, lane & 8);
        if constexpr ((a & 4) == 0) c.xs[2] = x;
        else {
          x = tstep<XOR7>(c.xs[2], x, lane & 4);
          if constexpr ((a & 8) == 0) c.xs[3] = x;
          else {
            x = tstep<XOR2>(c.xs[3], x, lane & 2);
            if constexpr ((a & 16) == 0) c.xs[4] = x;
            else {
              x = tstep<XOR1>(c.xs[4], x, lane & 1) * (0.0625f * 1.4426950408889634f);
              if constexpr (a == 31) c.s0 = x; else c.s1 = x;
            }
          }
        }
      }
    }
  } else if constexpr (op == SM2) {
    ST(4);
    float m = max8(fmaxf(c.s0, c.s1)); m = fmaxf(m, dpp<XOR15>(m)); m = xrow_max(m);
    const float p0 = __builtin_amdgcn_exp2f(c.s0 - m), p1 = __builtin_amdgcn_exp2f(c.s1 - m);
    __bf16* pw = (__bf16*)&lds[L_P + w * 64] + (__builtin_bitreverse32(lane) >> 26);
    pw[0] = (__bf16)p0; pw[64] = (__bf16)p1;
    c.mw = m; c.s0 = p0 + p1;
    c.pp = *(const v4u*)&lds[L_P + w * 64];
  } else if constexpr (op == SM3) {
    float l = sum8(c.s0); l += dpp<XOR15>(l); l = xrow_sum(l);
    lds[L_ML + 2 * w + (lane & 1)] = (lane & 1) ? l : c.mw;
    flag_set(lds, 1, w);
  } else if constexpr (op == MLPUB) {
    flag_wait(lds, 1, lane);
    const f32x2 ml = *(const f32x2*)&lds[L_ML + 2 * (lane & 7)];
    const float M = max8(ml.x), L = sum8(ml.y * __builtin_amdgcn_exp2f(ml.x - M));
    const v4u g = {__float_as_uint(M), c.tag, __float_as_uint(L), c.tag};
    __builtin_amdgcn_raw_buffer_store_b128(g, c.rw, c.ml_st, 0, AUX_SC1);
  } else if constexpr (op == CV) {
    constexpr int i = a & 3;
    c.acc = mfma(c.diag(c.pp[i]), c.vb[a], c.acc);
    if constexpr (i == 3 && a + 1 < N_V) c.pp = *(const v4u*)&lds[L_P + w * 64 + (a + 1)];
  } else if constexpr (op == PEERLD) {
    c.peer = __builtin_amdgcn_raw_buffer_load_b128(c.rw, c.ml_off + (lane & 3) * 16, 0, AUX_SC1);
  } else if constexpr (op == PEERCHK) {
    await_tags(c.peer, c.rw, c.ml_off + (lane & 3) * 16, c.tag);
    const float m = __uint_as_float(c.peer.x);
    float M = fmaxf(m, dpp<XOR1>(m)); M = fmaxf(M, dpp<XOR2>(M));
    float L = __uint_as_float(c.peer.z) * __builtin_amdgcn_exp2f(m - M); L += dpp<XOR1>(L); L += dpp<XOR2>(L);
    c.scale = __builtin_amdgcn_exp2f(c.mw - M) / L;
  }
  __builtin_amdgcn_sched_barrier(0);
}
template <int... I> __device__ __forceinline__ void run(Ctx& c, std::integer_sequence<int, I...>) { (step<I>(c), ...); }
template <int B, int... I> __device__ __forceinline__ void run_from(Ctx& c, std::integer_sequence<int, I...>) { (step<B + I>(c), ...); }
constexpr int N_LAST = 3;

__global__ void __launch_bounds__(NTHR, 2)
mha_decode(const __bf16* __restrict__ wqA, const __bf16* __restrict__ wqB, const __bf16* __restrict__ kA,
           const __bf16* __restrict__ kB, const __bf16* __restrict__ xA, const __bf16* __restrict__ xB, unsigned char* ws,
           const __bf16* __restrict__ vA, const __bf16* __restrict__ vB, const __bf16* __restrict__ woA,
           const __bf16* __restrict__ woB, __bf16* out) {
  __shared__ __attribute__((aligned(16))) float lds[L_END];
  Ctx c;
  ST(0);
  unsigned long long tstart;
  tstart = __builtin_amdgcn_s_memrealtime();
  __builtin_amdgcn_sched_barrier(0);
  const int bid = blockIdx.x;
  const int rk = bid >> 3, bh = (bid & 7) * 4 + (rk >> 3), br = bh >> 4, head = bh & 15, b = (rk >> 2) & 1, split = rk & 3;
  const int grp = br * 2 + b, prod = head * 4 + split;
  const int tid = threadIdx.x, lane = tid & 63, w = __builtin_amdgcn_readfirstlane(tid >> 6);
  const size_t kvoff = ((size_t)(b * NH + head) * SK + split * 1024) * DH;
  c.rq = __builtin_amdgcn_make_buffer_rsrc((void*)((br ? wqB : wqA) + head * DH + (rk & 7) * 32), 0, (DM - 1) * 8192 + 64, RSRC_W3);
  c.rk = __builtin_amdgcn_make_buffer_rsrc((void*)((br ? kB : kA) + kvoff), 0, 1024 * 512, RSRC_W3);
  c.w = w; c.lane = lane; c.lds = lds;
  c.vo_q = (lane >> 2) * 8192 + (lane & 3) * 16;
  c.vo_v = lane * 16;
  c.so_kv = w * 4096 + ((ROT == 1 ? head + 8 * br : ROT == 2 ? split * 4 : ROT == 3 ? (head >> 2) + 4 * split + 2 * b + br : ROT == 4 ? split * 4 + (head & 3) + 2 * b + br : ROT == 5 ? split * 4 + b * 2 + (head & 1) : ROT == 6 ? split * 4 + 2 * br : ROT == 7 ? split * 4 + 2 * b + br : ROT == 8 ? head : ROT == 9 ? ((head >> 1) & 1) | (((head ^ (head >> 1)) & 1) << 1) | (b << 2) | ((((head >> 1) ^ (split >> 1)) & 1) << 3) : 0) & 15) * 32768;
  run(c, std::make_integer_sequence<int, 1>{});
  c.t0 = (unsigned)tstart;
  run_from<1>(c, std::make_integer_sequence<int, N_FIRST - 1>{});
  c.rx = __builtin_amdgcn_make_buffer_rsrc((void*)(br ? xB : xA), 0, 2 * DM * 2, RSRC_W3);
  c.rw = __builtin_amdgcn_make_buffer_rsrc((void*)ws, 0, WS_END, RSRC_W3);
  c.vo_x = (((lane & 1) * DM + 64 * w + 8 * (lane >> 4)) * 2) | (w < 7 ? 0 : OOB);
  c.ep_off = br * 256;
  c.xv[0] = __builtin_amdgcn_raw_buffer_load_b128(c.rx, c.vo_x, 0, 0);
  c.xv[1] = __builtin_amdgcn_raw_buffer_load_b128(c.rx, c.vo_x + 64, 0, 0);
  c.pep = (const __attribute__((address_space(4))) unsigned*)(ws + c.ep_off);
  __builtin_amdgcn_sched_barrier(0);
  int br2 = br;
  asm volatile("" : "+s"(br2));
  c.pv = (br2 ? vB : vA) + kvoff; c.po = (br2 ? woB : woA) + head * DH * DM; c.pout = out;
  c.rv = __builtin_amdgcn_make_buffer_rsrc((void*)c.pv, 0, 1024 * 512, RSRC_W3);
  c.ro = __builtin_amdgcn_make_buffer_rsrc((void*)c.po, 0, DH * DM * 2, RSRC_W3);
  c.rout = __builtin_amdgcn_make_buffer_rsrc((void*)out, 0, 4 * DM * 2, RSRC_W3);
  c.orot = OROT ? rk & 7 : 0;
  c.vo_o = ((lane >> 3) * (DM * 2) + w * 128 + (lane & 7) * 16) | (w < 7 ? 0 : OOB);
  c.ml_off = WS_ML + (grp * 16 + head) * 128;
  c.ml_st = (c.ml_off + split * 16) | (tid == 0 ? 0 : OOB);
  c.q_st = (WS_Q + bh * 2048 + (lane >> 5) * 1024 + (rk & 7) * 128 + (lane & 31) * 4) | ((tid & ~60) == 0 ? 0 : OOB);
  c.q_ld = WS_Q + bh * 2048 + b * 1024 + lane * 16;
  run_from<N_FIRST>(c, std::make_integer_sequence<int, S.n - N_FIRST - N_LAST>{});
  const int pk = WS_PKT + grp * 64 * PKT_BYTES;
  int a_wr = (L_RED + w * A_STR + (((c.dim4 >> 2) - 8 * c.orot) & 63)) * 4, a_rd = (L_RED + (lane & 15) * A_STR + 4 * (lane >> 4)) * 4;
  int idx = (lane & 8) ? 0xEEEE : 0x4444, y_off = (pk + prod * PKT_BYTES + tid * 4) | (w < 7 && !(lane & 1) ? 0 : OOB);
  int p_off = pk + (lane >> 2) * PKT_BYTES + prod * 64 + (lane & 3) * 16;
  int o_off = ((b * (2 * DM) + br * DM + prod * 16 + (lane & 3) * 4 + (lane >> 4)) * 2) | ((lane & 12) << 27);
  int e_off = (prod == 0 && b == 0 && lane == 0) ? c.ep_off : OOB;
  asm volatile("" : "+v"(a_wr), "+v"(a_rd), "+v"(idx), "+v"(y_off), "+v"(p_off), "+v"(o_off), "+v"(e_off));
  f32x4 y[4];
#pragma unroll
  for (int ct = 0; ct < 4; ++ct) { y[ct] = f32x4{0.f, 0.f, 0.f, 0.f}; asm volatile("" : "+v"(y[ct])); }
  run_from<S.n - N_LAST>(c, std::make_integer_sequence<int, N_LAST>{});
  ST(5);
  typedef __attribute__((address_space(3))) unsigned char lds_byte;
  lds_byte* const lb = (lds_byte*)lds;
  *(__attribute__((address_space(3))) unsigned*)(lb + a_wr) = cvtpk(c.acc.x * c.scale, c.acc.y * c.scale);
  *(__attribute__((address_space(3))) unsigned*)(lb + a_wr + 8 * A_STR * 4) = cvtpk(c.acc.z * c.scale, c.acc.w * c.scale);
  __syncthreads();
  ST(6);
  {
    v4u am[4];
#pragma unroll
    for (int ks = 0; ks < 4; ++ks) am[ks] = *(__attribute__((address_space(3))) v4u*)(lb + a_rd + 64 * ks);
    __builtin_amdgcn_sched_barrier(0);
#pragma unroll
    for (int ks = 0; ks < 4; ++ks)
#pragma unroll
      for (int ct = 0; ct < 4; ++ct) {
        const v2u b0 = c.of[2 * ks][ct][0], b1 = c.of[2 * ks][ct][1], b2 = c.of[2 * ks + 1][ct][0], b3 = c.of[2 * ks + 1][ct][1];
        const v8u bm = {b0.x, b0.y, b1.x, b1.y, b2.x, b2.y, b3.x, b3.y};
        y[ct] = __builtin_amdgcn_smfmac_f32_16x16x64_bf16(__builtin_bit_cast(bf16x8, am[ks]), __builtin_bit_cast(bf16x16, bm), y[ct], idx, 0, 0);
      }
    float sm[4];
#pragma unroll
    for (int ct = 0; ct < 4; ++ct) sm[ct] = (y[ct].x + y[ct].y) + (y[ct].z + y[ct].w);
    const auto u0 = __builtin_amdgcn_permlane16_swap(__float_as_uint(sm[0]), __float_as_uint(sm[1]), false, false);
    const auto u1 = __builtin_amdgcn_permlane16_swap(__float_as_uint(sm[2]), __float_as_uint(sm[3]), false, false);
    const float t0 = __uint_as_float(u0[0]) + __uint_as_float(u0[1]), t1 = __uint_as_float(u1[0]) + __uint_as_float(u1[1]);
    const auto u2 = __builtin_amdgcn_permlane32_swap(__float_as_uint(t0), __float_as_uint(t1), false, false);
    const float yv = __uint_as_float(u2[0]) + __uint_as_float(u2[1]);
    const v2u g = {(__float_as_uint(yv) & ~255u) | (c.tag & 255u), (__float_as_uint(dpp<XOR1>(yv)) & ~255u) | ((c.tag >> 8) & 255u)};
    __builtin_amdgcn_raw_buffer_store_b64(g, c.rw, y_off, 0, AUX_SC1);
    ST(8);
  }
  if (w == NW - 1 && prod < DM / 16) {
    v4u v[4], u[4];
    const unsigned t0 = c.tag & 255u, t1 = (c.tag >> 8) & 255u;
    bool got[4] = {false, false, false, false}, done = false;
#pragma unroll 1
    for (unsigned spins = 0; !done && spins < SPIN_CAP; ++spins) {
      asm volatile("" ::: "memory");
#pragma unroll
      for (int i = 0; i < 4; ++i)
        if (!got[i]) v[i] = __builtin_amdgcn_raw_buffer_load_b128(c.rw, p_off, i * 16 * PKT_BYTES, AUX_SC1);
#pragma unroll
      for (int i = 0; i < 4; ++i) {
        u[i] = v[i] ^ v4u{t0, t1, t0, t1};
        got[i] = __all(((u[i].x | u[i].y | u[i].z | u[i].w) & 255u) == 0u);
      }
      done = got[0] & got[1] & got[2] & got[3];
    }
    const f32x4 r = (__builtin_bit_cast(f32x4, u[0]) + __builtin_bit_cast(f32x4, u[1])) + (__builtin_bit_cast(f32x4, u[2]) + __builtin_bit_cast(f32x4, u[3]));
    const auto s0 = __builtin_amdgcn_permlane32_swap(__float_as_uint(r[0]), __float_as_uint(r[2]), false, false);
    const auto s1 = __builtin_amdgcn_permlane32_swap(__float_as_uint(r[1]), __float_as_uint(r[3]), false, false);
    float e0 = __uint_as_float(s0[0]) + __uint_as_float(s0[1]), e1 = __uint_as_float(s1[0]) + __uint_as_float(s1[1]);
    const auto s2 = __builtin_amdgcn_permlane16_swap(__float_as_uint(e0), __float_as_uint(e1), false, false);
    float z = __uint_as_float(s2[0]) + __uint_as_float(s2[1]);
    z += dpp<ROR4>(z);
    z += dpp<ROR8>(z);
    const __bf16 zb = (__bf16)z;
    __builtin_amdgcn_raw_buffer_store_b16(done ? __builtin_bit_cast(unsigned short, zb) : (unsigned short)0x7fc0u, c.rout, o_off, 0, 0);
    __builtin_amdgcn_raw_buffer_store_b32(c.tag, c.rw, e_off, 0, AUX_SC1);
    ST(9);
  }
}

}

extern "C" void kernel_launch(void* const* d_in, const int* in_sizes, int n_in, void* d_out, int out_size, void* d_ws,
                              size_t ws_size, hipStream_t stream) {
  (void)in_sizes; (void)n_in; (void)out_size; (void)ws_size;
  mha_decode<<<256, NTHR, 0, stream>>>((const __bf16*)d_in[3], (const __bf16*)d_in[8], (const __bf16*)d_in[1],
                                       (const __bf16*)d_in[6], (const __bf16*)d_in[0], (const __bf16*)d_in[5],
                                       (unsigned char*)d_ws, (const __bf16*)d_in[2], (const __bf16*)d_in[7],
                                       (const __bf16*)d_in[4], (const __bf16*)d_in[9], (__bf16*)d_out);
}
```

```cpp
#include <hip/hip_runtime.h>
#include <hip/hip_bf16.h>
#include <utility>

namespace {

typedef unsigned v2u __attribute__((ext_vector_type(2)));
typedef unsigned v4u __attribute__((ext_vector_type(4)));
typedef unsigned v8u __attribute__((ext_vector_type(8)));
typedef float f32x2 __attribute__((ext_vector_type(2)));
typedef float f32x4 __attribute__((ext_vector_type(4)));
typedef __bf16 bf16x2 __attribute__((ext_vector_type(2)));
typedef __bf16 bf16x8 __attribute__((ext_vector_type(8)));
typedef __bf16 bf16x16 __attribute__((ext_vector_type(16)));
typedef short i16x4 __attribute__((ext_vector_type(4)));
typedef __amdgpu_buffer_rsrc_t rsrc_t;
#define LDS_AS __attribute__((address_space(3)))
#define COMPILER_FENCE() asm volatile("" ::: "memory")

constexpr int DM = 448, NH = 16, SK = 4096, DH = 256;
constexpr int NW = 8, NTHR = NW * 64;
constexpr int N_Q = 4, N_K = 64, N_V = 64, N_O = 32;
constexpr unsigned SPIN_CAP = 1u << 20;
constexpr int RSRC_W3 = 0x00020000, AUX_NT = 2, AUX_SC1 = 16, OOB = 0x40000000;
constexpr int WS_PKT = 4096, PKT_BYTES = DM * 4, WS_Q = WS_PKT + 256 * PKT_BYTES, WS_ML = WS_Q + 64 * 1024,
              WS_END = WS_ML + 64 * 128;
constexpr int A_STR = 68;
constexpr int L_RED = 0, L_T = L_RED + 16 * A_STR, L_TQ = L_T + NW * 1024, L_ML = L_TQ + NW * 1024, L_P = L_ML + 2 * NW,
              L_F = L_P + NW * 64, L_END = L_F + NW * 2;

constexpr int WIN = 3;
constexpr int N_END = 8;
constexpr int O_FIRST = 4;
constexpr int O_START = 44, O_EVERY = 2;
constexpr int QMM_AT = 3, QPUB_AT = 6, QLD_AT = 14, QCHK_AT = QLD_AT + 4;
constexpr int BOOST_AT = 6, BOOST_MAX = 40, BOOST_T = 430, BOOST_G = 4;
constexpr int N_FIRST = N_Q + O_FIRST + 1, N_LAST = 3;

enum Op : int { IQ, IK, IV, IO, QPUB, QLD, MLPUB, PEERLD,
                WAITV, WAITB, LAG, SETUP, QTR, QMM, QCHK, CK, SM1, SM2, CV, PEERCHK, TRW, TRR };
struct Sched { int n; int op[640]; int a[640]; };
constexpr Sched make_sched() {
  Sched s{};
  int vmk[256] = {}, vmi[256] = {}, nvm = 0;
  auto emit = [&](int op, int a) {
    s.op[s.n] = op; s.a[s.n] = a; ++s.n;
    if (op <= PEERLD) { vmk[nvm] = op; vmi[nvm] = a; ++nvm; }
    if (op == SETUP) { vmk[nvm++] = QLD; vmk[nvm++] = QLD; }
  };
  int no = 0, nk = 0, nv = 0, nw = 0, nr = 0, sm = 0;
  int lk = 0, lv = 0, lo = 0;
  for (int j = 0; j < N_Q; ++j) emit(IQ, j);
  while (no < O_FIRST) emit(IO, no++);
  for (int j = 0; j < N_K + N_V; ++j) {
    if (j > 0) {
      const int win = j >= N_K + N_V - N_END ? WIN + 1 : WIN;
      int p = nvm, seen = 0;
      while (seen < win) { --p; if (vmk[p] <= IV) ++seen; }
      if (j - 1 > BOOST_AT && j - 1 <= BOOST_AT + BOOST_MAX) {
        int p2 = p;
        do --p2; while (vmk[p2] > IV);
        emit(WAITV, nvm - 1 - p2);
        emit(WAITB, (nvm - 1 - p) | ((j - 1 - BOOST_AT) << 8));
        p = p2;
      } else emit(WAITV, nvm - 1 - p);
      if (j - 1 == BOOST_AT) emit(LAG, 0);
      if (vmk[p] == IK) lk = vmi[p] + 1;
      if (vmk[p] == IV) { lk = N_K; lv = vmi[p] + 1; }
      for (int t = 0; t < p; ++t) if (vmk[t] == IO) lo = vmi[t] + 1;
    }
    if (j < N_K) emit(IK, j); else emit(IV, j - N_K);
    if (j == 0) emit(SETUP, 0);
    if (j >= O_START && (j - O_START) % O_EVERY == 0 && no < N_O) emit(IO, no++);
    if (j == QMM_AT - 1) emit(QTR, 0);
    if (j == QMM_AT) emit(QMM, 0);
    if (j == QPUB_AT) emit(QPUB, 0);
    if (j == QLD_AT) emit(QLD, 0);
    if (j == QCHK_AT) emit(QCHK, 0);
    else if (j > QCHK_AT && nk < N_K) { for (int t = 0; t < 3 && nk < lk; ++t) emit(CK, nk++); }
    else if (nk == N_K && sm < 2) { emit(sm == 0 ? SM1 : SM2, 0); ++sm; }
    else if (sm == 2) {
      for (int t = 0; t < 3 && nv < lv; ++t) {
        emit(CV, nv++);
        if (nv == 12) emit(MLPUB, 0);
        if (nv == N_V - 20) emit(PEERLD, 0);
        if (nv == N_V - 10) emit(PEERCHK, 0);
      }
    }
    if (nr < nw) emit(TRR, nr++);
    else if (4 * nw + 4 <= lo) emit(TRW, nw++);
  }
  if (no != N_O || nr != N_O / 4 || nv < N_V - 10 || nv > N_V - N_LAST) s.n = -1;
  else while (nv < N_V) emit(CV, nv++);
  return s;
}
constexpr Sched S = make_sched();
static_assert(S.n > 0, "the schedule must finish everything but the last V loads inside the stream");

__device__ __forceinline__ float dot2(unsigned a, unsigned b, float c) {
  return __builtin_amdgcn_fdot2_f32_bf16(__builtin_bit_cast(bf16x2, a), __builtin_bit_cast(bf16x2, b), c, false);
}
__device__ __forceinline__ unsigned cvtpk(float lo, float hi) {
  const bf16x2 r = {(__bf16)lo, (__bf16)hi};
  return __builtin_bit_cast(unsigned, r);
}
template <int CTRL> __device__ __forceinline__ float dpp(float x) {
  return __builtin_bit_cast(float, __builtin_amdgcn_mov_dpp(__builtin_bit_cast(int, x), CTRL, 0xf, 0xf, true));
}
constexpr int XOR1 = 0xB1, XOR2 = 0x4E, XOR7 = 0x141, XOR15 = 0x140, ROR4 = 0x124, ROR8 = 0x128;
__device__ __forceinline__ float sum8(float x) { x += dpp<XOR1>(x); x += dpp<XOR2>(x); x += dpp<XOR7>(x); return x; }
__device__ __forceinline__ float max8(float x) {
  x = fmaxf(x, dpp<XOR1>(x)); x = fmaxf(x, dpp<XOR2>(x)); x = fmaxf(x, dpp<XOR7>(x));
  return x;
}
__device__ __forceinline__ float add16(float a, float b) {
  const auto s = __builtin_amdgcn_permlane16_swap(__float_as_uint(a), __float_as_uint(b), false, false);
  return __uint_as_float(s[0]) + __uint_as_float(s[1]);
}
__device__ __forceinline__ float add32(float a, float b) {
  const auto s = __builtin_amdgcn_permlane32_swap(__float_as_uint(a), __float_as_uint(b), false, false);
  return __uint_as_float(s[0]) + __uint_as_float(s[1]);
}
template <int CTRL> __device__ __forceinline__ float tstep(float lo, float hi, bool sel) {
  const float a = lo + dpp<CTRL>(lo), b = hi + dpp<CTRL>(hi);
  return sel ? b : a;
}
__device__ __forceinline__ float wave_sum(float x) {
  x = sum8(x); x += dpp<XOR15>(x); x = add16(x, x);
  return add32(x, x);
}
__device__ __forceinline__ float wave_max(float x) {
  x = max8(x); x = fmaxf(x, dpp<XOR15>(x));
  const auto s = __builtin_amdgcn_permlane16_swap(__float_as_uint(x), __float_as_uint(x), false, false);
  x = fmaxf(__uint_as_float(s[0]), __uint_as_float(s[1]));
  const auto t = __builtin_amdgcn_permlane32_swap(__float_as_uint(x), __float_as_uint(x), false, false);
  return fmaxf(__uint_as_float(t[0]), __uint_as_float(t[1]));
}
__device__ __forceinline__ f32x4 mfma(v4u a, v4u b, f32x4 c) {
  return __builtin_amdgcn_mfma_f32_16x16x32_bf16(__builtin_bit_cast(bf16x8, a), __builtin_bit_cast(bf16x8, b), c, 0, 0, 0);
}
__device__ __forceinline__ v2u tr_read(const float* p) {
  return __builtin_bit_cast(v2u, __builtin_amdgcn_ds_read_tr16_b64_v4i16((LDS_AS i16x4*)p));
}
__device__ __forceinline__ void await_tags(v4u& g, rsrc_t r, int off, unsigned tag) {
  if (__builtin_expect(!__all((g.y == tag) & (g.w == tag)), 0)) {
    unsigned spins = 0;
    bool ok;
#pragma unroll 1
    do {
      COMPILER_FENCE();
      g = __builtin_amdgcn_raw_buffer_load_b128(r, off, 0, AUX_SC1);
      ok = __all((g.y == tag) & (g.w == tag));
    } while (!ok && ++spins < SPIN_CAP);
    if (!ok) g.x = 0x7fc07fc0u;
  }
}
__device__ __forceinline__ void flag_set(float* lds, int k, int w) {
  asm volatile("s_waitcnt lgkmcnt(0)" ::: "memory");
  ((LDS_AS volatile unsigned*)lds)[L_F + k * NW + w] = 1u;
}
__device__ __forceinline__ void flag_wait(float* lds, int k, int lane) {
  while (!__all(((LDS_AS volatile unsigned*)lds)[L_F + k * NW + (lane & 7)] != 0u)) {}
  COMPILER_FENCE();
}

struct Ctx {
  rsrc_t rq, rk, rv, ro, rw;
  float* lds;
  int w, lane, vo_q, vo_kv, vo_o, so_q, so_kv, so_o, orot, q_st, q_ld, ml_st, ml_ld, boost;
  unsigned t0, epoch, tag, sel[4];
  v4u qb[N_Q], kb[N_K], vb[N_V], ob[N_O], xv[2], qg, qr, pp, peer;
  v2u qf[2][2][2];
  v2u of[8][4][2];
  f32x4 acc;
  float xs[5], s0, s1, mw, scale;
};

template <int I> __device__ __forceinline__ void step(Ctx& c) {
  constexpr int op = S.op[I], a = S.a[I];
  float* const lds = c.lds;
  const int w = c.w, lane = c.lane;
  if constexpr (op == IQ) {
    c.qb[a] = __builtin_amdgcn_raw_buffer_load_b128(c.rq, c.vo_q, c.so_q + 16 * a * (NH * DH * 2), AUX_NT);
  } else if constexpr (op == IK) {
    c.kb[a] = __builtin_amdgcn_raw_buffer_load_b128(c.rk, c.vo_kv + (a & 3) * 1024, c.so_kv ^ ((a >> 2) * 32768), AUX_NT);
  } else if constexpr (op == IV) {
    c.vb[a] = __builtin_amdgcn_raw_buffer_load_b128(c.rv, c.vo_kv + (a & 3) * 1024, c.so_kv ^ ((a >> 2) * 32768), AUX_NT);
  } else if constexpr (op == IO) {
    c.ob[a] = __builtin_amdgcn_raw_buffer_load_b128(c.ro, c.vo_o, c.so_o + ((8 * a + 32 * c.orot) & 255) * (DM * 2), 0);
  } else if constexpr (op == WAITV) {
    __builtin_amdgcn_s_waitcnt((a & 15) | ((a >> 4) << 14) | 0x0F70);
  } else if constexpr (op == LAG) {
    c.boost = ((int)((unsigned)__builtin_amdgcn_s_memrealtime() - c.t0) - BOOST_T) * BOOST_G >> 4;
  } else if constexpr (op == WAITB) {
    asm volatile("s_cmp_ge_i32 %0, %1\n\ts_cbranch_scc1 1f\n\ts_waitcnt vmcnt(%2)\n1:"
                 :: "s"(c.boost), "i"(a >> 8), "i"(a & 255) : "scc", "memory");
  } else if constexpr (op == SETUP) {
    const int row = lane & 15, j = row & 7;
    const bool act = ((lane >> 4) & 1) == (row >> 3);
    const unsigned two = (lane >> 5) ? 0x0302u : 0x0100u;
    const unsigned s = (j & 1) ? ((two << 16) | 0x0c0cu) : (0x0c0c0000u | two);
#pragma unroll
    for (int i = 0; i < 4; ++i) c.sel[i] = (act && (j >> 1) == i) ? s : 0x0c0c0c0cu;
    c.acc = f32x4{0.f, 0.f, 0.f, 0.f};
    ((unsigned*)lds)[L_F + (lane & 1) * NW + w] = 0u;
    __syncthreads();
  } else if constexpr (op == QTR) {
#pragma unroll
    for (int i = 0; i < 4; ++i) *(v4u*)&lds[L_TQ + w * 1024 + i * 256 + lane * 4] = c.qb[i];
    COMPILER_FENCE();
    const float* base = &lds[L_TQ + w * 1024 + (lane >> 4) * 128 + ((lane >> 2) & 3) * 16 + (lane & 3) * 2];
#pragma unroll
    for (int ks = 0; ks < 2; ++ks)
#pragma unroll
      for (int ct = 0; ct < 2; ++ct)
#pragma unroll
        for (int h = 0; h < 2; ++h) c.qf[ks][ct][h] = tr_read(base + ks * 512 + h * 64 + ct * 8);
    COMPILER_FENCE();
  } else if constexpr (op == QMM) {
    f32x4 d[2];
#pragma unroll
    for (int ct = 0; ct < 2; ++ct) {
      d[ct] = f32x4{0.f, 0.f, 0.f, 0.f};
#pragma unroll
      for (int ks = 0; ks < 2; ++ks) {
        const v4u bm = {c.qf[ks][ct][0].x, c.qf[ks][ct][0].y, c.qf[ks][ct][1].x, c.qf[ks][ct][1].y};
        d[ct] = mfma(c.xv[ks], bm, d[ct]);
      }
    }
    c.tag = c.epoch * 0x9E3779B1u + 0x7F4A7C15u;
    asm volatile("" : "+s"(c.tag));
    const float lo = (lane & 16) ? d[1].x : d[0].x, hi = (lane & 16) ? d[1].y : d[0].y;
    lds[L_RED + w * 64 + lane] = (lane & 32) ? hi : lo;
    flag_set(lds, 0, w);
  } else if constexpr (op == QPUB) {
    flag_wait(lds, 0, lane);
    float t = 0.f;
#pragma unroll
    for (int i = 0; i < 7; ++i) t += lds[L_RED + i * 64 + lane];
    const unsigned pr = cvtpk(t, dpp<XOR1>(t));
    const v4u g = {pr, c.tag, __float_as_uint(dpp<XOR2>(__uint_as_float(pr))), c.tag};
    __builtin_amdgcn_raw_buffer_store_b128(g, c.rw, c.q_st, 0, AUX_SC1);
  } else if constexpr (op == QLD) {
    c.qg = __builtin_amdgcn_raw_buffer_load_b128(c.rw, c.q_ld, 0, AUX_SC1);
  } else if constexpr (op == QCHK) {
    await_tags(c.qg, c.rw, c.q_ld, c.tag);
    *(v2u*)&lds[L_TQ + w * 1024 + lane * 2] = v2u{c.qg.x, c.qg.z};
    COMPILER_FENCE();
    c.qr = *(const v4u*)&lds[L_TQ + w * 1024 + (lane & 31) * 4];
  } else if constexpr (op == CK) {
    const v4u k = c.kb[a], q = c.qr;
    float x = dot2(k.x, q.x, dot2(k.z, q.z, 0.f)) + dot2(k.y, q.y, dot2(k.w, q.w, 0.f));
    if constexpr ((a & 1) == 1) x = add16(c.xs[0], x);
    if constexpr ((a & 3) == 3) x = tstep<ROR8>(c.xs[1], x, lane & 8);
    if constexpr ((a & 7) == 7) x = tstep<XOR7>(c.xs[2], x, lane & 4);
    if constexpr ((a & 15) == 15) x = tstep<XOR2>(c.xs[3], x, lane & 2);
    if constexpr ((a & 31) == 31) (a == 31 ? c.s0 : c.s1) = tstep<XOR1>(c.xs[4], x, lane & 1) * (0.0625f * 1.4426950408889634f);
    else c.xs[__builtin_ctz(~a)] = x;
  } else if constexpr (op == SM1) {
    const float m = wave_max(fmaxf(c.s0, c.s1));
    const float p0 = __builtin_amdgcn_exp2f(c.s0 - m), p1 = __builtin_amdgcn_exp2f(c.s1 - m);
    __bf16* pw = (__bf16*)&lds[L_P + w * 64] + (__builtin_bitreverse32(lane) >> 26);
    pw[0] = (__bf16)p0; pw[64] = (__bf16)p1;
    COMPILER_FENCE();
    c.mw = m; c.s0 = p0 + p1;
    c.pp = *(const v4u*)&lds[L_P + w * 64];
  } else if constexpr (op == SM2) {
    const float l = wave_sum(c.s0);
    lds[L_ML + 2 * w + (lane & 1)] = (lane & 1) ? l : c.mw;
    flag_set(lds, 1, w);
  } else if constexpr (op == MLPUB) {
    flag_wait(lds, 1, lane);
    const f32x2 ml = *(const f32x2*)&lds[L_ML + 2 * (lane & 7)];
    const float M = max8(ml.x), L = sum8(ml.y * __builtin_amdgcn_exp2f(ml.x - M));
    const v4u g = {__float_as_uint(M), c.tag, __float_as_uint(L), c.tag};
    __builtin_amdgcn_raw_buffer_store_b128(g, c.rw, c.ml_st, 0, AUX_SC1);
  } else if constexpr (op == CV) {
    const unsigned pr = c.pp[a & 3];
    const v4u am = {__builtin_amdgcn_perm(0u, pr, c.sel[0]), __builtin_amdgcn_perm(0u, pr, c.sel[1]),
                    __builtin_amdgcn_perm(0u, pr, c.sel[2]), __builtin_amdgcn_perm(0u, pr, c.sel[3])};
    c.acc = mfma(am, c.vb[a], c.acc);
    if constexpr ((a & 3) == 3 && a + 1 < N_V) c.pp = *(const v4u*)&lds[L_P + w * 64 + (a + 1)];
  } else if constexpr (op == PEERLD) {
    c.peer = __builtin_amdgcn_raw_buffer_load_b128(c.rw, c.ml_ld, 0, AUX_SC1);
  } else if constexpr (op == PEERCHK) {
    await_tags(c.peer, c.rw, c.ml_ld, c.tag);
    const float m = __uint_as_float(c.peer.x);
    float M = fmaxf(m, dpp<XOR1>(m)); M = fmaxf(M, dpp<XOR2>(M));
    float L = __uint_as_float(c.peer.z) * __builtin_amdgcn_exp2f(m - M); L += dpp<XOR1>(L); L += dpp<XOR2>(L);
    c.scale = __builtin_amdgcn_exp2f(c.mw - M) / L;
  } else if constexpr (op == TRW) {
#pragma unroll
    for (int i = 0; i < 4; ++i) *(v4u*)&lds[L_T + w * 1024 + i * 256 + lane * 4] = c.ob[4 * a + i];
    COMPILER_FENCE();
  } else if constexpr (op == TRR) {
    const float* base = &lds[L_T + w * 1024 + (lane >> 4) * 256 + ((lane >> 2) & 3) * 32 + (lane & 3) * 2];
#pragma unroll
    for (int ct = 0; ct < 4; ++ct)
#pragma unroll
      for (int h = 0; h < 2; ++h) c.of[a][ct][h] = tr_read(base + h * 128 + ct * 8);
    COMPILER_FENCE();
  }
  __builtin_amdgcn_sched_barrier(0);
}
template <int B, int... I> __device__ __forceinline__ void run_seq(Ctx& c, std::integer_sequence<int, I...>) { (step<B + I>(c), ...); }
template <int B, int E> __device__ __forceinline__ void run(Ctx& c) { run_seq<B>(c, std::make_integer_sequence<int, E - B>{}); }

__global__ void __launch_bounds__(NTHR, 2)
mha_decode(const __bf16* __restrict__ xA, const __bf16* __restrict__ kA, const __bf16* __restrict__ vA,
           const __bf16* __restrict__ wqA, const __bf16* __restrict__ woA,
           const __bf16* __restrict__ xB, const __bf16* __restrict__ kB, const __bf16* __restrict__ vB,
           const __bf16* __restrict__ wqB, const __bf16* __restrict__ woB, __bf16* out, unsigned char* ws) {
  __shared__ __attribute__((aligned(16))) float lds[L_END];
  Ctx c;
  const unsigned long long tstart = __builtin_amdgcn_s_memrealtime();
  __builtin_amdgcn_sched_barrier(0);
  const int bid = blockIdx.x, rk = bid >> 3, bh = (bid & 7) * 4 + (rk >> 3), br = bh >> 4, head = bh & 15;
  const int b = (rk >> 2) & 1, split = rk & 3, grp = br * 2 + b, prod = head * 4 + split;
  const int tid = threadIdx.x, lane = tid & 63, w = __builtin_amdgcn_readfirstlane(tid >> 6);
  const int off7 = w < 7 ? 0 : OOB;
  c.w = w; c.lane = lane; c.lds = lds;
  c.t0 = (unsigned)tstart;
  c.vo_q = ((lane >> 2) * (NH * DH * 2) + (lane & 3) * 16) | off7;
  c.so_q = (64 * w * NH * DH + head * DH + (rk & 7) * 32) * 2;
  c.vo_kv = lane * 16;
  c.so_kv = ((b * NH + head) * SK + split * 1024) * (DH * 2) + w * 4096 + (head ^ (8 * br)) * 32768;
  c.vo_o = ((lane >> 3) * (DM * 2) + w * 128 + (lane & 7) * 16) | off7;
  c.so_o = head * DH * DM * 2;
  c.orot = rk & 7;
  int ep_off = br * 256;
  asm volatile("" : "+s"(c.so_q), "+s"(c.so_kv), "+s"(c.so_o), "+s"(c.orot), "+s"(ep_off), "+v"(c.vo_q), "+v"(c.vo_kv), "+v"(c.vo_o)
               : "s"(tstart));
  __builtin_amdgcn_sched_barrier(0);
  c.rq = __builtin_amdgcn_make_buffer_rsrc((void*)(br ? wqB : wqA), 0, DM * NH * DH * 2, RSRC_W3);
  c.ro = __builtin_amdgcn_make_buffer_rsrc((void*)(br ? woB : woA), 0, NH * DH * DM * 2, RSRC_W3);
  c.rk = __builtin_amdgcn_make_buffer_rsrc((void*)(br ? kB : kA), 0, 2 * NH * SK * DH * 2, RSRC_W3);
  run<0, 1>(c);
  c.epoch = *(const __attribute__((address_space(4))) unsigned*)(ws + ep_off);
  run<1, N_FIRST>(c);
  int br2 = br;
  asm volatile("" : "+s"(br2));
  const rsrc_t rx = __builtin_amdgcn_make_buffer_rsrc((void*)(br2 ? xB : xA), 0, 2 * DM * 2, RSRC_W3);
  const int vo_x = (((lane & 1) * DM + 64 * w + 8 * (lane >> 4)) * 2) | off7;
  c.xv[0] = __builtin_amdgcn_raw_buffer_load_b128(rx, vo_x, 0, 0);
  c.xv[1] = __builtin_amdgcn_raw_buffer_load_b128(rx, vo_x + 64, 0, 0);
  __builtin_amdgcn_sched_barrier(0);
  c.rv = __builtin_amdgcn_make_buffer_rsrc((void*)(br2 ? vB : vA), 0, 2 * NH * SK * DH * 2, RSRC_W3);
  c.rw = __builtin_amdgcn_make_buffer_rsrc((void*)ws, 0, WS_END, RSRC_W3);
  const rsrc_t rout = __builtin_amdgcn_make_buffer_rsrc((void*)out, 0, 4 * DM * 2, RSRC_W3);
  c.q_st = (WS_Q + bh * 2048 + (lane >> 5) * 1024 + (rk & 7) * 128 + (lane & 31) * 4) | ((tid & ~60) == 0 ? 0 : OOB);
  c.q_ld = WS_Q + bh * 2048 + b * 1024 + lane * 16;
  c.ml_st = (WS_ML + (grp * NH + head) * 128 + split * 16) | (tid == 0 ? 0 : OOB);
  c.ml_ld = WS_ML + (grp * NH + head) * 128 + (lane & 3) * 16;

  run<N_FIRST, S.n - N_LAST>(c);

  const int pk = WS_PKT + grp * 64 * PKT_BYTES;
  const int dq = 32 * (lane >> 5) + 2 * (lane & 15) + ((lane >> 4) & 1);
  int a_wr = (L_RED + w * A_STR + ((dq - 8 * c.orot) & 63)) * 4, a_rd = (L_RED + (lane & 15) * A_STR + 4 * (lane >> 4)) * 4;
  int idx = (lane & 8) ? 0xEEEE : 0x4444;
  int y_off = (pk + prod * PKT_BYTES + tid * 4) | off7 | ((lane & 1) ? OOB : 0);
  int p_off = pk + (lane >> 2) * PKT_BYTES + prod * 64 + (lane & 3) * 16;
  int o_off = ((b * (2 * DM) + br * DM + prod * 16 + (lane & 3) * 4 + (lane >> 4)) * 2) | ((lane & 12) ? OOB : 0);
  int e_off = (prod == 0 && b == 0 && lane == 0) ? ep_off : OOB;
  asm volatile("" : "+v"(a_wr), "+v"(a_rd), "+v"(idx), "+v"(y_off), "+v"(p_off), "+v"(o_off), "+v"(e_off));
  f32x4 y[4];
#pragma unroll
  for (int ct = 0; ct < 4; ++ct) { y[ct] = f32x4{0.f, 0.f, 0.f, 0.f}; asm volatile("" : "+v"(y[ct])); }

  run<S.n - N_LAST, S.n>(c);
  LDS_AS unsigned char* const lb = (LDS_AS unsigned char*)lds;
  *(LDS_AS unsigned*)(lb + a_wr) = cvtpk(c.acc.x * c.scale, c.acc.y * c.scale);
  *(LDS_AS unsigned*)(lb + a_wr + 8 * A_STR * 4) = cvtpk(c.acc.z * c.scale, c.acc.w * c.scale);
  __syncthreads();
  v4u am[4];
#pragma unroll
  for (int ks = 0; ks < 4; ++ks) am[ks] = *(LDS_AS v4u*)(lb + a_rd + 64 * ks);
  __builtin_amdgcn_sched_barrier(0);
#pragma unroll
  for (int ks = 0; ks < 4; ++ks)
#pragma unroll
    for (int ct = 0; ct < 4; ++ct) {
      const v2u b0 = c.of[2 * ks][ct][0], b1 = c.of[2 * ks][ct][1], b2 = c.of[2 * ks + 1][ct][0], b3 = c.of[2 * ks + 1][ct][1];
      const v8u bm = {b0.x, b0.y, b1.x, b1.y, b2.x, b2.y, b3.x, b3.y};
      y[ct] = __builtin_amdgcn_smfmac_f32_16x16x64_bf16(__builtin_bit_cast(bf16x8, am[ks]), __builtin_bit_cast(bf16x16, bm), y[ct], idx, 0, 0);
    }
  float sm[4];
#pragma unroll
  for (int ct = 0; ct < 4; ++ct) sm[ct] = (y[ct].x + y[ct].y) + (y[ct].z + y[ct].w);
  const float yv = add32(add16(sm[0], sm[1]), add16(sm[2], sm[3]));
  const unsigned t0 = c.tag & 255u, t1 = (c.tag >> 8) & 255u;
  const v2u g = {(__float_as_uint(yv) & ~255u) | t0, (__float_as_uint(dpp<XOR1>(yv)) & ~255u) | t1};
  __builtin_amdgcn_raw_buffer_store_b64(g, c.rw, y_off, 0, AUX_SC1);

  if (w == NW - 1 && prod < DM / 16) {
    v4u v[4], u[4];
    bool got[4] = {false, false, false, false}, done = false;
#pragma unroll 1
    for (unsigned spins = 0; !done && spins < SPIN_CAP; ++spins) {
      COMPILER_FENCE();
#pragma unroll
      for (int i = 0; i < 4; ++i)
        if (!got[i]) v[i] = __builtin_amdgcn_raw_buffer_load_b128(c.rw, p_off, i * 16 * PKT_BYTES, AUX_SC1);
#pragma unroll
      for (int i = 0; i < 4; ++i) {
        u[i] = v[i] ^ v4u{t0, t1, t0, t1};
        got[i] = __all(((u[i].x | u[i].y | u[i].z | u[i].w) & 255u) == 0u);
      }
      done = got[0] & got[1] & got[2] & got[3];
    }
    const f32x4 r = (__builtin_bit_cast(f32x4, u[0]) + __builtin_bit_cast(f32x4, u[1])) +
                    (__builtin_bit_cast(f32x4, u[2]) + __builtin_bit_cast(f32x4, u[3]));
    float z = add16(add32(r.x, r.z), add32(r.y, r.w));
    z += dpp<ROR4>(z);
    z += dpp<ROR8>(z);
    const __bf16 zb = (__bf16)z;
    __builtin_amdgcn_raw_buffer_store_b16(done ? __builtin_bit_cast(unsigned short, zb) : (unsigned short)0x7fc0u, rout, o_off, 0, 0);
    __builtin_amdgcn_raw_buffer_store_b32(c.tag, c.rw, e_off, 0, AUX_SC1);
  }
}

}

extern "C" void kernel_launch(void* const* d_in, const int* in_sizes, int n_in, void* d_out, int out_size, void* d_ws,
                              size_t ws_size, hipStream_t stream) {
  (void)in_sizes; (void)n_in; (void)out_size; (void)ws_size;
  mha_decode<<<256, NTHR, 0, stream>>>((const __bf16*)d_in[0], (const __bf16*)d_in[1], (const __bf16*)d_in[2],
                                       (const __bf16*)d_in[3], (const __bf16*)d_in[4], (const __bf16*)d_in[5],
                                       (const __bf16*)d_in[6], (const __bf16*)d_in[7], (const __bf16*)d_in[8],
                                       (const __bf16*)d_in[9], (__bf16*)d_out, (unsigned char*)d_ws);
}
```

```cpp
#include <hip/hip_runtime.h>
#include <hip/hip_bf16.h>
#include <utility>

namespace {

typedef unsigned v2u __attribute__((ext_vector_type(2)));
typedef unsigned v4u __attribute__((ext_vector_type(4)));
typedef unsigned v8u __attribute__((ext_vector_type(8)));
typedef float f32x2 __attribute__((ext_vector_type(2)));
typedef float f32x4 __attribute__((ext_vector_type(4)));
typedef __bf16 bf16x2 __attribute__((ext_vector_type(2)));
typedef __bf16 bf16x8 __attribute__((ext_vector_type(8)));
typedef __bf16 bf16x16 __attribute__((ext_vector_type(16)));
typedef short i16x4 __attribute__((ext_vector_type(4)));
typedef __amdgpu_buffer_rsrc_t rsrc_t;
#define LDS_AS __attribute__((address_space(3)))
#define COMPILER_FENCE() asm volatile("" ::: "memory")

constexpr int DM = 448, NH = 16, SK = 4096, DH = 256;
constexpr int NW = 8, NTHR = NW * 64;
constexpr int N_Q = 4, N_K = 64, N_V = 64, N_O = 32;
constexpr unsigned SPIN_CAP = 1u << 20;
constexpr int RSRC_W3 = 0x00020000, AUX_NT = 2, AUX_SC1 = 16, OOB = 0x40000000;
constexpr int WS_PKT = 4096, PKT_BYTES = DM * 4, WS_Q = WS_PKT + 256 * PKT_BYTES, WS_ML = WS_Q + 64 * 1024,
              WS_END = WS_ML + 64 * 128;
constexpr int A_STR = 68;
constexpr int L_RED = 0, L_T = L_RED + 16 * A_STR, L_TQ = L_T + NW * 1024, L_ML = L_TQ + NW * 1024, L_P = L_ML + 2 * NW,
              L_F = L_P + NW * 64, L_END = L_F + NW * 2;

constexpr int WIN = 3;
constexpr int N_END = 8;
constexpr int O_FIRST = 4;
constexpr int O_START = 44, O_EVERY = 2;
constexpr int X_AT = 2, QMM_AT = 6, QPUB_AT = 9, QLD_AT = 17, QCHK_AT = QLD_AT + 4, YPF_AT = 100;
constexpr int BOOST_AT = 6, BOOST_MAX = 40, BOOST_T = 440, BOOST_G = 4;
constexpr int N_FIRST = N_Q + O_FIRST + 1, N_LAST = 3;

enum Op : int { IQ, IK, IV, IO, IX, QPUB, QLD, MLPUB, YPF, PEERLD,
                WAITV, WAITB, LAG, SETUP, QTR, QMM, QCHK, CK, SM1, SM2, CV, PEERCHK, TRW, TRR };
struct Sched { int n; int op[640]; int a[640]; };
constexpr Sched make_sched() {
  Sched s{};
  int vmk[256] = {}, vmi[256] = {}, nvm = 0;
  auto emit = [&](int op, int a) {
    s.op[s.n] = op; s.a[s.n] = a; ++s.n;
    if (op <= PEERLD) { vmk[nvm] = op; vmi[nvm] = a; ++nvm; }
  };
  int no = 0, nk = 0, nv = 0, nw = 0, nr = 0, sm = 0;
  int lk = 0, lv = 0, lo = 0;
  for (int j = 0; j < N_Q; ++j) emit(IQ, j);
  while (no < O_FIRST) emit(IO, no++);
  for (int j = 0; j < N_K + N_V; ++j) {
    if (j > 0) {
      const int win = j >= N_K + N_V - N_END ? WIN + 1 : WIN;
      int p = nvm, seen = 0;
      while (seen < win) { --p; if (vmk[p] <= IV) ++seen; }
      if (j - 1 > BOOST_AT && j - 1 <= BOOST_AT + BOOST_MAX) {
        int p2 = p;
        do --p2; while (vmk[p2] > IV);
        emit(WAITV, nvm - 1 - p2);
        emit(WAITB, (nvm - 1 - p) | ((j - 1 - BOOST_AT) << 8));
        p = p2;
      } else emit(WAITV, nvm - 1 - p);
      if (j - 1 == BOOST_AT) emit(LAG, 0);
      if (vmk[p] == IK) lk = vmi[p] + 1;
      if (vmk[p] == IV) { lk = N_K; lv = vmi[p] + 1; }
      for (int t = 0; t < p; ++t) if (vmk[t] == IO) lo = vmi[t] + 1;
    }
    if (j < N_K) emit(IK, j); else emit(IV, j - N_K);
    if (j == 0) emit(SETUP, 0);
    if (j >= O_START && (j - O_START) % O_EVERY == 0 && no < N_O) emit(IO, no++);
    if (j == X_AT) { emit(IX, 0); emit(IX, 1); }
    if (j == YPF_AT) emit(YPF, 0);
    if (j == QMM_AT - 1) emit(QTR, 0);
    if (j == QMM_AT) emit(QMM, 0);
    if (j == QPUB_AT) emit(QPUB, 0);
    if (j == QLD_AT) emit(QLD, 0);
    if (j == QCHK_AT) emit(QCHK, 0);
    else if (j > QCHK_AT && nk < N_K) { for (int t = 0; t < 3 && nk < lk; ++t) emit(CK, nk++); }
    else if (nk == N_K && sm < 2) { emit(sm == 0 ? SM1 : SM2, 0); ++sm; }
    else if (sm == 2) {
      for (int t = 0; t < 3 && nv < lv; ++t) {
        emit(CV, nv++);
        if (nv == 12) emit(MLPUB, 0);
        if (nv == N_V - 20) emit(PEERLD, 0);
        if (nv == N_V - 10) emit(PEERCHK, 0);
      }
    }
    if (nr < nw) emit(TRR, nr++);
    else if (4 * nw + 4 <= lo) emit(TRW, nw++);
  }
  if (no != N_O || nr != N_O / 4 || nv < N_V - 10 || nv > N_V - N_LAST) s.n = -1;
  else while (nv < N_V) emit(CV, nv++);
  return s;
}
constexpr Sched S = make_sched();
static_assert(S.n > 0, "the schedule must finish everything but the last V loads inside the stream");

constexpr int group_off(int g) { return ((g & 3) << 16) | ((g >> 2) << 19); }

__device__ __forceinline__ float dot2(unsigned a, unsigned b, float c) {
  return __builtin_amdgcn_fdot2_f32_bf16(__builtin_bit_cast(bf16x2, a), __builtin_bit_cast(bf16x2, b), c, false);
}
__device__ __forceinline__ unsigned cvtpk(float lo, float hi) {
  const bf16x2 r = {(__bf16)lo, (__bf16)hi};
  return __builtin_bit_cast(unsigned, r);
}
template <int CTRL> __device__ __forceinline__ float dpp(float x) {
  return __builtin_bit_cast(float, __builtin_amdgcn_mov_dpp(__builtin_bit_cast(int, x), CTRL, 0xf, 0xf, true));
}
constexpr int XOR1 = 0xB1, XOR2 = 0x4E, XOR7 = 0x141, XOR15 = 0x140, ROR4 = 0x124, ROR8 = 0x128;
__device__ __forceinline__ float sum8(float x) { x += dpp<XOR1>(x); x += dpp<XOR2>(x); x += dpp<XOR7>(x); return x; }
__device__ __forceinline__ float max8(float x) {
  x = fmaxf(x, dpp<XOR1>(x)); x = fmaxf(x, dpp<XOR2>(x)); x = fmaxf(x, dpp<XOR7>(x));
  return x;
}
__device__ __forceinline__ float add16(float a, float b) {
  const auto s = __builtin_amdgcn_permlane16_swap(__float_as_uint(a), __float_as_uint(b), false, false);
  return __uint_as_float(s[0]) + __uint_as_float(s[1]);
}
__device__ __forceinline__ float add32(float a, float b) {
  const auto s = __builtin_amdgcn_permlane32_swap(__float_as_uint(a), __float_as_uint(b), false, false);
  return __uint_as_float(s[0]) + __uint_as_float(s[1]);
}
template <int CTRL> __device__ __forceinline__ float tstep(float lo, float hi, bool sel) {
  const float a = lo + dpp<CTRL>(lo), b = hi + dpp<CTRL>(hi);
  return sel ? b : a;
}
__device__ __forceinline__ float wave_sum(float x) {
  x = sum8(x); x += dpp<XOR15>(x); x = add16(x, x);
  return add32(x, x);
}
__device__ __forceinline__ float wave_max(float x) {
  x = max8(x); x = fmaxf(x, dpp<XOR15>(x));
  const auto s = __builtin_amdgcn_permlane16_swap(__float_as_uint(x), __float_as_uint(x), false, false);
  x = fmaxf(__uint_as_float(s[0]), __uint_as_float(s[1]));
  const auto t = __builtin_amdgcn_permlane32_swap(__float_as_uint(x), __float_as_uint(x), false, false);
  return fmaxf(__uint_as_float(t[0]), __uint_as_float(t[1]));
}
__device__ __forceinline__ f32x4 mfma(v4u a, v4u b, f32x4 c) {
  return __builtin_amdgcn_mfma_f32_16x16x32_bf16(__builtin_bit_cast(bf16x8, a), __builtin_bit_cast(bf16x8, b), c, 0, 0, 0);
}
__device__ __forceinline__ v2u tr_read(const float* p) {
  return __builtin_bit_cast(v2u, __builtin_amdgcn_ds_read_tr16_b64_v4i16((LDS_AS i16x4*)p));
}
__device__ __forceinline__ void await_tags(v4u& g, rsrc_t r, int off, unsigned tag) {
  if (__builtin_expect(!__all((g.y == tag) & (g.w == tag)), 0)) {
    unsigned spins = 0;
    bool ok;
#pragma unroll 1
    do {
      COMPILER_FENCE();
      g = __builtin_amdgcn_raw_buffer_load_b128(r, off, 0, AUX_SC1);
      ok = __all((g.y == tag) & (g.w == tag));
    } while (!ok && ++spins < SPIN_CAP);
    if (!ok) g.x = 0x7fc07fc0u;
  }
}
__device__ __forceinline__ void flag_set(float* lds, int k, int w) {
  asm volatile("s_waitcnt lgkmcnt(0)" ::: "memory");
  ((LDS_AS volatile unsigned*)lds)[L_F + k * NW + w] = 1u;
}
__device__ __forceinline__ void flag_wait(float* lds, int k, int lane) {
  while (!__all(((LDS_AS volatile unsigned*)lds)[L_F + k * NW + (lane & 7)] != 0u)) {}
  COMPILER_FENCE();
}

struct Ctx {
  rsrc_t rq, rk, rv, ro, rx, rw;
  float* lds;
  int w, lane, vo_q, vo_kv, vo_o, vo_x, so_q, so_kv, so_o, orot, q_st, q_ld, ml_st, ml_ld, y_pf, boost;
  unsigned t0, epoch, tag, pf, sel[4];
  v4u qb[N_Q], kb[N_K], vb[N_V], ob[N_O], xv[2], qg, qr, pp, peer;
  v2u qf[2][2][2];
  v2u of[8][4][2];
  f32x4 acc;
  float xs[5], s0, s1, mw, scale;
};

template <int I> __device__ __forceinline__ void step(Ctx& c) {
  constexpr int op = S.op[I], a = S.a[I];
  float* const lds = c.lds;
  const int w = c.w, lane = c.lane;
  if constexpr (op == IQ) {
    c.qb[a] = __builtin_amdgcn_raw_buffer_load_b128(c.rq, c.vo_q, c.so_q + 16 * a * (NH * DH * 2), AUX_NT);
  } else if constexpr (op == IK) {
    c.kb[a] = __builtin_amdgcn_raw_buffer_load_b128(c.rk, c.vo_kv + (a & 3) * 1024, c.so_kv ^ group_off(a >> 2), AUX_NT);
  } else if constexpr (op == IV) {
    c.vb[a] = __builtin_amdgcn_raw_buffer_load_b128(c.rv, c.vo_kv + (a & 3) * 1024, c.so_kv ^ group_off(a >> 2), AUX_NT);
  } else if constexpr (op == IO) {
    c.ob[a] = __builtin_amdgcn_raw_buffer_load_b128(c.ro, c.vo_o, c.so_o + ((8 * a + 32 * c.orot) & 255) * (DM * 2), 0);
  } else if constexpr (op == IX) {
    c.xv[a] = __builtin_amdgcn_raw_buffer_load_b128(c.rx, c.vo_x + 64 * a, 0, 0);
  } else if constexpr (op == WAITV) {
    __builtin_amdgcn_s_waitcnt((a & 15) | ((a >> 4) << 14) | 0x0F70);
  } else if constexpr (op == LAG) {
    c.boost = ((int)((unsigned)__builtin_amdgcn_s_memrealtime() - c.t0) - BOOST_T) * BOOST_G >> 4;
  } else if constexpr (op == WAITB) {
    asm volatile("s_cmp_ge_i32 %0, %1\n\ts_cbranch_scc1 1f\n\ts_waitcnt vmcnt(%2)\n1:"
                 :: "s"(c.boost), "i"(a >> 8), "i"(a & 255) : "scc", "memory");
  } else if constexpr (op == SETUP) {
    const int row = lane & 15, j = row & 7;
    const bool act = ((lane >> 4) & 1) == (row >> 3);
    const unsigned two = (lane >> 5) ? 0x0302u : 0x0100u;
    const unsigned s = (j & 1) ? ((two << 16) | 0x0c0cu) : (0x0c0c0000u | two);
#pragma unroll
    for (int i = 0; i < 4; ++i) c.sel[i] = (act && (j >> 1) == i) ? s : 0x0c0c0c0cu;
    c.acc = f32x4{0.f, 0.f, 0.f, 0.f};
    ((unsigned*)lds)[L_F + (lane & 1) * NW + w] = 0u;
    __syncthreads();
  } else if constexpr (op == QTR) {
#pragma unroll
    for (int i = 0; i < 4; ++i) *(v4u*)&lds[L_TQ + w * 1024 + i * 256 + lane * 4] = c.qb[i];
    COMPILER_FENCE();
    const float* base = &lds[L_TQ + w * 1024 + (lane >> 4) * 128 + ((lane >> 2) & 3) * 16 + (lane & 3) * 2];
#pragma unroll
    for (int ks = 0; ks < 2; ++ks)
#pragma unroll
      for (int ct = 0; ct < 2; ++ct)
#pragma unroll
        for (int h = 0; h < 2; ++h) c.qf[ks][ct][h] = tr_read(base + ks * 512 + h * 64 + ct * 8);
    COMPILER_FENCE();
  } else if constexpr (op == QMM) {
    f32x4 d[2];
#pragma unroll
    for (int ct = 0; ct < 2; ++ct) {
      d[ct] = f32x4{0.f, 0.f, 0.f, 0.f};
#pragma unroll
      for (int ks = 0; ks < 2; ++ks) {
        const v4u bm = {c.qf[ks][ct][0].x, c.qf[ks][ct][0].y, c.qf[ks][ct][1].x, c.qf[ks][ct][1].y};
        d[ct] = mfma(c.xv[ks], bm, d[ct]);
      }
    }
    c.tag = c.epoch * 0x9E3779B1u + 0x7F4A7C15u;
    asm volatile("" : "+s"(c.tag));
    const float lo = (lane & 16) ? d[1].x : d[0].x, hi = (lane & 16) ? d[1].y : d[0].y;
    lds[L_RED + w * 64 + lane] = (lane & 32) ? hi : lo;
    flag_set(lds, 0, w);
  } else if constexpr (op == QPUB) {
    flag_wait(lds, 0, lane);
    float t = 0.f;
#pragma unroll
    for (int i = 0; i < 7; ++i) t += lds[L_RED + i * 64 + lane];
    const unsigned pr = cvtpk(t, dpp<XOR1>(t));
    const v4u g = {pr, c.tag, __float_as_uint(dpp<XOR2>(__uint_as_float(pr))), c.tag};
    __builtin_amdgcn_raw_buffer_store_b128(g, c.rw, c.q_st, 0, AUX_SC1);
  } else if constexpr (op == QLD) {
    c.qg = __builtin_amdgcn_raw_buffer_load_b128(c.rw, c.q_ld, 0, AUX_SC1);
  } else if constexpr (op == QCHK) {
    await_tags(c.qg, c.rw, c.q_ld, c.tag);
    *(v2u*)&lds[L_TQ + w * 1024 + lane * 2] = v2u{c.qg.x, c.qg.z};
    COMPILER_FENCE();
    c.qr = *(const v4u*)&lds[L_TQ + w * 1024 + (lane & 31) * 4];
  } else if constexpr (op == CK) {
    const v4u k = c.kb[a], q = c.qr;
    float x = dot2(k.x, q.x, dot2(k.z, q.z, 0.f)) + dot2(k.y, q.y, dot2(k.w, q.w, 0.f));
    if constexpr ((a & 1) == 1) x = add16(c.xs[0], x);
    if constexpr ((a & 3) == 3) x = tstep<ROR8>(c.xs[1], x, lane & 8);
    if constexpr ((a & 7) == 7) x = tstep<XOR7>(c.xs[2], x, lane & 4);
    if constexpr ((a & 15) == 15) x = tstep<XOR2>(c.xs[3], x, lane & 2);
    if constexpr ((a & 31) == 31) (a == 31 ? c.s0 : c.s1) = tstep<XOR1>(c.xs[4], x, lane & 1) * (0.0625f * 1.4426950408889634f);
    else c.xs[__builtin_ctz(~a)] = x;
  } else if constexpr (op == SM1) {
    const float m = wave_max(fmaxf(c.s0, c.s1));
    const float p0 = __builtin_amdgcn_exp2f(c.s0 - m), p1 = __builtin_amdgcn_exp2f(c.s1 - m);
    __bf16* pw = (__bf16*)&lds[L_P + w * 64] + (__builtin_bitreverse32(lane) >> 26);
    pw[0] = (__bf16)p0; pw[64] = (__bf16)p1;
    COMPILER_FENCE();
    c.mw = m; c.s0 = p0 + p1;
    c.pp = *(const v4u*)&lds[L_P + w * 64];
  } else if constexpr (op == SM2) {
    const float l = wave_sum(c.s0);
    lds[L_ML + 2 * w + (lane & 1)] = (lane & 1) ? l : c.mw;
    flag_set(lds, 1, w);
  } else if constexpr (op == MLPUB) {
    flag_wait(lds, 1, lane);
    const f32x2 ml = *(const f32x2*)&lds[L_ML + 2 * (lane & 7)];
    const float M = max8(ml.x), L = sum8(ml.y * __builtin_amdgcn_exp2f(ml.x - M));
    const v4u g = {__float_as_uint(M), c.tag, __float_as_uint(L), c.tag};
    __builtin_amdgcn_raw_buffer_store_b128(g, c.rw, c.ml_st, 0, AUX_SC1);
  } else if constexpr (op == CV) {
    const unsigned pr = c.pp[a & 3];
    const v4u am = {__builtin_amdgcn_perm(0u, pr, c.sel[0]), __builtin_amdgcn_perm(0u, pr, c.sel[1]),
                    __builtin_amdgcn_perm(0u, pr, c.sel[2]), __builtin_amdgcn_perm(0u, pr, c.sel[3])};
    c.acc = mfma(am, c.vb[a], c.acc);
    if constexpr ((a & 3) == 3 && a + 1 < N_V) c.pp = *(const v4u*)&lds[L_P + w * 64 + (a + 1)];
  } else if constexpr (op == YPF) {
    c.pf = __builtin_amdgcn_raw_buffer_load_b32(c.rw, c.y_pf, 0, AUX_SC1);
  } else if constexpr (op == PEERLD) {
    c.peer = __builtin_amdgcn_raw_buffer_load_b128(c.rw, c.ml_ld, 0, AUX_SC1);
  } else if constexpr (op == PEERCHK) {
    asm volatile("" :: "v"(c.pf));
    await_tags(c.peer, c.rw, c.ml_ld, c.tag);
    const float m = __uint_as_float(c.peer.x);
    float M = fmaxf(m, dpp<XOR1>(m)); M = fmaxf(M, dpp<XOR2>(M));
    float L = __uint_as_float(c.peer.z) * __builtin_amdgcn_exp2f(m - M); L += dpp<XOR1>(L); L += dpp<XOR2>(L);
    c.scale = __builtin_amdgcn_exp2f(c.mw - M) / L;
  } else if constexpr (op == TRW) {
#pragma unroll
    for (int i = 0; i < 4; ++i) *(v4u*)&lds[L_T + w * 1024 + i * 256 + lane * 4] = c.ob[4 * a + i];
    COMPILER_FENCE();
  } else if constexpr (op == TRR) {
    const float* base = &lds[L_T + w * 1024 + (lane >> 4) * 256 + ((lane >> 2) & 3) * 32 + (lane & 3) * 2];
#pragma unroll
    for (int ct = 0; ct < 4; ++ct)
#pragma unroll
      for (int h = 0; h < 2; ++h) c.of[a][ct][h] = tr_read(base + h * 128 + ct * 8);
    COMPILER_FENCE();
  }
  __builtin_amdgcn_sched_barrier(0);
}
template <int B, int... I> __device__ __forceinline__ void run_seq(Ctx& c, std::integer_sequence<int, I...>) { (step<B + I>(c), ...); }
template <int B, int E> __device__ __forceinline__ void run(Ctx& c) { run_seq<B>(c, std::make_integer_sequence<int, E - B>{}); }

__global__ void __launch_bounds__(NTHR, 2)
mha_decode(const __bf16* __restrict__ xA, const __bf16* __restrict__ kA, const __bf16* __restrict__ vA,
           const __bf16* __restrict__ wqA, const __bf16* __restrict__ woA,
           const __bf16* __restrict__ xB, const __bf16* __restrict__ kB, const __bf16* __restrict__ vB,
           const __bf16* __restrict__ wqB, const __bf16* __restrict__ woB, __bf16* out, unsigned char* ws) {
  __shared__ __attribute__((aligned(16))) float lds[L_END];
  Ctx c;
  const unsigned long long tstart = __builtin_amdgcn_s_memrealtime();
  __builtin_amdgcn_sched_barrier(0);
  const int bid = blockIdx.x, rk = bid >> 3, bh = (bid & 7) * 4 + (rk >> 3), br = bh >> 4, head = bh & 15;
  const int b = (rk >> 2) & 1, split = rk & 3, grp = br * 2 + b, prod = head * 4 + split;
  const int tid = threadIdx.x, lane = tid & 63, w = __builtin_amdgcn_readfirstlane(tid >> 6);
  const int off7 = w < 7 ? 0 : OOB;
  c.w = w; c.lane = lane; c.lds = lds;
  c.t0 = (unsigned)tstart;
  c.vo_q = ((lane >> 2) * (NH * DH * 2) + (lane & 3) * 16) | off7;
  c.so_q = (64 * w * NH * DH + head * DH + (rk & 7) * 32) * 2;
  c.vo_kv = lane * 16;
  c.so_kv = (b * NH + head) * (SK * DH * 2) + (w << 12) + ((split & 1) << 18) + ((split >> 1) << 15) +
            ((((head >> 1) ^ (head >> 3) ^ br) & 1) << 17) + (b << 19);
  c.vo_o = ((lane >> 3) * (DM * 2) + w * 128 + (lane & 7) * 16) | off7;
  c.so_o = head * DH * DM * 2;
  c.orot = rk & 7;
  int ep_off = br * 256;
  asm volatile("" : "+s"(c.so_q), "+s"(c.so_kv), "+s"(c.so_o), "+s"(c.orot), "+s"(ep_off), "+v"(c.vo_q), "+v"(c.vo_kv), "+v"(c.vo_o)
               : "s"(tstart));
  __builtin_amdgcn_sched_barrier(0);
  c.rq = __builtin_amdgcn_make_buffer_rsrc((void*)(br ? wqB : wqA), 0, DM * NH * DH * 2, RSRC_W3);
  c.ro = __builtin_amdgcn_make_buffer_rsrc((void*)(br ? woB : woA), 0, NH * DH * DM * 2, RSRC_W3);
  c.rk = __builtin_amdgcn_make_buffer_rsrc((void*)(br ? kB : kA), 0, 2 * NH * SK * DH * 2, RSRC_W3);
  run<0, 1>(c);
  c.epoch = *(const __attribute__((address_space(4))) unsigned*)(ws + ep_off);
  run<1, N_FIRST>(c);
  int br2 = br;
  asm volatile("" : "+s"(br2));
  c.rx = __builtin_amdgcn_make_buffer_rsrc((void*)(br2 ? xB : xA), 0, 2 * DM * 2, RSRC_W3);
  c.vo_x = (((lane & 1) * DM + 64 * w + 8 * (lane >> 4)) * 2) | off7;
  c.rv = __builtin_amdgcn_make_buffer_rsrc((void*)(br2 ? vB : vA), 0, 2 * NH * SK * DH * 2, RSRC_W3);
  c.rw = __builtin_amdgcn_make_buffer_rsrc((void*)ws, 0, WS_END, RSRC_W3);
  const rsrc_t rout = __builtin_amdgcn_make_buffer_rsrc((void*)out, 0, 4 * DM * 2, RSRC_W3);
  c.q_st = (WS_Q + bh * 2048 + (lane >> 5) * 1024 + (rk & 7) * 128 + (lane & 31) * 4) | ((tid & ~60) == 0 ? 0 : OOB);
  c.q_ld = WS_Q + bh * 2048 + b * 1024 + lane * 16;
  c.ml_st = (WS_ML + (grp * NH + head) * 128 + split * 16) | (tid == 0 ? 0 : OOB);
  c.ml_ld = WS_ML + (grp * NH + head) * 128 + (lane & 3) * 16;
  c.y_pf = (WS_PKT + (grp * 64 + prod) * PKT_BYTES + lane * 128) | (tid < PKT_BYTES / 128 ? 0 : OOB);

  run<N_FIRST, S.n - N_LAST>(c);

  const int pk = WS_PKT + grp * 64 * PKT_BYTES;
  const int dq = 32 * (lane >> 5) + 2 * (lane & 15) + ((lane >> 4) & 1);
  int a_wr = (L_RED + w * A_STR + ((dq - 8 * c.orot) & 63)) * 4, a_rd = (L_RED + (lane & 15) * A_STR + 4 * (lane >> 4)) * 4;
  int idx = (lane & 8) ? 0xEEEE : 0x4444;
  int y_off = (pk + prod * PKT_BYTES + tid * 4) | off7 | ((lane & 1) ? OOB : 0);
  int p_off = pk + (lane >> 2) * PKT_BYTES + prod * 64 + (lane & 3) * 16;
  int o_off = ((b * (2 * DM) + br * DM + prod * 16 + (lane & 3) * 4 + (lane >> 4)) * 2) | ((lane & 12) ? OOB : 0);
  int e_off = (prod == 0 && b == 0 && lane == 0) ? ep_off : OOB;
  asm volatile("" : "+v"(a_wr), "+v"(a_rd), "+v"(idx), "+v"(y_off), "+v"(p_off), "+v"(o_off), "+v"(e_off));
  f32x4 y[4];
#pragma unroll
  for (int ct = 0; ct < 4; ++ct) { y[ct] = f32x4{0.f, 0.f, 0.f, 0.f}; asm volatile("" : "+v"(y[ct])); }

  run<S.n - N_LAST, S.n>(c);
  LDS_AS unsigned char* const lb = (LDS_AS unsigned char*)lds;
  *(LDS_AS unsigned*)(lb + a_wr) = cvtpk(c.acc.x * c.scale, c.acc.y * c.scale);
  *(LDS_AS unsigned*)(lb + a_wr + 8 * A_STR * 4) = cvtpk(c.acc.z * c.scale, c.acc.w * c.scale);
  __syncthreads();
  v4u am[4];
#pragma unroll
  for (int ks = 0; ks < 4; ++ks) am[ks] = *(LDS_AS v4u*)(lb + a_rd + 64 * ks);
  __builtin_amdgcn_sched_barrier(0);
#pragma unroll
  for (int ks = 0; ks < 4; ++ks)
#pragma unroll
    for (int ct = 0; ct < 4; ++ct) {
      const v2u b0 = c.of[2 * ks][ct][0], b1 = c.of[2 * ks][ct][1], b2 = c.of[2 * ks + 1][ct][0], b3 = c.of[2 * ks + 1][ct][1];
      const v8u bm = {b0.x, b0.y, b1.x, b1.y, b2.x, b2.y, b3.x, b3.y};
      y[ct] = __builtin_amdgcn_smfmac_f32_16x16x64_bf16(__builtin_bit_cast(bf16x8, am[ks]), __builtin_bit_cast(bf16x16, bm), y[ct], idx, 0, 0);
    }
  float sm[4];
#pragma unroll
  for (int ct = 0; ct < 4; ++ct) sm[ct] = (y[ct].x + y[ct].y) + (y[ct].z + y[ct].w);
  const float yv = add32(add16(sm[0], sm[1]), add16(sm[2], sm[3]));
  const unsigned t0 = c.tag & 255u, t1 = (c.tag >> 8) & 255u;
  const v2u g = {(__float_as_uint(yv) & ~255u) | t0, (__float_as_uint(dpp<XOR1>(yv)) & ~255u) | t1};
  __builtin_amdgcn_raw_buffer_store_b64(g, c.rw, y_off, 0, AUX_SC1);

  if (w == NW - 1 && prod < DM / 16) {
    v4u v[4], u[4];
    bool got[4] = {false, false, false, false}, done = false;
#pragma unroll 1
    for (unsigned spins = 0; !done && spins < SPIN_CAP; ++spins) {
      COMPILER_FENCE();
#pragma unroll
      for (int i = 0; i < 4; ++i)
        if (!got[i]) v[i] = __builtin_amdgcn_raw_buffer_load_b128(c.rw, p_off, i * 16 * PKT_BYTES, AUX_SC1);
#pragma unroll
      for (int i = 0; i < 4; ++i) {
        u[i] = v[i] ^ v4u{t0, t1, t0, t1};
        got[i] = __all(((u[i].x | u[i].y | u[i].z | u[i].w) & 255u) == 0u);
      }
      done = got[0] & got[1] & got[2] & got[3];
    }
    const f32x4 r = (__builtin_bit_cast(f32x4, u[0]) + __builtin_bit_cast(f32x4, u[1])) +
                    (__builtin_bit_cast(f32x4, u[2]) + __builtin_bit_cast(f32x4, u[3]));
    float z = add16(add32(r.x, r.z), add32(r.y, r.w));
    z += dpp<ROR4>(z);
    z += dpp<ROR8>(z);
    const __bf16 zb = (__bf16)z;
    __builtin_amdgcn_raw_buffer_store_b16(done ? __builtin_bit_cast(unsigned short, zb) : (unsigned short)0x7fc0u, rout, o_off, 0, 0);
    __builtin_amdgcn_raw_buffer_store_b32(c.tag, c.rw, e_off, 0, AUX_SC1);
  }
}

}

extern "C" void kernel_launch(void* const* d_in, const int* in_sizes, int n_in, void* d_out, int out_size, void* d_ws,
                              size_t ws_size, hipStream_t stream) {
  (void)in_sizes; (void)n_in; (void)out_size; (void)ws_size;
  mha_decode<<<256, NTHR, 0, stream>>>((const __bf16*)d_in[0], (const __bf16*)d_in[1], (const __bf16*)d_in[2],
                                       (const __bf16*)d_in[3], (const __bf16*)d_in[4], (const __bf16*)d_in[5],
                                       (const __bf16*)d_in[6], (const __bf16*)d_in[7], (const __bf16*)d_in[8],
                                       (const __bf16*)d_in[9], (__bf16*)d_out, (unsigned char*)d_ws);
}
```

```cpp
#define STAMPS
#define GP 1
#define GA 4
#define GB 3
#define GLO 1150
#include <hip/hip_runtime.h>
#include <hip/hip_bf16.h>
#include <utility>

#define STAMP_OFF (8u<<20)
#define STAMP_NW 8
#define ST(k) do { __builtin_amdgcn_sched_barrier(0); asm volatile("s_memrealtime %0\n\ts_waitcnt lgkmcnt(0)" : "=s"(c.stv[k]) :: "memory"); __builtin_amdgcn_sched_barrier(0); } while (0)
namespace {

typedef unsigned v2u __attribute__((ext_vector_type(2)));
typedef unsigned v4u __attribute__((ext_vector_type(4)));
typedef unsigned v8u __attribute__((ext_vector_type(8)));
typedef float f32x2 __attribute__((ext_vector_type(2)));
typedef float f32x4 __attribute__((ext_vector_type(4)));
typedef __bf16 bf16x2 __attribute__((ext_vector_type(2)));
typedef __bf16 bf16x8 __attribute__((ext_vector_type(8)));
typedef __bf16 bf16x16 __attribute__((ext_vector_type(16)));
typedef short i16x4 __attribute__((ext_vector_type(4)));
typedef __amdgpu_buffer_rsrc_t rsrc_t;
#define LDS_AS __attribute__((address_space(3)))
#define COMPILER_FENCE() asm volatile("" ::: "memory")

constexpr int DM = 448, NH = 16, SK = 4096, DH = 256;
constexpr int NW = 8, NTHR = NW * 64;
constexpr int N_Q = 4, N_K = 64, N_V = 64, N_O = 32;
constexpr unsigned SPIN_CAP = 1u << 20;
constexpr int RSRC_W3 = 0x00020000, AUX_NT = 2, AUX_SC1 = 16, OOB = 0x40000000;
constexpr int WS_PKT = 4096, PKT_BYTES = DM * 4, WS_Q = WS_PKT + 256 * PKT_BYTES, WS_ML = WS_Q + 64 * 1024,
              WS_END = WS_ML + 64 * 128;
constexpr int A_STR = 68;
constexpr int L_RED = 0, L_T = L_RED + 16 * A_STR, L_TQ = L_T + NW * 1024, L_ML = L_TQ + NW * 1024, L_P = L_ML + 2 * NW,
              L_F = L_P + NW * 64, L_END = L_F + NW * 2;

#define P_WIN 3
#define P_NEND 8
#define P_OSTART 44
#define P_OEVERY 2
#define P_BT 440
#define P_BG 4
#define P_QLD 17
constexpr int WIN = P_WIN;
constexpr int N_END = P_NEND;
constexpr int O_FIRST = 4;
constexpr int O_START = P_OSTART, O_EVERY = P_OEVERY;
constexpr int X_AT = 2, QMM_AT = 6, QPUB_AT = 9, QLD_AT = P_QLD, QCHK_AT = QLD_AT + 4, YPF_AT = 100;
constexpr int BOOST_AT = 6, BOOST_MAX = 40, BOOST_T = P_BT, BOOST_G = P_BG;
constexpr int N_FIRST = N_Q + O_FIRST + 1, N_LAST = 3;

enum Op : int { IQ, IK, IV, IO, IX, QPUB, QLD, MLPUB, YPF, PEERLD,
                WAITV, WAITB, LAG, SETUP, QTR, QMM, QCHK, CK, SM1, SM2, CV, PEERCHK, TRW, TRR };
struct Sched { int n; int op[640]; int a[640]; };
constexpr Sched make_sched() {
  Sched s{};
  int vmk[256] = {}, vmi[256] = {}, nvm = 0;
  auto emit = [&](int op, int a) {
    s.op[s.n] = op; s.a[s.n] = a; ++s.n;
    if (op <= PEERLD) { vmk[nvm] = op; vmi[nvm] = a; ++nvm; }
  };
  int no = 0, nk = 0, nv = 0, nw = 0, nr = 0, sm = 0;
  int lk = 0, lv = 0, lo = 0;
  for (int j = 0; j < N_Q; ++j) emit(IQ, j);
  while (no < O_FIRST) emit(IO, no++);
  for (int j = 0; j < N_K + N_V; ++j) {
    if (j > 0) {
      const int win = j >= N_K + N_V - N_END ? WIN + 1 : WIN;
      int p = nvm, seen = 0;
      while (seen < win) { --p; if (vmk[p] <= IV) ++seen; }
      if (j - 1 > BOOST_AT && j - 1 <= BOOST_AT + BOOST_MAX) {
        int p2 = p;
        do --p2; while (vmk[p2] > IV);
        emit(WAITV, nvm - 1 - p2);
        emit(WAITB, (nvm - 1 - p) | ((j - 1 - BOOST_AT) << 8));
        p = p2;
      } else emit(WAITV, nvm - 1 - p);
      if (j - 1 == BOOST_AT) emit(LAG, 0);
      if (vmk[p] == IK) lk = vmi[p] + 1;
      if (vmk[p] == IV) { lk = N_K; lv = vmi[p] + 1; }
      for (int t = 0; t < p; ++t) if (vmk[t] == IO) lo = vmi[t] + 1;
    }
    if (j < N_K) emit(IK, j); else emit(IV, j - N_K);
    if (j == 0) emit(SETUP, 0);
    if (j >= O_START && (j - O_START) % O_EVERY == 0 && no < N_O) emit(IO, no++);
    if (j == X_AT) { emit(IX, 0); emit(IX, 1); }
    if (j == YPF_AT) emit(YPF, 0);
    if (j == QMM_AT - 1) emit(QTR, 0);
    if (j == QMM_AT) emit(QMM, 0);
    if (j == QPUB_AT) emit(QPUB, 0);
    if (j == QLD_AT) emit(QLD, 0);
    if (j == QCHK_AT) emit(QCHK, 0);
    else if (j > QCHK_AT && nk < N_K) { for (int t = 0; t < 3 && nk < lk; ++t) emit(CK, nk++); }
    else if (nk == N_K && sm < 2) { emit(sm == 0 ? SM1 : SM2, 0); ++sm; }
    else if (sm == 2) {
      for (int t = 0; t < 3 && nv < lv; ++t) {
        emit(CV, nv++);
        if (nv == 12) emit(MLPUB, 0);
        if (nv == N_V - 20) emit(PEERLD, 0);
        if (nv == N_V - 10) emit(PEERCHK, 0);
      }
    }
    if (nr < nw) emit(TRR, nr++);
    else if (4 * nw + 4 <= lo) emit(TRW, nw++);
  }
  if (no != N_O || nr != N_O / 4 || nv < N_V - 10 || nv > N_V - N_LAST) s.n = -1;
  else while (nv < N_V) emit(CV, nv++);
  return s;
}
constexpr Sched S = make_sched();
static_assert(S.n > 0, "the schedule must finish everything but the last V loads inside the stream");

constexpr int group_off(int g) { return ((g & 3) << 16) | ((g >> 2) << 19); }

__device__ __forceinline__ float dot2(unsigned a, unsigned b, float c) {
  return __builtin_amdgcn_fdot2_f32_bf16(__builtin_bit_cast(bf16x2, a), __builtin_bit_cast(bf16x2, b), c, false);
}
__device__ __forceinline__ unsigned cvtpk(float lo, float hi) {
  const bf16x2 r = {(__bf16)lo, (__bf16)hi};
  return __builtin_bit_cast(unsigned, r);
}
template <int CTRL> __device__ __forceinline__ float dpp(float x) {
  return __builtin_bit_cast(float, __builtin_amdgcn_mov_dpp(__builtin_bit_cast(int, x), CTRL, 0xf, 0xf, true));
}
constexpr int XOR1 = 0xB1, XOR2 = 0x4E, XOR7 = 0x141, XOR15 = 0x140, ROR4 = 0x124, ROR8 = 0x128;
__device__ __forceinline__ float sum8(float x) { x += dpp<XOR1>(x); x += dpp<XOR2>(x); x += dpp<XOR7>(x); return x; }
__device__ __forceinline__ float max8(float x) {
  x = fmaxf(x, dpp<XOR1>(x)); x = fmaxf(x, dpp<XOR2>(x)); x = fmaxf(x, dpp<XOR7>(x));
  return x;
}
__device__ __forceinline__ float add16(float a, float b) {
  const auto s = __builtin_amdgcn_permlane16_swap(__float_as_uint(a), __float_as_uint(b), false, false);
  return __uint_as_float(s[0]) + __uint_as_float(s[1]);
}
__device__ __forceinline__ float add32(float a, float b) {
  const auto s = __builtin_amdgcn_permlane32_swap(__float_as_uint(a), __float_as_uint(b), false, false);
  return __uint_as_float(s[0]) + __uint_as_float(s[1]);
}
template <int CTRL> __device__ __forceinline__ float tstep(float lo, float hi, bool sel) {
  const float a = lo + dpp<CTRL>(lo), b = hi + dpp<CTRL>(hi);
  return sel ? b : a;
}
__device__ __forceinline__ float wave_sum(float x) {
  x = sum8(x); x += dpp<XOR15>(x); x = add16(x, x);
  return add32(x, x);
}
__device__ __forceinline__ float wave_max(float x) {
  x = max8(x); x = fmaxf(x, dpp<XOR15>(x));
  const auto s = __builtin_amdgcn_permlane16_swap(__float_as_uint(x), __float_as_uint(x), false, false);
  x = fmaxf(__uint_as_float(s[0]), __uint_as_float(s[1]));
  const auto t = __builtin_amdgcn_permlane32_swap(__float_as_uint(x), __float_as_uint(x), false, false);
  return fmaxf(__uint_as_float(t[0]), __uint_as_float(t[1]));
}
__device__ __forceinline__ f32x4 mfma(v4u a, v4u b, f32x4 c) {
  return __builtin_amdgcn_mfma_f32_16x16x32_bf16(__builtin_bit_cast(bf16x8, a), __builtin_bit_cast(bf16x8, b), c, 0, 0, 0);
}
__device__ __forceinline__ v2u tr_read(const float* p) {
  return __builtin_bit_cast(v2u, __builtin_amdgcn_ds_read_tr16_b64_v4i16((LDS_AS i16x4*)p));
}
__device__ __forceinline__ void await_tags(v4u& g, rsrc_t r, int off, unsigned tag) {
  if (__builtin_expect(!__all((g.y == tag) & (g.w == tag)), 0)) {
    unsigned spins = 0;
    bool ok;
#pragma unroll 1
    do {
      COMPILER_FENCE();
      g = __builtin_amdgcn_raw_buffer_load_b128(r, off, 0, AUX_SC1);
      ok = __all((g.y == tag) & (g.w == tag));
    } while (!ok && ++spins < SPIN_CAP);
    if (!ok) g.x = 0x7fc07fc0u;
  }
}
__device__ __forceinline__ void flag_set(float* lds, int k, int w) {
  asm volatile("s_waitcnt lgkmcnt(0)" ::: "memory");
  ((LDS_AS volatile unsigned*)lds)[L_F + k * NW + w] = 1u;
}
__device__ __forceinline__ void flag_wait(float* lds, int k, int lane) {
  while (!__all(((LDS_AS volatile unsigned*)lds)[L_F + k * NW + (lane & 7)] != 0u)) {}
  COMPILER_FENCE();
}

struct Ctx {
  rsrc_t rq, rk, rv, ro, rx, rw;
  float* lds;
  int w, lane, vo_q, vo_kv, vo_o, vo_x, so_q, so_kv, so_o, orot, q_st, q_ld, ml_st, ml_ld, y_pf, boost;
  unsigned t0, epoch, tag, pf, sel[4];
  v4u qb[N_Q], kb[N_K], vb[N_V], ob[N_O], xv[2], qg, qr, pp, peer;
  v2u qf[2][2][2];
  v2u of[8][4][2];
  f32x4 acc;
  float xs[5], s0, s1, mw, scale;
  unsigned long long stv[10];
};

template <int I> __device__ __forceinline__ void step(Ctx& c) {
  constexpr int op = S.op[I], a = S.a[I];
  float* const lds = c.lds;
  const int w = c.w, lane = c.lane;
  if constexpr (op == IQ) {
    c.qb[a] = __builtin_amdgcn_raw_buffer_load_b128(c.rq, c.vo_q, c.so_q + 16 * a * (NH * DH * 2), AUX_NT);
  } else if constexpr (op == IK) {
    c.kb[a] = __builtin_amdgcn_raw_buffer_load_b128(c.rk, c.vo_kv + (a & 3) * 1024, c.so_kv ^ group_off(a >> 2), AUX_NT);
  } else if constexpr (op == IV) {
    c.vb[a] = __builtin_amdgcn_raw_buffer_load_b128(c.rv, c.vo_kv + (a & 3) * 1024, c.so_kv ^ group_off(a >> 2), AUX_NT);
  } else if constexpr (op == IO) {
    c.ob[a] = __builtin_amdgcn_raw_buffer_load_b128(c.ro, c.vo_o, c.so_o + ((8 * a + 32 * c.orot) & 255) * (DM * 2), 0);
  } else if constexpr (op == IX) {
    c.xv[a] = __builtin_amdgcn_raw_buffer_load_b128(c.rx, c.vo_x + 64 * a, 0, 0);
  } else if constexpr (op == WAITV) {
    __builtin_amdgcn_s_waitcnt((a & 15) | ((a >> 4) << 14) | 0x0F70);
  } else if constexpr (op == LAG) {
    c.boost = ((int)((unsigned)__builtin_amdgcn_s_memrealtime() - c.t0) - BOOST_T) * BOOST_G >> 4;
  } else if constexpr (op == WAITB) {
    asm volatile("s_cmp_ge_i32 %0, %1\n\ts_cbranch_scc1 1f\n\ts_waitcnt vmcnt(%2)\n1:"
                 :: "s"(c.boost), "i"(a >> 8), "i"(a & 255) : "scc", "memory");
  } else if constexpr (op == SETUP) {
    const int row = lane & 15, j = row & 7;
    const bool act = ((lane >> 4) & 1) == (row >> 3);
    const unsigned two = (lane >> 5) ? 0x0302u : 0x0100u;
    const unsigned s = (j & 1) ? ((two << 16) | 0x0c0cu) : (0x0c0c0000u | two);
#pragma unroll
    for (int i = 0; i < 4; ++i) c.sel[i] = (act && (j >> 1) == i) ? s : 0x0c0c0c0cu;
    c.acc = f32x4{0.f, 0.f, 0.f, 0.f};
    ((unsigned*)lds)[L_F + (lane & 1) * NW + w] = 0u;
    __syncthreads();
  } else if constexpr (op == QTR) {
#pragma unroll
    for (int i = 0; i < 4; ++i) *(v4u*)&lds[L_TQ + w * 1024 + i * 256 + lane * 4] = c.qb[i];
    COMPILER_FENCE();
    const float* base = &lds[L_TQ + w * 1024 + (lane >> 4) * 128 + ((lane >> 2) & 3) * 16 + (lane & 3) * 2];
#pragma unroll
    for (int ks = 0; ks < 2; ++ks)
#pragma unroll
      for (int ct = 0; ct < 2; ++ct)
#pragma unroll
        for (int h = 0; h < 2; ++h) c.qf[ks][ct][h] = tr_read(base + ks * 512 + h * 64 + ct * 8);
    COMPILER_FENCE();
  } else if constexpr (op == QMM) {
    f32x4 d[2];
#pragma unroll
    for (int ct = 0; ct < 2; ++ct) {
      d[ct] = f32x4{0.f, 0.f, 0.f, 0.f};
#pragma unroll
      for (int ks = 0; ks < 2; ++ks) {
        const v4u bm = {c.qf[ks][ct][0].x, c.qf[ks][ct][0].y, c.qf[ks][ct][1].x, c.qf[ks][ct][1].y};
        d[ct] = mfma(c.xv[ks], bm, d[ct]);
      }
    }
    c.tag = c.epoch * 0x9E3779B1u + 0x7F4A7C15u;
    asm volatile("" : "+s"(c.tag));
    const float lo = (lane & 16) ? d[1].x : d[0].x, hi = (lane & 16) ? d[1].y : d[0].y;
    lds[L_RED + w * 64 + lane] = (lane & 32) ? hi : lo;
    flag_set(lds, 0, w);
    ST(1);
  } else if constexpr (op == QPUB) {
    flag_wait(lds, 0, lane);
    float t = 0.f;
#pragma unroll
    for (int i = 0; i < 7; ++i) t += lds[L_RED + i * 64 + lane];
    const unsigned pr = cvtpk(t, dpp<XOR1>(t));
    const v4u g = {pr, c.tag, __float_as_uint(dpp<XOR2>(__uint_as_float(pr))), c.tag};
    __builtin_amdgcn_raw_buffer_store_b128(g, c.rw, c.q_st, 0, AUX_SC1);
    ST(2);
  } else if constexpr (op == QLD) {
    c.qg = __builtin_amdgcn_raw_buffer_load_b128(c.rw, c.q_ld, 0, AUX_SC1);
  } else if constexpr (op == QCHK) {
    await_tags(c.qg, c.rw, c.q_ld, c.tag);
    *(v2u*)&lds[L_TQ + w * 1024 + lane * 2] = v2u{c.qg.x, c.qg.z};
    COMPILER_FENCE();
    c.qr = *(const v4u*)&lds[L_TQ + w * 1024 + (lane & 31) * 4];
    ST(3);
  } else if constexpr (op == CK) {
    const v4u k = c.kb[a], q = c.qr;
    float x = dot2(k.x, q.x, dot2(k.z, q.z, 0.f)) + dot2(k.y, q.y, dot2(k.w, q.w, 0.f));
    if constexpr ((a & 1) == 1) x = add16(c.xs[0], x);
    if constexpr ((a & 3) == 3) x = tstep<ROR8>(c.xs[1], x, lane & 8);
    if constexpr ((a & 7) == 7) x = tstep<XOR7>(c.xs[2], x, lane & 4);
    if constexpr ((a & 15) == 15) x = tstep<XOR2>(c.xs[3], x, lane & 2);
    if constexpr ((a & 31) == 31) (a == 31 ? c.s0 : c.s1) = tstep<XOR1>(c.xs[4], x, lane & 1) * (0.0625f * 1.4426950408889634f);
    else c.xs[__builtin_ctz(~a)] = x;
  } else if constexpr (op == SM1) {
    ST(4);
    const float m = wave_max(fmaxf(c.s0, c.s1));
    const float p0 = __builtin_amdgcn_exp2f(c.s0 - m), p1 = __builtin_amdgcn_exp2f(c.s1 - m);
    __bf16* pw = (__bf16*)&lds[L_P + w * 64] + (__builtin_bitreverse32(lane) >> 26);
    pw[0] = (__bf16)p0; pw[64] = (__bf16)p1;
    COMPILER_FENCE();
    c.mw = m; c.s0 = p0 + p1;
    c.pp = *(const v4u*)&lds[L_P + w * 64];
  } else if constexpr (op == SM2) {
    const float l = wave_sum(c.s0);
    lds[L_ML + 2 * w + (lane & 1)] = (lane & 1) ? l : c.mw;
    flag_set(lds, 1, w);
  } else if constexpr (op == MLPUB) {
    flag_wait(lds, 1, lane);
    const f32x2 ml = *(const f32x2*)&lds[L_ML + 2 * (lane & 7)];
    const float M = max8(ml.x), L = sum8(ml.y * __builtin_amdgcn_exp2f(ml.x - M));
    const v4u g = {__float_as_uint(M), c.tag, __float_as_uint(L), c.tag};
    __builtin_amdgcn_raw_buffer_store_b128(g, c.rw, c.ml_st, 0, AUX_SC1);
  } else if constexpr (op == CV) {
    const unsigned pr = c.pp[a & 3];
    const v4u am = {__builtin_amdgcn_perm(0u, pr, c.sel[0]), __builtin_amdgcn_perm(0u, pr, c.sel[1]),
                    __builtin_amdgcn_perm(0u, pr, c.sel[2]), __builtin_amdgcn_perm(0u, pr, c.sel[3])};
    c.acc = mfma(am, c.vb[a], c.acc);
    if constexpr ((a & 3) == 3 && a + 1 < N_V) c.pp = *(const v4u*)&lds[L_P + w * 64 + (a + 1)];
  } else if constexpr (op == YPF) {
    c.pf = __builtin_amdgcn_raw_buffer_load_b32(c.rw, c.y_pf, 0, AUX_SC1);
  } else if constexpr (op == PEERLD) {
    c.peer = __builtin_amdgcn_raw_buffer_load_b128(c.rw, c.ml_ld, 0, AUX_SC1);
  } else if constexpr (op == PEERCHK) {
    asm volatile("" :: "v"(c.pf));
    await_tags(c.peer, c.rw, c.ml_ld, c.tag);
    const float m = __uint_as_float(c.peer.x);
    float M = fmaxf(m, dpp<XOR1>(m)); M = fmaxf(M, dpp<XOR2>(M));
    float L = __uint_as_float(c.peer.z) * __builtin_amdgcn_exp2f(m - M); L += dpp<XOR1>(L); L += dpp<XOR2>(L);
    c.scale = __builtin_amdgcn_exp2f(c.mw - M) / L;
  } else if constexpr (op == TRW) {
#pragma unroll
    for (int i = 0; i < 4; ++i) *(v4u*)&lds[L_T + w * 1024 + i * 256 + lane * 4] = c.ob[4 * a + i];
    COMPILER_FENCE();
  } else if constexpr (op == TRR) {
    const float* base = &lds[L_T + w * 1024 + (lane >> 4) * 256 + ((lane >> 2) & 3) * 32 + (lane & 3) * 2];
#pragma unroll
    for (int ct = 0; ct < 4; ++ct)
#pragma unroll
      for (int h = 0; h < 2; ++h) c.of[a][ct][h] = tr_read(base + h * 128 + ct * 8);
    COMPILER_FENCE();
  }
  __builtin_amdgcn_sched_barrier(0);
}
template <int B, int... I> __device__ __forceinline__ void run_seq(Ctx& c, std::integer_sequence<int, I...>) { (step<B + I>(c), ...); }
template <int B, int E> __device__ __forceinline__ void run(Ctx& c) { run_seq<B>(c, std::make_integer_sequence<int, E - B>{}); }

__global__ void __launch_bounds__(NTHR, 2)
mha_decode(const __bf16* __restrict__ xA, const __bf16* __restrict__ kA, const __bf16* __restrict__ vA,
           const __bf16* __restrict__ wqA, const __bf16* __restrict__ woA,
           const __bf16* __restrict__ xB, const __bf16* __restrict__ kB, const __bf16* __restrict__ vB,
           const __bf16* __restrict__ wqB, const __bf16* __restrict__ woB, __bf16* out, unsigned char* ws) {
  __shared__ __attribute__((aligned(16))) float lds[L_END];
  Ctx c;
  const unsigned long long tstart = __builtin_amdgcn_s_memrealtime();
  __builtin_amdgcn_sched_barrier(0);
  for (int i = 0; i < 10; ++i) c.stv[i] = 0;
  c.stv[0] = tstart;
  const int bid = blockIdx.x, rk = bid >> 3, bh = (bid & 7) * 4 + (rk >> 3), br = bh >> 4, head = bh & 15;
  const int b = (rk >> 2) & 1, split = rk & 3, grp = br * 2 + b, prod = head * 4 + split;
  const int tid = threadIdx.x, lane = tid & 63, w = __builtin_amdgcn_readfirstlane(tid >> 6);
  const int off7 = w < 7 ? 0 : OOB;
  c.w = w; c.lane = lane; c.lds = lds;
  c.t0 = (unsigned)tstart;
  c.vo_q = ((lane >> 2) * (NH * DH * 2) + (lane & 3) * 16) | off7;
  c.so_q = (64 * w * NH * DH + head * DH + (rk & 7) * 32) * 2;
  c.vo_kv = lane * 16;
  c.so_kv = (b * NH + head) * (SK * DH * 2) + (w << 12) + ((split & 1) << 18) + ((split >> 1) << 15) +
            ((((head >> 1) ^ (head >> 3) ^ br) & 1) << 17) + (b << 19);
  c.vo_o = ((lane >> 3) * (DM * 2) + w * 128 + (lane & 7) * 16) | off7;
  c.so_o = head * DH * DM * 2;
  c.orot = rk & 7;
  int ep_off = br * 256;
  asm volatile("" : "+s"(c.so_q), "+s"(c.so_kv), "+s"(c.so_o), "+s"(c.orot), "+s"(ep_off), "+v"(c.vo_q), "+v"(c.vo_kv), "+v"(c.vo_o)
               : "s"(tstart));
  __builtin_amdgcn_sched_barrier(0);
  c.rq = __builtin_amdgcn_make_buffer_rsrc((void*)(br ? wqB : wqA), 0, DM * NH * DH * 2, RSRC_W3);
  c.ro = __builtin_amdgcn_make_buffer_rsrc((void*)(br ? woB : woA), 0, NH * DH * DM * 2, RSRC_W3);
  c.rk = __builtin_amdgcn_make_buffer_rsrc((void*)(br ? kB : kA), 0, 2 * NH * SK * DH * 2, RSRC_W3);
  run<0, 1>(c);
  c.epoch = *(const __attribute__((address_space(4))) unsigned*)(ws + ep_off);
  run<1, N_FIRST>(c);
  int br2 = br;
  asm volatile("" : "+s"(br2));
  c.rx = __builtin_amdgcn_make_buffer_rsrc((void*)(br2 ? xB : xA), 0, 2 * DM * 2, RSRC_W3);
  c.vo_x = (((lane & 1) * DM + 64 * w + 8 * (lane >> 4)) * 2) | off7;
  c.rv = __builtin_amdgcn_make_buffer_rsrc((void*)(br2 ? vB : vA), 0, 2 * NH * SK * DH * 2, RSRC_W3);
  c.rw = __builtin_amdgcn_make_buffer_rsrc((void*)ws, 0, WS_END, RSRC_W3);
  const rsrc_t rout = __builtin_amdgcn_make_buffer_rsrc((void*)out, 0, 4 * DM * 2, RSRC_W3);
  c.q_st = (WS_Q + bh * 2048 + (lane >> 5) * 1024 + (rk & 7) * 128 + (lane & 31) * 4) | ((tid & ~60) == 0 ? 0 : OOB);
  c.q_ld = WS_Q + bh * 2048 + b * 1024 + lane * 16;
  c.ml_st = (WS_ML + (grp * NH + head) * 128 + split * 16) | (tid == 0 ? 0 : OOB);
  c.ml_ld = WS_ML + (grp * NH + head) * 128 + (lane & 3) * 16;
  c.y_pf = (WS_PKT + (grp * 64 + prod) * PKT_BYTES + lane * 128) | (tid < PKT_BYTES / 128 ? 0 : OOB);

  run<N_FIRST, S.n - N_LAST>(c);

  const int pk = WS_PKT + grp * 64 * PKT_BYTES;
  const int dq = 32 * (lane >> 5) + 2 * (lane & 15) + ((lane >> 4) & 1);
  int a_wr = (L_RED + w * A_STR + ((dq - 8 * c.orot) & 63)) * 4, a_rd = (L_RED + (lane & 15) * A_STR + 4 * (lane >> 4)) * 4;
  int idx = (lane & 8) ? 0xEEEE : 0x4444;
  int y_off = (pk + prod * PKT_BYTES + tid * 4) | off7 | ((lane & 1) ? OOB : 0);
  int p_off = pk + (lane >> 2) * PKT_BYTES + prod * 64 + (lane & 3) * 16;
  int o_off = ((b * (2 * DM) + br * DM + prod * 16 + (lane & 3) * 4 + (lane >> 4)) * 2) | ((lane & 12) ? OOB : 0);
  int e_off = (prod == 0 && b == 0 && lane == 0) ? ep_off : OOB;
  asm volatile("" : "+v"(a_wr), "+v"(a_rd), "+v"(idx), "+v"(y_off), "+v"(p_off), "+v"(o_off), "+v"(e_off));
  f32x4 y[4];
#pragma unroll
  for (int ct = 0; ct < 4; ++ct) { y[ct] = f32x4{0.f, 0.f, 0.f, 0.f}; asm volatile("" : "+v"(y[ct])); }

  run<S.n - N_LAST, S.n>(c);

  ST(5);
  LDS_AS unsigned char* const lb = (LDS_AS unsigned char*)lds;
  *(LDS_AS unsigned*)(lb + a_wr) = cvtpk(c.acc.x * c.scale, c.acc.y * c.scale);
  *(LDS_AS unsigned*)(lb + a_wr + 8 * A_STR * 4) = cvtpk(c.acc.z * c.scale, c.acc.w * c.scale);
  __syncthreads();
  ST(6);
  v4u am[4];
#pragma unroll
  for (int ks = 0; ks < 4; ++ks) am[ks] = *(LDS_AS v4u*)(lb + a_rd + 64 * ks);
  __builtin_amdgcn_sched_barrier(0);
#pragma unroll
  for (int ks = 0; ks < 4; ++ks)
#pragma unroll
    for (int ct = 0; ct < 4; ++ct) {
      const v2u b0 = c.of[2 * ks][ct][0], b1 = c.of[2 * ks][ct][1], b2 = c.of[2 * ks + 1][ct][0], b3 = c.of[2 * ks + 1][ct][1];
      const v8u bm = {b0.x, b0.y, b1.x, b1.y, b2.x, b2.y, b3.x, b3.y};
      y[ct] = __builtin_amdgcn_smfmac_f32_16x16x64_bf16(__builtin_bit_cast(bf16x8, am[ks]), __builtin_bit_cast(bf16x16, bm), y[ct], idx, 0, 0);
    }
  float sm[4];
#pragma unroll
  for (int ct = 0; ct < 4; ++ct) sm[ct] = (y[ct].x + y[ct].y) + (y[ct].z + y[ct].w);
  const float yv = add32(add16(sm[0], sm[1]), add16(sm[2], sm[3]));
  const unsigned t0 = c.tag & 255u, t1 = (c.tag >> 8) & 255u;
  const v2u g = {(__float_as_uint(yv) & ~255u) | t0, (__float_as_uint(dpp<XOR1>(yv)) & ~255u) | t1};
  __builtin_amdgcn_raw_buffer_store_b64(g, c.rw, y_off, 0, AUX_SC1);
  ST(8);

  if (w == NW - 1 && prod < DM / 16) {
    v4u v[4], u[4];
    bool got[4] = {false, false, false, false}, done = false;
#pragma unroll 1
    for (unsigned spins = 0; !done && spins < SPIN_CAP; ++spins) {
      COMPILER_FENCE();
#pragma unroll
      for (int i = 0; i < 4; ++i)
        if (!got[i]) v[i] = __builtin_amdgcn_raw_buffer_load_b128(c.rw, p_off, i * 16 * PKT_BYTES, AUX_SC1);
#pragma unroll
      for (int i = 0; i < 4; ++i) {
        u[i] = v[i] ^ v4u{t0, t1, t0, t1};
        got[i] = __all(((u[i].x | u[i].y | u[i].z | u[i].w) & 255u) == 0u);
      }
      done = got[0] & got[1] & got[2] & got[3];
    }
    const f32x4 r = (__builtin_bit_cast(f32x4, u[0]) + __builtin_bit_cast(f32x4, u[1])) +
                    (__builtin_bit_cast(f32x4, u[2]) + __builtin_bit_cast(f32x4, u[3]));
    float z = add16(add32(r.x, r.z), add32(r.y, r.w));
    z += dpp<ROR4>(z);
    z += dpp<ROR8>(z);
    const __bf16 zb = (__bf16)z;
    __builtin_amdgcn_raw_buffer_store_b16(done ? __builtin_bit_cast(unsigned short, zb) : (unsigned short)0x7fc0u, rout, o_off, 0, 0);
    __builtin_amdgcn_raw_buffer_store_b32(c.tag, c.rw, e_off, 0, AUX_SC1);
    ST(9);
  }
  if (w == NW - 1) {
    unsigned* sp = (unsigned*)(ws + (6u << 20));
    if (lane == 0) { __hip_atomic_store(sp + bid, (unsigned)c.stv[0], __ATOMIC_RELAXED, __HIP_MEMORY_SCOPE_AGENT);
                     __hip_atomic_store(sp + 256 + bid, (unsigned)(c.stv[GA] - c.stv[GB]), __ATOMIC_RELAXED, __HIP_MEMORY_SCOPE_AGENT);
                     __hip_atomic_store(sp + 512 + bid, (unsigned)c.stv[9], __ATOMIC_RELAXED, __HIP_MEMORY_SCOPE_AGENT); }
    if (bid == 0) {
      const unsigned long long t1 = __builtin_amdgcn_s_memrealtime();
      while (__builtin_amdgcn_s_memrealtime() - t1 < 1000) __builtin_amdgcn_s_sleep(2);
      const unsigned ref = (unsigned)c.stv[0] - 100000;
      unsigned mn0 = ~0u, mx = 0, sum = 0;
      for (int i = lane; i < 256; i += 64) {
        const unsigned a = __hip_atomic_load(sp + i, __ATOMIC_RELAXED, __HIP_MEMORY_SCOPE_AGENT) - ref;
        sum += __hip_atomic_load(sp + 256 + i, __ATOMIC_RELAXED, __HIP_MEMORY_SCOPE_AGENT);
        const unsigned e = __hip_atomic_load(sp + 512 + i, __ATOMIC_RELAXED, __HIP_MEMORY_SCOPE_AGENT);
        mn0 = a < mn0 ? a : mn0;
        if (e) { const unsigned bb = e - ref; mx = bb > mx ? bb : mx; }
      }
      for (int o = 32; o; o >>= 1) { const unsigned a = __shfl_xor(mn0, o), bq = __shfl_xor(mx, o), cq = __shfl_xor(sum, o); mn0 = a < mn0 ? a : mn0; mx = bq > mx ? bq : mx; sum += cq; }
      int hi = ((int)(mx - mn0) - 3800) / 20, lo = ((int)(sum / 256) - GLO) / 10;
      hi = hi < 0 ? 0 : hi > 31 ? 31 : hi; lo = lo < 0 ? 0 : lo > 31 ? 31 : lo;
      const unsigned tgt = __builtin_amdgcn_readfirstlane(mn0 + ref + 10000 + (hi * 32 + lo) * 400);
      while ((int)((unsigned)__builtin_amdgcn_s_memrealtime() - tgt) < 0) __builtin_amdgcn_s_sleep(2);
    }
  }
}

}

extern "C" void kernel_launch(void* const* d_in, const int* in_sizes, int n_in, void* d_out, int out_size, void* d_ws,
                              size_t ws_size, hipStream_t stream) {
  (void)in_sizes; (void)n_in; (void)out_size; (void)ws_size;
  mha_decode<<<256, NTHR, 0, stream>>>((const __bf16*)d_in[0], (const __bf16*)d_in[1], (const __bf16*)d_in[2],
                                       (const __bf16*)d_in[3], (const __bf16*)d_in[4], (const __bf16*)d_in[5],
                                       (const __bf16*)d_in[6], (const __bf16*)d_in[7], (const __bf16*)d_in[8],
                                       (const __bf16*)d_in[9], (__bf16*)d_out, (unsigned char*)d_ws);
}
```

```cpp
#include <hip/hip_runtime.h>
#include <hip/hip_bf16.h>
#include <utility>

namespace {

typedef unsigned v2u __attribute__((ext_vector_type(2)));
typedef unsigned v4u __attribute__((ext_vector_type(4)));
typedef unsigned v8u __attribute__((ext_vector_type(8)));
typedef float f32x2 __attribute__((ext_vector_type(2)));
typedef float f32x4 __attribute__((ext_vector_type(4)));
typedef __bf16 bf16x2 __attribute__((ext_vector_type(2)));
typedef __bf16 bf16x8 __attribute__((ext_vector_type(8)));
typedef __bf16 bf16x16 __attribute__((ext_vector_type(16)));
typedef short i16x4 __attribute__((ext_vector_type(4)));
typedef __amdgpu_buffer_rsrc_t rsrc_t;
#define LDS_AS __attribute__((address_space(3)))
#define CONST_AS __attribute__((address_space(4)))
#define COMPILER_FENCE() asm volatile("" ::: "memory")

constexpr int DM = 448, NH = 16, SK = 4096, DH = 256;
constexpr int NW = 8, NTHR = NW * 64;
constexpr int N_Q = 4, N_K = 64, N_V = 64, N_O = 32;
constexpr unsigned SPIN_CAP = 1u << 20;
constexpr int RSRC_W3 = 0x00020000, AUX_NT = 2, AUX_SC1 = 16, OOB = 0x40000000;
constexpr int WS_PKT = 4096, PKT_BYTES = DM * 4, WS_Q = WS_PKT + 256 * PKT_BYTES, WS_ML = WS_Q + 64 * 2048,
              WS_T = WS_ML + 64 * 128, WS_END = WS_T + 4 * 512;
constexpr int A_STR = 68;
constexpr int L_RED = 0, L_T = L_RED + 16 * A_STR, L_TQ = L_T + NW * 1024, L_ML = L_TQ + NW * 1024, L_P = L_ML + 2 * NW,
              L_F = L_P + NW * 64, L_R = L_F + NW * 2, L_END = L_R + 2;

constexpr int WIN = 3;
constexpr int N_END = 0;
constexpr int O_FIRST = 4;
constexpr int O_START = 24, O_EVERY = 2;
constexpr int X_AT = 2, QMM_AT = 6, QPUB_AT = 9, QLD_AT = 17, QCHK_AT = QLD_AT + 4, YPF_AT = 100;
constexpr int BOOST_AT = 6, BOOST_MAX = 40, BOOST_T = 410, BOOST_G = 5;
constexpr int N_FIRST = N_Q + O_FIRST + 1, N_LAST = 3;

enum Op : int { IQ, IK, IV, IO, IX, QPUB, QLD, MLPUB, YPF, TPUB, TLD, PEERLD,
                WAITV, WAITB, WAITS, LAG, TCHK, SETUP, QTR, QMM, QCHK, CK, SM1, SM2, CV, PEERCHK, TRW, TRR };
struct Sched { int n; int op[900]; int a[900]; };
constexpr Sched make_sched() {
  Sched s{};
  int vmk[256] = {}, vmi[256] = {}, nvm = 0;
  auto emit = [&](int op, int a) {
    s.op[s.n] = op; s.a[s.n] = a; ++s.n;
    if (op <= PEERLD) { vmk[nvm] = op; vmi[nvm] = a; ++nvm; }
  };
  int no = 0, nk = 0, nv = 0, nw = 0, nr = 0, sm = 0;
  int bs = -1;
  int lk = 0, lv = 0, lo = 0;
  for (int j = 0; j < N_Q; ++j) emit(IQ, j);
  while (no < O_FIRST) emit(IO, no++);
  for (int j = 0; j < N_K + N_V; ++j) {
    if (j > 0) {
      const int win = j >= N_K + N_V - N_END ? WIN + 1 : WIN;
      int p = nvm, seen = 0;
      while (seen < win) { --p; if (vmk[p] <= IV) ++seen; }
      if (bs >= 0 && j - bs <= BOOST_MAX && win == WIN) {
        int p2 = p;
        do --p2; while (vmk[p2] > IV);
        emit(WAITV, nvm - 1 - p2);
        emit(WAITB, (nvm - 1 - p) | ((j - bs) << 8));
        if (3 && j - bs <= 4) { int p0 = p; do ++p0; while (vmk[p0] > IV); emit(WAITS, (nvm - 1 - p0) | ((j - bs) << 8)); }
        p = p2;
      } else emit(WAITV, nvm - 1 - p);
      if (j >= 7 && (j - 7) % 8 == 0 && j <= 120) { emit(LAG, j * 436 / 16); bs = j; }
      if (vmk[p] == IK) lk = vmi[p] + 1;
      if (vmk[p] == IV) { lk = N_K; lv = vmi[p] + 1; }
      for (int t = 0; t < p; ++t) if (vmk[t] == IO) lo = vmi[t] + 1;
    }
    if (j < N_K) emit(IK, j); else emit(IV, j - N_K);
    if (j == 0) emit(SETUP, 0);
    if (j >= O_START && (j - O_START) % O_EVERY == 0 && no < N_O) emit(IO, no++);
    if (j == X_AT) emit(IX, 0);
    if (j == YPF_AT) emit(YPF, 0);
    for (int t = 0; t < 1; ++t) {
      const int cp = t == 0 ? 10 : t == 1 ? 50 : 84;
      if (j == cp) emit(TPUB, t);
      if (j == cp + 8) emit(TLD, t);
      if (j == cp + 8 + 4) emit(TCHK, cp);
    }
    if (j == QMM_AT - 1) emit(QTR, 0);
    if (j == QMM_AT) emit(QMM, 0);
    if (j == QPUB_AT) emit(QPUB, 0);
    if (j == QLD_AT) { emit(QLD, 0); emit(QLD, 1); }
    if (j == QCHK_AT) emit(QCHK, 0);
    else if (j > QCHK_AT && nk < N_K) { for (int t = 0; t < 3 && nk < lk; ++t) emit(CK, nk++); }
    else if (nk == N_K && sm < 2) { emit(sm == 0 ? SM1 : SM2, 0); ++sm; }
    else if (sm == 2) {
      for (int t = 0; t < 3 && nv < lv; ++t) {
        emit(CV, nv++);
        if (nv == 12) emit(MLPUB, 0);
        if (nv == N_V - 20) emit(PEERLD, 0);
        if (nv == N_V - 10) emit(PEERCHK, 0);
      }
    }
    if (nr < nw) emit(TRR, nr++);
    else if (4 * nw + 4 <= lo) emit(TRW, nw++);
  }
  if (no != N_O || nr != N_O / 4 || nv < N_V - 10 || nv > N_V - N_LAST) s.n = -1;
  else while (nv < N_V) emit(CV, nv++);
  return s;
}
constexpr Sched S = make_sched();
static_assert(S.n > 0, "the schedule must finish everything but the last V loads inside the stream");

constexpr int group_off(int g) { return ((g & 3) << 16) | ((g >> 2) << 19); }

__device__ __forceinline__ float dot2(unsigned a, unsigned b, float c) {
  return __builtin_amdgcn_fdot2_f32_bf16(__builtin_bit_cast(bf16x2, a), __builtin_bit_cast(bf16x2, b), c, false);
}
__device__ __forceinline__ unsigned cvtpk(float lo, float hi) {
  const bf16x2 r = {(__bf16)lo, (__bf16)hi};
  return __builtin_bit_cast(unsigned, r);
}
template <int CTRL> __device__ __forceinline__ float dpp(float x) {
  return __builtin_bit_cast(float, __builtin_amdgcn_mov_dpp(__builtin_bit_cast(int, x), CTRL, 0xf, 0xf, true));
}
constexpr int XOR1 = 0xB1, XOR2 = 0x4E, XOR7 = 0x141, XOR15 = 0x140, ROR4 = 0x124, ROR8 = 0x128;
__device__ __forceinline__ float sum8(float x) { x += dpp<XOR1>(x); x += dpp<XOR2>(x); x += dpp<XOR7>(x); return x; }
__device__ __forceinline__ float max8(float x) {
  x = fmaxf(x, dpp<XOR1>(x)); x = fmaxf(x, dpp<XOR2>(x)); x = fmaxf(x, dpp<XOR7>(x));
  return x;
}
__device__ __forceinline__ float add16(float a, float b) {
  const auto s = __builtin_amdgcn_permlane16_swap(__float_as_uint(a), __float_as_uint(b), false, false);
  return __uint_as_float(s[0]) + __uint_as_float(s[1]);
}
__device__ __forceinline__ float add32(float a, float b) {
  const auto s = __builtin_amdgcn_permlane32_swap(__float_as_uint(a), __float_as_uint(b), false, false);
  return __uint_as_float(s[0]) + __uint_as_float(s[1]);
}
template <int CTRL> __device__ __forceinline__ float tstep(float lo, float hi, bool sel) {
  const float a = lo + dpp<CTRL>(lo), b = hi + dpp<CTRL>(hi);
  return sel ? b : a;
}
__device__ __forceinline__ float wave_sum(float x) {
  x = sum8(x); x += dpp<XOR15>(x); x = add16(x, x);
  return add32(x, x);
}
__device__ __forceinline__ float wave_max(float x) {
  x = max8(x); x = fmaxf(x, dpp<XOR15>(x));
  const auto s = __builtin_amdgcn_permlane16_swap(__float_as_uint(x), __float_as_uint(x), false, false);
  x = fmaxf(__uint_as_float(s[0]), __uint_as_float(s[1]));
  const auto t = __builtin_amdgcn_permlane32_swap(__float_as_uint(x), __float_as_uint(x), false, false);
  return fmaxf(__uint_as_float(t[0]), __uint_as_float(t[1]));
}
__device__ __forceinline__ f32x4 mfma(v4u a, v4u b, f32x4 c) {
  return __builtin_amdgcn_mfma_f32_16x16x32_bf16(__builtin_bit_cast(bf16x8, a), __builtin_bit_cast(bf16x8, b), c, 0, 0, 0);
}
__device__ __forceinline__ v2u tr_read(const float* p) {
  return __builtin_bit_cast(v2u, __builtin_amdgcn_ds_read_tr16_b64_v4i16((LDS_AS i16x4*)p));
}
__device__ __forceinline__ void await_tags(v4u& g, rsrc_t r, int off, unsigned tag) {
  if (__builtin_expect(!__all((g.y == tag) & (g.w == tag)), 0)) {
    unsigned spins = 0;
    bool ok;
#pragma unroll 1
    do {
      COMPILER_FENCE();
      g = __builtin_amdgcn_raw_buffer_load_b128(r, off, 0, AUX_SC1);
      ok = __all((g.y == tag) & (g.w == tag));
    } while (!ok && ++spins < SPIN_CAP);
    if (!ok) g.x = 0x7fc00000u;
  }
}
__device__ __forceinline__ v2u granule(float a, float b, unsigned tag) {
  return v2u{(__float_as_uint(a) & ~255u) | (tag & 255u), (__float_as_uint(b) & ~255u) | ((tag >> 8) & 255u)};
}
__device__ __forceinline__ bool untag(v4u& g, unsigned tag) {
  const unsigned t0 = tag & 255u, t1 = (tag >> 8) & 255u;
  g ^= v4u{t0, t1, t0, t1};
  return __all(((g.x | g.y | g.z | g.w) & 255u) == 0u);
}
__device__ __forceinline__ f32x4 await16(v4u g, rsrc_t r, int off, unsigned tag) {
  if (__builtin_expect(!untag(g, tag), 0)) {
    unsigned spins = 0;
    bool ok;
#pragma unroll 1
    do {
      COMPILER_FENCE();
      g = __builtin_amdgcn_raw_buffer_load_b128(r, off, 0, AUX_SC1);
      ok = untag(g, tag);
    } while (!ok && ++spins < SPIN_CAP);
    if (!ok) g.x = 0x7fc00000u;
  }
  return __builtin_bit_cast(f32x4, g);
}
__device__ __forceinline__ void flag_set(float* lds, int k, int w) {
  asm volatile("s_waitcnt lgkmcnt(0)" ::: "memory");
  ((LDS_AS volatile unsigned*)lds)[L_F + k * NW + w] = 1u;
}
__device__ __forceinline__ void flag_wait(float* lds, int k, int lane) {
  while (!__all(((LDS_AS volatile unsigned*)lds)[L_F + k * NW + (lane & 7)] != 0u)) {}
  COMPILER_FENCE();
}

struct Ctx {
  rsrc_t rq, rk, rv, ro, rx, rw;
  float* lds;
  int w, lane, vo_q, vo_kv, vo_o, vo_x, so_q, so_kv, so_o, orot, q_st, q_ld, ml_st, ml_ld, y_pf, boost, slow, t_st;
  unsigned tc, tagt, epa;
  v2u tg;
  unsigned t0, epoch, tag, pf, cpf, sel[4];
  v4u qb[N_Q], kb[N_K], vb[N_V], ob[N_O], xv, qg[2], qr, pp, peer;
  v2u qf[4][2];
  v2u of[8][4][2];
  f32x4 acc;
  float xs[5], s0, s1, mw, scale;
};

template <int I> __device__ __forceinline__ void step(Ctx& c) {
  constexpr int op = S.op[I], a = S.a[I];
  float* const lds = c.lds;
  const int w = c.w, lane = c.lane;
  if constexpr (op == IQ) {
    c.qb[a] = __builtin_amdgcn_raw_buffer_load_b128(c.rq, c.vo_q, c.so_q + 8 * a * (NH * DH * 2), AUX_NT);
  } else if constexpr (op == IK) {
    c.kb[a] = __builtin_amdgcn_raw_buffer_load_b128(c.rk, c.vo_kv + (a & 3) * 1024, c.so_kv ^ group_off(a >> 2), AUX_NT);
  } else if constexpr (op == IV) {
    c.vb[a] = __builtin_amdgcn_raw_buffer_load_b128(c.rv, c.vo_kv + (a & 3) * 1024, c.so_kv ^ group_off(a >> 2), AUX_NT);
  } else if constexpr (op == IO) {
    c.ob[a] = __builtin_amdgcn_raw_buffer_load_b128(c.ro, c.vo_o, c.so_o + ((8 * a + 32 * c.orot) & 255) * (DM * 2), 0);
  } else if constexpr (op == IX) {
    c.xv = __builtin_amdgcn_raw_buffer_load_b128(c.rx, c.vo_x, 0, 0);
  } else if constexpr (op == WAITV) {
    __builtin_amdgcn_s_waitcnt((a & 15) | ((a >> 4) << 14) | 0x0F70);
  } else if constexpr (op == LAG) {
    unsigned rv = ((LDS_AS volatile unsigned*)lds)[L_R];
    asm volatile("" : "+v"(rv));
    const unsigned ref = __builtin_amdgcn_readfirstlane(rv);
    const int late = (int)((unsigned)__builtin_amdgcn_s_memrealtime() - (ref ? ref : c.t0 + 260)) - a;
    c.boost = late * BOOST_G >> 4;
    c.slow = (-late - 65) * 3 >> 4;
  } else if constexpr (op == TPUB) {
    c.tc = (unsigned)__builtin_amdgcn_s_memrealtime();
    __builtin_amdgcn_raw_buffer_store_b64(v2u{c.tc, c.tagt}, c.rw, c.t_st, a * 512, AUX_SC1);
  } else if constexpr (op == TLD) {
    c.tg = __builtin_amdgcn_raw_buffer_load_b64(c.rw, (WS_T + lane * 8) | (w == NW - 1 ? 0 : OOB), a * 512, AUX_SC1);
  } else if constexpr (op == TCHK) {
    const bool ok = c.tg.y == c.tagt;
    const float d = wave_sum(ok ? (float)(int)(c.tg.x - c.tc) : 0.f), n = wave_sum(ok ? 1.f : 0.f);
    const unsigned ref = (c.tc + (int)(d * __builtin_amdgcn_rcpf(fmaxf(n, 1.f))) - a * 436 / 16 + 28) | 1u;
    ((LDS_AS volatile unsigned*)lds)[L_R + (w == NW - 1 && n > 16.f ? 0 : 1)] = ref;
  } else if constexpr (op == WAITS) {
    asm volatile("s_cmp_lt_i32 %0, %1\n\ts_cbranch_scc1 1f\n\ts_waitcnt vmcnt(%2)\n1:"
                 :: "s"(c.slow), "i"(a >> 8), "i"(a & 255) : "scc", "memory");
  } else if constexpr (op == WAITB) {
    asm volatile("s_cmp_ge_i32 %0, %1\n\ts_cbranch_scc1 1f\n\ts_waitcnt vmcnt(%2)\n1:"
                 :: "s"(c.boost), "i"(a >> 8), "i"(a & 255) : "scc", "memory");
  } else if constexpr (op == SETUP) {
    const int row = lane & 15, j = row & 7;
    const bool act = ((lane >> 4) & 1) == (row >> 3);
    const unsigned two = (lane >> 5) ? 0x0302u : 0x0100u;
    const unsigned s = (j & 1) ? ((two << 16) | 0x0c0cu) : (0x0c0c0000u | two);
#pragma unroll
    for (int i = 0; i < 4; ++i) c.sel[i] = (act && (j >> 1) == i) ? s : 0x0c0c0c0cu;
    c.acc = f32x4{0.f, 0.f, 0.f, 0.f};
    ((unsigned*)lds)[L_F + (lane & 1) * NW + w] = 0u;
    ((unsigned*)lds)[L_R] = 0u;
    asm volatile("" :: "s"(c.cpf));
    __syncthreads();
  } else if constexpr (op == QTR) {
#pragma unroll
    for (int i = 0; i < 4; ++i) *(v4u*)&lds[L_TQ + w * 1024 + i * 256 + lane * 4] = c.qb[i];
    COMPILER_FENCE();
    const float* base = &lds[L_TQ + w * 1024 + (lane >> 4) * 256 + ((lane >> 2) & 3) * 32 + (lane & 3) * 2];
#pragma unroll
    for (int ct = 0; ct < 4; ++ct)
#pragma unroll
      for (int h = 0; h < 2; ++h) c.qf[ct][h] = tr_read(base + h * 128 + ct * 8);
    COMPILER_FENCE();
  } else if constexpr (op == QMM) {
    f32x4 d[4];
#pragma unroll
    for (int ct = 0; ct < 4; ++ct)
      d[ct] = mfma(c.xv, v4u{c.qf[ct][0].x, c.qf[ct][0].y, c.qf[ct][1].x, c.qf[ct][1].y}, f32x4{0.f, 0.f, 0.f, 0.f});
    c.tag = c.epoch * 0x9E3779B1u + 0x7F4A7C15u;
    c.tagt = c.epa * 0x9E3779B1u + 0x7F4A7C15u;
    asm volatile("" : "+s"(c.tag));
    const f32x4 lo = (lane & 16) ? d[1] : d[0], hi = (lane & 16) ? d[3] : d[2];
    *(f32x2*)&lds[L_RED + w * 128 + lane * 2] = (lane & 32) ? f32x2{hi.x, hi.y} : f32x2{lo.x, lo.y};
    flag_set(lds, 0, w);
  } else if constexpr (op == QPUB) {
    flag_wait(lds, 0, lane);
    f32x2 t = {0.f, 0.f};
#pragma unroll
    for (int i = 0; i < 7; ++i) t += *(const f32x2*)&lds[L_RED + i * 128 + lane * 2];
    const float nx = dpp<XOR1>(t.x), ny = dpp<XOR1>(t.y);
    __builtin_amdgcn_raw_buffer_store_b64(granule((lane & 1) ? ny : t.x, (lane & 1) ? t.y : nx, c.tag), c.rw, c.q_st, 0, AUX_SC1);
  } else if constexpr (op == QLD) {
    c.qg[a] = __builtin_amdgcn_raw_buffer_load_b128(c.rw, c.q_ld + a * 1024, 0, AUX_SC1);
  } else if constexpr (op == QCHK) {
    const f32x4 q = await16(c.qg[0], c.rw, c.q_ld, c.tag) + await16(c.qg[1], c.rw, c.q_ld + 1024, c.tag);
    *(v2u*)&lds[L_TQ + w * 1024 + lane * 2] = v2u{cvtpk(q.x, q.y), cvtpk(q.z, q.w)};
    COMPILER_FENCE();
    c.qr = *(const v4u*)&lds[L_TQ + w * 1024 + (lane & 31) * 4];
  } else if constexpr (op == CK) {
    const v4u k = c.kb[a], q = c.qr;
    float x = dot2(k.x, q.x, dot2(k.z, q.z, 0.f)) + dot2(k.y, q.y, dot2(k.w, q.w, 0.f));
    if constexpr ((a & 1) == 1) x = add16(c.xs[0], x);
    if constexpr ((a & 3) == 3) x = tstep<ROR8>(c.xs[1], x, lane & 8);
    if constexpr ((a & 7) == 7) x = tstep<XOR7>(c.xs[2], x, lane & 4);
    if constexpr ((a & 15) == 15) x = tstep<XOR2>(c.xs[3], x, lane & 2);
    if constexpr ((a & 31) == 31) (a == 31 ? c.s0 : c.s1) = tstep<XOR1>(c.xs[4], x, lane & 1) * (0.0625f * 1.4426950408889634f);
    else c.xs[__builtin_ctz(~a)] = x;
  } else if constexpr (op == SM1) {
    const float m = wave_max(fmaxf(c.s0, c.s1));
    const float p0 = __builtin_amdgcn_exp2f(c.s0 - m), p1 = __builtin_amdgcn_exp2f(c.s1 - m);
    __bf16* pw = (__bf16*)&lds[L_P + w * 64] + (__builtin_bitreverse32(lane) >> 26);
    pw[0] = (__bf16)p0; pw[64] = (__bf16)p1;
    COMPILER_FENCE();
    c.mw = m; c.s0 = p0 + p1;
    c.pp = *(const v4u*)&lds[L_P + w * 64];
  } else if constexpr (op == SM2) {
    const float l = wave_sum(c.s0);
    lds[L_ML + 2 * w + (lane & 1)] = (lane & 1) ? l : c.mw;
    flag_set(lds, 1, w);
  } else if constexpr (op == MLPUB) {
    flag_wait(lds, 1, lane);
    const f32x2 ml = *(const f32x2*)&lds[L_ML + 2 * (lane & 7)];
    const float M = max8(ml.x), L = sum8(ml.y * __builtin_amdgcn_exp2f(ml.x - M));
    const v4u g = {__float_as_uint(M), c.tag, __float_as_uint(L), c.tag};
    __builtin_amdgcn_raw_buffer_store_b128(g, c.rw, c.ml_st, 0, AUX_SC1);
  } else if constexpr (op == CV) {
    const unsigned pr = c.pp[a & 3];
    const v4u am = {__builtin_amdgcn_perm(0u, pr, c.sel[0]), __builtin_amdgcn_perm(0u, pr, c.sel[1]),
                    __builtin_amdgcn_perm(0u, pr, c.sel[2]), __builtin_amdgcn_perm(0u, pr, c.sel[3])};
    c.acc = mfma(am, c.vb[a], c.acc);
    if constexpr ((a & 3) == 3 && a + 1 < N_V) c.pp = *(const v4u*)&lds[L_P + w * 64 + (a + 1)];
  } else if constexpr (op == YPF) {
    c.pf = __builtin_amdgcn_raw_buffer_load_b32(c.rw, c.y_pf, 0, AUX_SC1);
  } else if constexpr (op == PEERLD) {
    c.peer = __builtin_amdgcn_raw_buffer_load_b128(c.rw, c.ml_ld, 0, AUX_SC1);
  } else if constexpr (op == PEERCHK) {
    asm volatile("" :: "v"(c.pf));
    await_tags(c.peer, c.rw, c.ml_ld, c.tag);
    const float m = __uint_as_float(c.peer.x);
    float M = fmaxf(m, dpp<XOR1>(m)); M = fmaxf(M, dpp<XOR2>(M));
    float L = __uint_as_float(c.peer.z) * __builtin_amdgcn_exp2f(m - M); L += dpp<XOR1>(L); L += dpp<XOR2>(L);
    c.scale = __builtin_amdgcn_exp2f(c.mw - M) / L;
  } else if constexpr (op == TRW) {
#pragma unroll
    for (int i = 0; i < 4; ++i) *(v4u*)&lds[L_T + w * 1024 + i * 256 + lane * 4] = c.ob[4 * a + i];
    COMPILER_FENCE();
  } else if constexpr (op == TRR) {
    const float* base = &lds[L_T + w * 1024 + (lane >> 4) * 256 + ((lane >> 2) & 3) * 32 + (lane & 3) * 2];
#pragma unroll
    for (int ct = 0; ct < 4; ++ct)
#pragma unroll
      for (int h = 0; h < 2; ++h) c.of[a][ct][h] = tr_read(base + h * 128 + ct * 8);
    COMPILER_FENCE();
  }
  __builtin_amdgcn_sched_barrier(0);
}
template <int B, int... I> __device__ __forceinline__ void run_seq(Ctx& c, std::integer_sequence<int, I...>) { (step<B + I>(c), ...); }
template <int B, int E> __device__ __forceinline__ void run(Ctx& c) { run_seq<B>(c, std::make_integer_sequence<int, E - B>{}); }

__global__ void __launch_bounds__(NTHR, 2)
mha_decode(const __bf16* __restrict__ xA, const __bf16* __restrict__ kA, const __bf16* __restrict__ vA,
           const __bf16* __restrict__ wqA, const __bf16* __restrict__ woA,
           const __bf16* __restrict__ xB, const __bf16* __restrict__ kB, const __bf16* __restrict__ vB,
           const __bf16* __restrict__ wqB, const __bf16* __restrict__ woB, __bf16* out, unsigned char* ws) {
  __shared__ __attribute__((aligned(16))) float lds[L_END];
  Ctx c;
  const unsigned long long tstart = __builtin_amdgcn_s_memrealtime();
  __builtin_amdgcn_sched_barrier(0);
  const int bid = blockIdx.x, rk = bid >> 3, bh = (bid & 7) * 4 + (rk >> 3), br = bh >> 4, head = bh & 15;
  const int b = (rk >> 2) & 1, split = rk & 3, grp = br * 2 + b, prod = head * 4 + split;
  const int tid = threadIdx.x, lane = tid & 63, w = __builtin_amdgcn_readfirstlane(tid >> 6);
  const int off7 = w < 7 ? 0 : OOB;
  c.w = w; c.lane = lane; c.lds = lds;
  c.t0 = (unsigned)tstart;
  c.vo_q = ((lane >> 3) * (NH * DH * 2) + (lane & 7) * 16) | off7;
  c.so_q = ((224 * b + 32 * w) * NH * DH + head * DH + split * 64) * 2;
  c.vo_kv = lane * 16;
  c.so_kv = (b * NH + head) * (SK * DH * 2) + (w << 12) + ((split & 1) << 18) + ((split >> 1) << 15) +
            ((((head >> 1) ^ (head >> 3)) & 1) << 17) + (b << 19);
  c.vo_o = ((lane >> 3) * (DM * 2) + w * 128 + (lane & 7) * 16) | off7;
  c.so_o = head * DH * DM * 2;
  c.orot = rk & 7;
  int ep_off = br * 256;
  asm volatile("" : "+s"(c.so_q), "+s"(c.so_kv), "+s"(c.so_o), "+s"(c.orot), "+s"(ep_off), "+v"(c.vo_q), "+v"(c.vo_kv), "+v"(c.vo_o)
               : "s"(tstart));
  __builtin_amdgcn_sched_barrier(0);
  c.rq = __builtin_amdgcn_make_buffer_rsrc((void*)(br ? wqB : wqA), 0, DM * NH * DH * 2, RSRC_W3);
  c.ro = __builtin_amdgcn_make_buffer_rsrc((void*)(br ? woB : woA), 0, NH * DH * DM * 2, RSRC_W3);
  c.rk = __builtin_amdgcn_make_buffer_rsrc((void*)(br ? kB : kA), 0, 2 * NH * SK * DH * 2, RSRC_W3);
  if (0) { c.t0 = (unsigned)__builtin_amdgcn_s_memrealtime(); __builtin_amdgcn_sched_barrier(0); }
  run<0, 1>(c);
  c.epoch = *(const CONST_AS unsigned*)(ws + ep_off);
  c.epa = *(const CONST_AS unsigned*)ws;
  c.cpf = *(const CONST_AS unsigned*)((__builtin_amdgcn_s_getpc() & ~63ull) + min(rk * NW + w, 243) * 64);
  run<1, N_FIRST>(c);
  int br2 = br;
  asm volatile("" : "+s"(br2));
  c.rx = __builtin_amdgcn_make_buffer_rsrc((void*)(br2 ? xB : xA), 0, 2 * DM * 2, RSRC_W3);
  c.vo_x = (((lane & 1) * DM + 224 * b + 32 * w + 8 * (lane >> 4)) * 2) | off7;
  c.rv = __builtin_amdgcn_make_buffer_rsrc((void*)(br2 ? vB : vA), 0, 2 * NH * SK * DH * 2, RSRC_W3);
  c.rw = __builtin_amdgcn_make_buffer_rsrc((void*)ws, 0, WS_END, RSRC_W3);
  const rsrc_t rout = __builtin_amdgcn_make_buffer_rsrc((void*)out, 0, 4 * DM * 2, RSRC_W3);
  c.q_st = (WS_Q + bh * 4096 + (lane & 1) * 2048 + b * 1024 + split * 256 + (lane & ~1) * 4) | (w == 0 ? 0 : OOB);
  c.q_ld = WS_Q + bh * 4096 + b * 2048 + lane * 16;
  c.ml_st = (WS_ML + (grp * NH + head) * 128 + split * 16) | (tid == 0 ? 0 : OOB);
  c.ml_ld = WS_ML + (grp * NH + head) * 128 + (lane & 3) * 16;
  c.t_st = (WS_T + ((bid >> 5) * 8 + (bid & 7)) * 8) | (tid == 64 * 3 && ((rk + (rk >> 2)) & 3) == 0 ? 0 : OOB);
  c.y_pf = (WS_PKT + (grp * 64 + prod) * PKT_BYTES + lane * 128) | (tid < PKT_BYTES / 128 ? 0 : OOB);

  run<N_FIRST, S.n - N_LAST>(c);

  const int pk = WS_PKT + grp * 64 * PKT_BYTES;
  const int dq = 32 * (lane >> 5) + 2 * (lane & 15) + ((lane >> 4) & 1);
  int a_wr = (L_RED + w * A_STR + ((dq - 8 * c.orot) & 63)) * 4, a_rd = (L_RED + (lane & 15) * A_STR + 4 * (lane >> 4)) * 4;
  int idx = (lane & 8) ? 0xEEEE : 0x4444;
  int y_off = (pk + prod * PKT_BYTES + tid * 4) | off7 | ((lane & 1) ? OOB : 0);
  int p_off = pk + (lane >> 2) * PKT_BYTES + prod * 64 + (lane & 3) * 16;
  int o_off = ((b * (2 * DM) + br * DM + prod * 16 + (lane & 3) * 4 + (lane >> 4)) * 2) | ((lane & 12) ? OOB : 0);
  int e_off = (prod == 0 && b == 0 && lane == 0) ? ep_off : OOB;
  asm volatile("" : "+v"(a_wr), "+v"(a_rd), "+v"(idx), "+v"(y_off), "+v"(p_off), "+v"(o_off), "+v"(e_off));
  f32x4 y[4];
#pragma unroll
  for (int ct = 0; ct < 4; ++ct) { y[ct] = f32x4{0.f, 0.f, 0.f, 0.f}; asm volatile("" : "+v"(y[ct])); }

  run<S.n - N_LAST, S.n>(c);
  LDS_AS unsigned char* const lb = (LDS_AS unsigned char*)lds;
  *(LDS_AS unsigned*)(lb + a_wr) = cvtpk(c.acc.x * c.scale, c.acc.y * c.scale);
  *(LDS_AS unsigned*)(lb + a_wr + 8 * A_STR * 4) = cvtpk(c.acc.z * c.scale, c.acc.w * c.scale);
  __syncthreads();
  v4u am[4];
#pragma unroll
  for (int ks = 0; ks < 4; ++ks) am[ks] = *(LDS_AS v4u*)(lb + a_rd + 64 * ks);
  __builtin_amdgcn_sched_barrier(0);
#pragma unroll
  for (int ks = 0; ks < 4; ++ks)
#pragma unroll
    for (int ct = 0; ct < 4; ++ct) {
      const v2u b0 = c.of[2 * ks][ct][0], b1 = c.of[2 * ks][ct][1], b2 = c.of[2 * ks + 1][ct][0], b3 = c.of[2 * ks + 1][ct][1];
      const v8u bm = {b0.x, b0.y, b1.x, b1.y, b2.x, b2.y, b3.x, b3.y};
      y[ct] = __builtin_amdgcn_smfmac_f32_16x16x64_bf16(__builtin_bit_cast(bf16x8, am[ks]), __builtin_bit_cast(bf16x16, bm), y[ct], idx, 0, 0);
    }
  float sm[4];
#pragma unroll
  for (int ct = 0; ct < 4; ++ct) sm[ct] = (y[ct].x + y[ct].y) + (y[ct].z + y[ct].w);
  const float yv = add32(add16(sm[0], sm[1]), add16(sm[2], sm[3]));
  __builtin_amdgcn_raw_buffer_store_b64(granule(yv, dpp<XOR1>(yv), c.tag), c.rw, y_off, 0, AUX_SC1);

  if (w == NW - 1 && prod < DM / 16) {
    v4u v[4], u[4];
    bool got[4] = {false, false, false, false}, done = false;
#pragma unroll 1
    for (unsigned spins = 0; !done && spins < SPIN_CAP; ++spins) {
      COMPILER_FENCE();
#pragma unroll
      for (int i = 0; i < 4; ++i)
        if (!got[i]) v[i] = __builtin_amdgcn_raw_buffer_load_b128(c.rw, p_off, i * 16 * PKT_BYTES, AUX_SC1);
#pragma unroll
      for (int i = 0; i < 4; ++i) { u[i] = v[i]; got[i] = untag(u[i], c.tag); }
      done = got[0] & got[1] & got[2] & got[3];
    }
    const f32x4 r = (__builtin_bit_cast(f32x4, u[0]) + __builtin_bit_cast(f32x4, u[1])) +
                    (__builtin_bit_cast(f32x4, u[2]) + __builtin_bit_cast(f32x4, u[3]));
    float z = add16(add32(r.x, r.z), add32(r.y, r.w));
    z += dpp<ROR4>(z);
    z += dpp<ROR8>(z);
    const __bf16 zb = (__bf16)z;
    __builtin_amdgcn_raw_buffer_store_b16(done ? __builtin_bit_cast(unsigned short, zb) : (unsigned short)0x7fc0u, rout, o_off, 0, 0);
    __builtin_amdgcn_raw_buffer_store_b32(c.tag, c.rw, e_off, 0, AUX_SC1);
  }
}

}

extern "C" void kernel_launch(void* const* d_in, const int* in_sizes, int n_in, void* d_out, int out_size, void* d_ws,
                              size_t ws_size, hipStream_t stream) {
  (void)in_sizes; (void)n_in; (void)out_size; (void)ws_size;
  mha_decode<<<256, NTHR, 0, stream>>>((const __bf16*)d_in[0], (const __bf16*)d_in[1], (const __bf16*)d_in[2],
                                       (const __bf16*)d_in[3], (const __bf16*)d_in[4], (const __bf16*)d_in[5],
                                       (const __bf16*)d_in[6], (const __bf16*)d_in[7], (const __bf16*)d_in[8],
                                       (const __bf16*)d_in[9], (__bf16*)d_out, (unsigned char*)d_ws);
}
```

```cpp
#include <hip/hip_runtime.h>
#include <hip/hip_bf16.h>
#include <utility>

namespace {

typedef unsigned v2u __attribute__((ext_vector_type(2)));
typedef unsigned v4u __attribute__((ext_vector_type(4)));
typedef unsigned v8u __attribute__((ext_vector_type(8)));
typedef float f32x2 __attribute__((ext_vector_type(2)));
typedef float f32x4 __attribute__((ext_vector_type(4)));
typedef __bf16 bf16x2 __attribute__((ext_vector_type(2)));
typedef __bf16 bf16x8 __attribute__((ext_vector_type(8)));
typedef __bf16 bf16x16 __attribute__((ext_vector_type(16)));
typedef short i16x4 __attribute__((ext_vector_type(4)));
typedef __amdgpu_buffer_rsrc_t rsrc_t;
#define LDS_AS __attribute__((address_space(3)))
#define CONST_AS __attribute__((address_space(4)))
#define COMPILER_FENCE() asm volatile("" ::: "memory")

constexpr int DM = 448, NH = 16, SK = 4096, DH = 256;
constexpr int NW = 8, NTHR = NW * 64;
constexpr int N_Q = 4, N_K = 64, N_V = 64, N_O = 32;
constexpr unsigned SPIN_CAP = 1u << 20;
constexpr int RSRC_W3 = 0x00020000, AUX_NT = 2, AUX_SC1 = 16, OOB = 0x40000000;
constexpr int WS_PKT = 4096, PKT_BYTES = DM * 4, WS_Q = WS_PKT + 256 * PKT_BYTES, WS_ML = WS_Q + 64 * 2048,
              WS_T = WS_ML + 64 * 128, WS_END = WS_T + 512;
constexpr int A_STR = 68;
constexpr int L_RED = 0, L_T = L_RED + 16 * A_STR, L_TQ = L_T + NW * 1024, L_ML = L_TQ + NW * 1024, L_P = L_ML + 2 * NW,
              L_F = L_P + NW * 64, L_R = L_F + NW * 2, L_END = L_R + 2;

constexpr int WIN = 3;
constexpr int O_FIRST = 4;
constexpr int O_START = 24, O_EVERY = 2;
constexpr int X_AT = 2, QMM_AT = 6, QPUB_AT = 9, QLD_AT = 17, QCHK_AT = 21, T_AT = 10, YPF_AT = 100;
constexpr int N_FIRST = N_Q + O_FIRST + 1, N_LAST = 3;
constexpr int TAU = 439, ORIGIN0 = 255, MARGIN = 22, BAND = 58, GAIN_UP = 9, GAIN_DOWN = 4, LAG_FROM = 7, LAG_EVERY = 4, END_AFTER = 110;

enum Op : int { IQ, IK, IV, IO, IX, QPUB, QLD, MLPUB, YPF, TPUB, TLD, PEERLD,
                WAITV, WAITB, WAITS, LAG, TCHK, SETUP, QTR, QMM, QCHK, CK, SM1, SM2, CV, PEERCHK, TRW, TRR };
struct Sched { int n; int op[900]; int a[900]; };
constexpr Sched make_sched() {
  Sched s{};
  int vmk[256] = {}, vmi[256] = {}, nvm = 0;
  auto emit = [&](int op, int a) {
    s.op[s.n] = op; s.a[s.n] = a; ++s.n;
    if (op <= PEERLD) { vmk[nvm] = op; vmi[nvm] = a; ++nvm; }
  };
  int no = 0, nk = 0, nv = 0, nw = 0, nr = 0, sm = 0;
  int lk = 0, lv = 0, lo = 0;
  for (int j = 0; j < N_Q; ++j) emit(IQ, j);
  while (no < O_FIRST) emit(IO, no++);
  for (int j = 0; j < N_K + N_V; ++j) {
    if (j > 0) {
      int p = nvm, seen = 0;
      while (seen < WIN) { --p; if (vmk[p] <= IV) ++seen; }
      if (j > LAG_FROM) {
        const int k = (j - LAG_FROM - 1) % LAG_EVERY + 1;
        int p2 = p, p0 = p;
        do --p2; while (vmk[p2] > IV);
        do ++p0; while (vmk[p0] > IV);
        emit(WAITV, nvm - 1 - p2);
        emit(WAITB, (nvm - 1 - p) | (k << 8));
        emit(WAITS, (nvm - 1 - p0) | (k << 8));
        p = p2;
      } else emit(WAITV, nvm - 1 - p);
      if (j >= LAG_FROM && (j - LAG_FROM) % LAG_EVERY == 0 && j + 1 < N_K + N_V) emit(LAG, j * TAU / 16);
      if (vmk[p] == IK) lk = vmi[p] + 1;
      if (vmk[p] == IV) { lk = N_K; lv = vmi[p] + 1; }
      for (int t = 0; t < p; ++t) if (vmk[t] == IO) lo = vmi[t] + 1;
    }
    if (j < N_K) emit(IK, j); else emit(IV, j - N_K);
    if (j == 0) emit(SETUP, 0);
    if (j >= O_START && (j - O_START) % O_EVERY == 0 && no < N_O) emit(IO, no++);
    if (j == X_AT) emit(IX, 0);
    if (j == YPF_AT) emit(YPF, 0);
    if (j == T_AT) emit(TPUB, 0);
    if (j == T_AT + 8) emit(TLD, 0);
    if (j == T_AT + 12) emit(TCHK, 0);
    if (j == QMM_AT - 1) emit(QTR, 0);
    if (j == QMM_AT) emit(QMM, 0);
    if (j == QPUB_AT) emit(QPUB, 0);
    if (j == QLD_AT) { emit(QLD, 0); emit(QLD, 1); }
    if (j == QCHK_AT) emit(QCHK, 0);
    else if (j > QCHK_AT && nk < N_K) { for (int t = 0; t < 3 && nk < lk; ++t) emit(CK, nk++); }
    else if (nk == N_K && sm < 2) { emit(sm == 0 ? SM1 : SM2, 0); ++sm; }
    else if (sm == 2) {
      for (int t = 0; t < 3 && nv < lv; ++t) {
        emit(CV, nv++);
        if (nv == 12) emit(MLPUB, 0);
        if (nv == N_V - 20) emit(PEERLD, 0);
        if (nv == N_V - 10) emit(PEERCHK, 0);
      }
    }
    if (nr < nw) emit(TRR, nr++);
    else if (4 * nw + 4 <= lo) emit(TRW, nw++);
  }
  if (no != N_O || nr != N_O / 4 || nv < N_V - 10 || nv > N_V - N_LAST) s.n = -1;
  else while (nv < N_V) emit(CV, nv++);
  return s;
}
constexpr Sched S = make_sched();
static_assert(S.n > 0, "the schedule must finish everything but the last V loads inside the stream");

constexpr int group_off(int g) { return ((g & 3) << 16) | ((g >> 2) << 19); }

__device__ __forceinline__ float dot2(unsigned a, unsigned b, float c) {
  return __builtin_amdgcn_fdot2_f32_bf16(__builtin_bit_cast(bf16x2, a), __builtin_bit_cast(bf16x2, b), c, false);
}
__device__ __forceinline__ unsigned cvtpk(float lo, float hi) {
  const bf16x2 r = {(__bf16)lo, (__bf16)hi};
  return __builtin_bit_cast(unsigned, r);
}
template <int CTRL> __device__ __forceinline__ float dpp(float x) {
  return __builtin_bit_cast(float, __builtin_amdgcn_mov_dpp(__builtin_bit_cast(int, x), CTRL, 0xf, 0xf, true));
}
constexpr int XOR1 = 0xB1, XOR2 = 0x4E, XOR7 = 0x141, XOR15 = 0x140, ROR4 = 0x124, ROR8 = 0x128;
__device__ __forceinline__ float sum8(float x) { x += dpp<XOR1>(x); x += dpp<XOR2>(x); x += dpp<XOR7>(x); return x; }
__device__ __forceinline__ float max8(float x) {
  x = fmaxf(x, dpp<XOR1>(x)); x = fmaxf(x, dpp<XOR2>(x)); x = fmaxf(x, dpp<XOR7>(x));
  return x;
}
__device__ __forceinline__ float add16(float a, float b) {
  const auto s = __builtin_amdgcn_permlane16_swap(__float_as_uint(a), __float_as_uint(b), false, false);
  return __uint_as_float(s[0]) + __uint_as_float(s[1]);
}
__device__ __forceinline__ float add32(float a, float b) {
  const auto s = __builtin_amdgcn_permlane32_swap(__float_as_uint(a), __float_as_uint(b), false, false);
  return __uint_as_float(s[0]) + __uint_as_float(s[1]);
}
template <int CTRL> __device__ __forceinline__ float tstep(float lo, float hi, bool sel) {
  const float a = lo + dpp<CTRL>(lo), b = hi + dpp<CTRL>(hi);
  return sel ? b : a;
}
__device__ __forceinline__ float wave_sum(float x) {
  x = sum8(x); x += dpp<XOR15>(x); x = add16(x, x);
  return add32(x, x);
}
__device__ __forceinline__ float wave_max(float x) {
  x = max8(x); x = fmaxf(x, dpp<XOR15>(x));
  const auto s = __builtin_amdgcn_permlane16_swap(__float_as_uint(x), __float_as_uint(x), false, false);
  x = fmaxf(__uint_as_float(s[0]), __uint_as_float(s[1]));
  const auto t = __builtin_amdgcn_permlane32_swap(__float_as_uint(x), __float_as_uint(x), false, false);
  return fmaxf(__uint_as_float(t[0]), __uint_as_float(t[1]));
}
__device__ __forceinline__ f32x4 mfma(v4u a, v4u b, f32x4 c) {
  return __builtin_amdgcn_mfma_f32_16x16x32_bf16(__builtin_bit_cast(bf16x8, a), __builtin_bit_cast(bf16x8, b), c, 0, 0, 0);
}
__device__ __forceinline__ v2u tr_read(const float* p) {
  return __builtin_bit_cast(v2u, __builtin_amdgcn_ds_read_tr16_b64_v4i16((LDS_AS i16x4*)p));
}
__device__ __forceinline__ void await_tags(v4u& g, rsrc_t r, int off, unsigned tag) {
  if (__builtin_expect(!__all((g.y == tag) & (g.w == tag)), 0)) {
    unsigned spins = 0;
    bool ok;
#pragma unroll 1
    do {
      COMPILER_FENCE();
      g = __builtin_amdgcn_raw_buffer_load_b128(r, off, 0, AUX_SC1);
      ok = __all((g.y == tag) & (g.w == tag));
    } while (!ok && ++spins < SPIN_CAP);
    if (!ok) g.x = 0x7fc00000u;
  }
}
__device__ __forceinline__ v2u granule(float a, float b, unsigned tag) {
  return v2u{(__float_as_uint(a) & ~255u) | (tag & 255u), (__float_as_uint(b) & ~255u) | ((tag >> 8) & 255u)};
}
__device__ __forceinline__ bool untag(v4u& g, unsigned tag) {
  const unsigned t0 = tag & 255u, t1 = (tag >> 8) & 255u;
  g ^= v4u{t0, t1, t0, t1};
  return __all(((g.x | g.y | g.z | g.w) & 255u) == 0u);
}
__device__ __forceinline__ f32x4 await16(v4u g, rsrc_t r, int off, unsigned tag) {
  if (__builtin_expect(!untag(g, tag), 0)) {
    unsigned spins = 0;
    bool ok;
#pragma unroll 1
    do {
      COMPILER_FENCE();
      g = __builtin_amdgcn_raw_buffer_load_b128(r, off, 0, AUX_SC1);
      ok = untag(g, tag);
    } while (!ok && ++spins < SPIN_CAP);
    if (!ok) g.x = 0x7fc00000u;
  }
  return __builtin_bit_cast(f32x4, g);
}
__device__ __forceinline__ void flag_set(float* lds, int k, int w) {
  asm volatile("s_waitcnt lgkmcnt(0)" ::: "memory");
  ((LDS_AS volatile unsigned*)lds)[L_F + k * NW + w] = 1u;
}
__device__ __forceinline__ void flag_wait(float* lds, int k, int lane) {
  while (!__all(((LDS_AS volatile unsigned*)lds)[L_F + k * NW + (lane & 7)] != 0u)) {}
  COMPILER_FENCE();
}
__device__ __forceinline__ unsigned read_origin(float* lds) {
  unsigned v = ((LDS_AS volatile unsigned*)lds)[L_R];
  asm volatile("" : "+v"(v));
  return __builtin_amdgcn_readfirstlane(v);
}

struct Ctx {
  rsrc_t rq, rk, rv, ro, rx, rw;
  float* lds;
  int w, lane, vo_q, vo_kv, vo_o, vo_x, so_q, so_kv, so_o, orot, q_st, q_ld, ml_st, ml_ld, y_pf, boost, slow, t_st;
  unsigned tc, tagt, epa;
  v2u tg;
  unsigned t0, epoch, tag, pf, cpf, sel[4];
  v4u qb[N_Q], kb[N_K], vb[N_V], ob[N_O], xv, qg[2], qr, pp, peer;
  v2u qf[4][2];
  v2u of[8][4][2];
  f32x4 acc;
  float xs[5], s0, s1, mw, scale;
};

template <int I> __device__ __forceinline__ void step(Ctx& c) {
  constexpr int op = S.op[I], a = S.a[I];
  float* const lds = c.lds;
  const int w = c.w, lane = c.lane;
  if constexpr (op == IQ) {
    c.qb[a] = __builtin_amdgcn_raw_buffer_load_b128(c.rq, c.vo_q, c.so_q + 8 * a * (NH * DH * 2), AUX_NT);
  } else if constexpr (op == IK) {
    c.kb[a] = __builtin_amdgcn_raw_buffer_load_b128(c.rk, c.vo_kv + (a & 3) * 1024, c.so_kv ^ group_off(a >> 2), AUX_NT);
  } else if constexpr (op == IV) {
    c.vb[a] = __builtin_amdgcn_raw_buffer_load_b128(c.rv, c.vo_kv + (a & 3) * 1024, c.so_kv ^ group_off(a >> 2), AUX_NT);
  } else if constexpr (op == IO) {
    c.ob[a] = __builtin_amdgcn_raw_buffer_load_b128(c.ro, c.vo_o, c.so_o + ((8 * a + 32 * c.orot) & 255) * (DM * 2), 0);
  } else if constexpr (op == IX) {
    c.xv = __builtin_amdgcn_raw_buffer_load_b128(c.rx, c.vo_x, 0, 0);
  } else if constexpr (op == WAITV) {
    __builtin_amdgcn_s_waitcnt((a & 15) | ((a >> 4) << 14) | 0x0F70);
  } else if constexpr (op == LAG) {
    const unsigned org = read_origin(lds);
    const int late = (int)((unsigned)__builtin_amdgcn_s_memrealtime() - (org ? org : c.t0 + ORIGIN0)) - a;
    c.boost = late * GAIN_UP >> 4;
    c.slow = (-late - BAND) * GAIN_DOWN >> 4;
  } else if constexpr (op == TPUB) {
    c.tc = (unsigned)__builtin_amdgcn_s_memrealtime();
    __builtin_amdgcn_raw_buffer_store_b64(v2u{c.tc, c.tagt}, c.rw, c.t_st, 0, AUX_SC1);
  } else if constexpr (op == TLD) {
    c.tg = __builtin_amdgcn_raw_buffer_load_b64(c.rw, (WS_T + lane * 8) | (w == NW - 1 ? 0 : OOB), 0, AUX_SC1);
  } else if constexpr (op == TCHK) {
    const bool ok = c.tg.y == c.tagt;
    const float d = wave_sum(ok ? (float)(int)(c.tg.x - c.tc) : 0.f), n = wave_sum(ok ? 1.f : 0.f);
    const unsigned org = (c.tc + (int)(d * __builtin_amdgcn_rcpf(fmaxf(n, 1.f))) - T_AT * TAU / 16 + MARGIN) | 1u;
    ((LDS_AS volatile unsigned*)lds)[L_R + (w == NW - 1 && n > 16.f ? 0 : 1)] = org;
  } else if constexpr (op == WAITS) {
    asm volatile("s_cmp_lt_i32 %0, %1\n\ts_cbranch_scc1 1f\n\ts_waitcnt vmcnt(%2)\n1:"
                 :: "s"(c.slow), "i"(a >> 8), "i"(a & 255) : "scc", "memory");
  } else if constexpr (op == WAITB) {
    asm volatile("s_cmp_ge_i32 %0, %1\n\ts_cbranch_scc1 1f\n\ts_waitcnt vmcnt(%2)\n1:"
                 :: "s"(c.boost), "i"(a >> 8), "i"(a & 255) : "scc", "memory");
  } else if constexpr (op == SETUP) {
    const int row = lane & 15, j = row & 7;
    const bool act = ((lane >> 4) & 1) == (row >> 3);
    const unsigned two = (lane >> 5) ? 0x0302u : 0x0100u;
    const unsigned s = (j & 1) ? ((two << 16) | 0x0c0cu) : (0x0c0c0000u | two);
#pragma unroll
    for (int i = 0; i < 4; ++i) c.sel[i] = (act && (j >> 1) == i) ? s : 0x0c0c0c0cu;
    c.acc = f32x4{0.f, 0.f, 0.f, 0.f};
    ((unsigned*)lds)[L_F + (lane & 1) * NW + w] = 0u;
    ((unsigned*)lds)[L_R] = 0u;
    asm volatile("" :: "s"(c.cpf));
    __syncthreads();
  } else if constexpr (op == QTR) {
#pragma unroll
    for (int i = 0; i < 4; ++i) *(v4u*)&lds[L_TQ + w * 1024 + i * 256 + lane * 4] = c.qb[i];
    COMPILER_FENCE();
    const float* base = &lds[L_TQ + w * 1024 + (lane >> 4) * 256 + ((lane >> 2) & 3) * 32 + (lane & 3) * 2];
#pragma unroll
    for (int ct = 0; ct < 4; ++ct)
#pragma unroll
      for (int h = 0; h < 2; ++h) c.qf[ct][h] = tr_read(base + h * 128 + ct * 8);
    COMPILER_FENCE();
  } else if constexpr (op == QMM) {
    f32x4 d[4];
#pragma unroll
    for (int ct = 0; ct < 4; ++ct)
      d[ct] = mfma(c.xv, v4u{c.qf[ct][0].x, c.qf[ct][0].y, c.qf[ct][1].x, c.qf[ct][1].y}, f32x4{0.f, 0.f, 0.f, 0.f});
    c.tag = c.epoch * 0x9E3779B1u + 0x7F4A7C15u;
    c.tagt = c.epa * 0x9E3779B1u + 0x7F4A7C15u;
    asm volatile("" : "+s"(c.tag));
    const f32x4 lo = (lane & 16) ? d[1] : d[0], hi = (lane & 16) ? d[3] : d[2];
    *(f32x2*)&lds[L_RED + w * 128 + lane * 2] = (lane & 32) ? f32x2{hi.x, hi.y} : f32x2{lo.x, lo.y};
    flag_set(lds, 0, w);
  } else if constexpr (op == QPUB) {
    flag_wait(lds, 0, lane);
    f32x2 t = {0.f, 0.f};
#pragma unroll
    for (int i = 0; i < 7; ++i) t += *(const f32x2*)&lds[L_RED + i * 128 + lane * 2];
    const float nx = dpp<XOR1>(t.x), ny = dpp<XOR1>(t.y);
    __builtin_amdgcn_raw_buffer_store_b64(granule((lane & 1) ? ny : t.x, (lane & 1) ? t.y : nx, c.tag), c.rw, c.q_st, 0, AUX_SC1);
  } else if constexpr (op == QLD) {
    c.qg[a] = __builtin_amdgcn_raw_buffer_load_b128(c.rw, c.q_ld + a * 1024, 0, AUX_SC1);
  } else if constexpr (op == QCHK) {
    const f32x4 q = await16(c.qg[0], c.rw, c.q_ld, c.tag) + await16(c.qg[1], c.rw, c.q_ld + 1024, c.tag);
    *(v2u*)&lds[L_TQ + w * 1024 + lane * 2] = v2u{cvtpk(q.x, q.y), cvtpk(q.z, q.w)};
    COMPILER_FENCE();
    c.qr = *(const v4u*)&lds[L_TQ + w * 1024 + (lane & 31) * 4];
  } else if constexpr (op == CK) {
    const v4u k = c.kb[a], q = c.qr;
    float x = dot2(k.x, q.x, dot2(k.z, q.z, 0.f)) + dot2(k.y, q.y, dot2(k.w, q.w, 0.f));
    if constexpr ((a & 1) == 1) x = add16(c.xs[0], x);
    if constexpr ((a & 3) == 3) x = tstep<ROR8>(c.xs[1], x, lane & 8);
    if constexpr ((a & 7) == 7) x = tstep<XOR7>(c.xs[2], x, lane & 4);
    if constexpr ((a & 15) == 15) x = tstep<XOR2>(c.xs[3], x, lane & 2);
    if constexpr ((a & 31) == 31) (a == 31 ? c.s0 : c.s1) = tstep<XOR1>(c.xs[4], x, lane & 1) * (0.0625f * 1.4426950408889634f);
    else c.xs[__builtin_ctz(~a)] = x;
  } else if constexpr (op == SM1) {
    const float m = wave_max(fmaxf(c.s0, c.s1));
    const float p0 = __builtin_amdgcn_exp2f(c.s0 - m), p1 = __builtin_amdgcn_exp2f(c.s1 - m);
    __bf16* pw = (__bf16*)&lds[L_P + w * 64] + (__builtin_bitreverse32(lane) >> 26);
    pw[0] = (__bf16)p0; pw[64] = (__bf16)p1;
    COMPILER_FENCE();
    c.mw = m; c.s0 = p0 + p1;
    c.pp = *(const v4u*)&lds[L_P + w * 64];
  } else if constexpr (op == SM2) {
    const float l = wave_sum(c.s0);
    lds[L_ML + 2 * w + (lane & 1)] = (lane & 1) ? l : c.mw;
    flag_set(lds, 1, w);
  } else if constexpr (op == MLPUB) {
    flag_wait(lds, 1, lane);
    const f32x2 ml = *(const f32x2*)&lds[L_ML + 2 * (lane & 7)];
    const float M = max8(ml.x), L = sum8(ml.y * __builtin_amdgcn_exp2f(ml.x - M));
    const v4u g = {__float_as_uint(M), c.tag, __float_as_uint(L), c.tag};
    __builtin_amdgcn_raw_buffer_store_b128(g, c.rw, c.ml_st, 0, AUX_SC1);
  } else if constexpr (op == CV) {
    const unsigned pr = c.pp[a & 3];
    const v4u am = {__builtin_amdgcn_perm(0u, pr, c.sel[0]), __builtin_amdgcn_perm(0u, pr, c.sel[1]),
                    __builtin_amdgcn_perm(0u, pr, c.sel[2]), __builtin_amdgcn_perm(0u, pr, c.sel[3])};
    c.acc = mfma(am, c.vb[a], c.acc);
    if constexpr ((a & 3) == 3 && a + 1 < N_V) c.pp = *(const v4u*)&lds[L_P + w * 64 + (a + 1)];
  } else if constexpr (op == YPF) {
    c.pf = __builtin_amdgcn_raw_buffer_load_b32(c.rw, c.y_pf, 0, AUX_SC1);
  } else if constexpr (op == PEERLD) {
    c.peer = __builtin_amdgcn_raw_buffer_load_b128(c.rw, c.ml_ld, 0, AUX_SC1);
  } else if constexpr (op == PEERCHK) {
    asm volatile("" :: "v"(c.pf));
    await_tags(c.peer, c.rw, c.ml_ld, c.tag);
    const float m = __uint_as_float(c.peer.x);
    float M = fmaxf(m, dpp<XOR1>(m)); M = fmaxf(M, dpp<XOR2>(M));
    float L = __uint_as_float(c.peer.z) * __builtin_amdgcn_exp2f(m - M); L += dpp<XOR1>(L); L += dpp<XOR2>(L);
    c.scale = __builtin_amdgcn_exp2f(c.mw - M) / L;
  } else if constexpr (op == TRW) {
#pragma unroll
    for (int i = 0; i < 4; ++i) *(v4u*)&lds[L_T + w * 1024 + i * 256 + lane * 4] = c.ob[4 * a + i];
    COMPILER_FENCE();
  } else if constexpr (op == TRR) {
    const float* base = &lds[L_T + w * 1024 + (lane >> 4) * 256 + ((lane >> 2) & 3) * 32 + (lane & 3) * 2];
#pragma unroll
    for (int ct = 0; ct < 4; ++ct)
#pragma unroll
      for (int h = 0; h < 2; ++h) c.of[a][ct][h] = tr_read(base + h * 128 + ct * 8);
    COMPILER_FENCE();
  }
  __builtin_amdgcn_sched_barrier(0);
}
template <int B, int... I> __device__ __forceinline__ void run_seq(Ctx& c, std::integer_sequence<int, I...>) { (step<B + I>(c), ...); }
template <int B, int E> __device__ __forceinline__ void run(Ctx& c) { run_seq<B>(c, std::make_integer_sequence<int, E - B>{}); }

__global__ void __launch_bounds__(NTHR, 2)
mha_decode(const __bf16* __restrict__ xA, const __bf16* __restrict__ kA, const __bf16* __restrict__ vA,
           const __bf16* __restrict__ wqA, const __bf16* __restrict__ woA,
           const __bf16* __restrict__ xB, const __bf16* __restrict__ kB, const __bf16* __restrict__ vB,
           const __bf16* __restrict__ wqB, const __bf16* __restrict__ woB, __bf16* out, unsigned char* ws) {
  __shared__ __attribute__((aligned(16))) float lds[L_END];
  Ctx c;
  const unsigned long long tstart = __builtin_amdgcn_s_memrealtime();
  __builtin_amdgcn_sched_barrier(0);
  const int bid = blockIdx.x, rk = bid >> 3, bh = (bid & 7) * 4 + (rk >> 3), br = bh >> 4, head = bh & 15;
  const int b = (rk >> 2) & 1, split = rk & 3, grp = br * 2 + b, prod = head * 4 + split;
  const int tid = threadIdx.x, lane = tid & 63, w = __builtin_amdgcn_readfirstlane(tid >> 6);
  const int off7 = w < 7 ? 0 : OOB;
  c.w = w; c.lane = lane; c.lds = lds;
  c.t0 = (unsigned)tstart;
  c.vo_q = ((lane >> 3) * (NH * DH * 2) + (lane & 7) * 16) | off7;
  c.so_q = ((224 * b + 32 * w) * NH * DH + head * DH + split * 64) * 2;
  c.vo_kv = lane * 16;
  c.so_kv = (b * NH + head) * (SK * DH * 2) + (w << 12) + ((split & 1) << 18) + ((split >> 1) << 15) +
            ((((head >> 1) ^ (head >> 3)) & 1) << 17) + (b << 19);
  c.vo_o = ((lane >> 3) * (DM * 2) + w * 128 + (lane & 7) * 16) | off7;
  c.so_o = head * DH * DM * 2;
  c.orot = rk & 7;
  int ep_off = br * 256;
  asm volatile("" : "+s"(c.so_q), "+s"(c.so_kv), "+s"(c.so_o), "+s"(c.orot), "+s"(ep_off), "+v"(c.vo_q), "+v"(c.vo_kv), "+v"(c.vo_o)
               : "s"(tstart));
  __builtin_amdgcn_sched_barrier(0);
  c.rq = __builtin_amdgcn_make_buffer_rsrc((void*)(br ? wqB : wqA), 0, DM * NH * DH * 2, RSRC_W3);
  c.ro = __builtin_amdgcn_make_buffer_rsrc((void*)(br ? woB : woA), 0, NH * DH * DM * 2, RSRC_W3);
  c.rk = __builtin_amdgcn_make_buffer_rsrc((void*)(br ? kB : kA), 0, 2 * NH * SK * DH * 2, RSRC_W3);
  run<0, 1>(c);
  c.epoch = *(const CONST_AS unsigned*)(ws + ep_off);
  c.epa = *(const CONST_AS unsigned*)ws;
  c.cpf = *(const CONST_AS unsigned*)((__builtin_amdgcn_s_getpc() & ~63ull) + min(rk * NW + w, 243) * 64);
  run<1, N_FIRST>(c);
  int br2 = br;
  asm volatile("" : "+s"(br2));
  c.rx = __builtin_amdgcn_make_buffer_rsrc((void*)(br2 ? xB : xA), 0, 2 * DM * 2, RSRC_W3);
  c.vo_x = (((lane & 1) * DM + 224 * b + 32 * w + 8 * (lane >> 4)) * 2) | off7;
  c.rv = __builtin_amdgcn_make_buffer_rsrc((void*)(br2 ? vB : vA), 0, 2 * NH * SK * DH * 2, RSRC_W3);
  c.rw = __builtin_amdgcn_make_buffer_rsrc((void*)ws, 0, WS_END, RSRC_W3);
  const rsrc_t rout = __builtin_amdgcn_make_buffer_rsrc((void*)out, 0, 4 * DM * 2, RSRC_W3);
  c.q_st = (WS_Q + bh * 4096 + (lane & 1) * 2048 + b * 1024 + split * 256 + (lane & ~1) * 4) | (w == 0 ? 0 : OOB);
  c.q_ld = WS_Q + bh * 4096 + b * 2048 + lane * 16;
  c.ml_st = (WS_ML + (grp * NH + head) * 128 + split * 16) | (tid == 0 ? 0 : OOB);
  c.ml_ld = WS_ML + (grp * NH + head) * 128 + (lane & 3) * 16;
  c.t_st = (WS_T + ((bid >> 5) * 8 + (bid & 7)) * 8) | (tid == 64 * 3 && ((rk + (rk >> 2)) & 3) == 0 ? 0 : OOB);
  c.y_pf = (WS_PKT + (grp * 64 + prod) * PKT_BYTES + lane * 128) | (tid < PKT_BYTES / 128 ? 0 : OOB);

  run<N_FIRST, S.n - N_LAST>(c);

  const int pk = WS_PKT + grp * 64 * PKT_BYTES;
  const int dq = 32 * (lane >> 5) + 2 * (lane & 15) + ((lane >> 4) & 1);
  int a_wr = (L_RED + w * A_STR + ((dq - 8 * c.orot) & 63)) * 4, a_rd = (L_RED + (lane & 15) * A_STR + 4 * (lane >> 4)) * 4;
  int idx = (lane & 8) ? 0xEEEE : 0x4444;
  int y_off = (pk + prod * PKT_BYTES + tid * 4) | off7 | ((lane & 1) ? OOB : 0);
  int p_off = pk + (lane >> 2) * PKT_BYTES + prod * 64 + (lane & 3) * 16;
  int o_off = ((b * (2 * DM) + br * DM + prod * 16 + (lane & 3) * 4 + (lane >> 4)) * 2) | ((lane & 12) ? OOB : 0);
  int e_off = (prod == 0 && b == 0 && lane == 0) ? ep_off : OOB;
  asm volatile("" : "+v"(a_wr), "+v"(a_rd), "+v"(idx), "+v"(y_off), "+v"(p_off), "+v"(o_off), "+v"(e_off));
  unsigned due = read_origin(lds) + (N_K + N_V - 1) * TAU / 16 + END_AFTER;
  asm volatile("" : "+s"(due));
  f32x4 y[4];
#pragma unroll
  for (int ct = 0; ct < 4; ++ct) { y[ct] = f32x4{0.f, 0.f, 0.f, 0.f}; asm volatile("" : "+v"(y[ct])); }

  run<S.n - N_LAST, S.n>(c);
  LDS_AS unsigned char* const lb = (LDS_AS unsigned char*)lds;
  *(LDS_AS unsigned*)(lb + a_wr) = cvtpk(c.acc.x * c.scale, c.acc.y * c.scale);
  *(LDS_AS unsigned*)(lb + a_wr + 8 * A_STR * 4) = cvtpk(c.acc.z * c.scale, c.acc.w * c.scale);
  __syncthreads();
  v4u am[4];
#pragma unroll
  for (int ks = 0; ks < 4; ++ks) am[ks] = *(LDS_AS v4u*)(lb + a_rd + 64 * ks);
  __builtin_amdgcn_sched_barrier(0);
#pragma unroll
  for (int ks = 0; ks < 4; ++ks)
#pragma unroll
    for (int ct = 0; ct < 4; ++ct) {
      const v2u b0 = c.of[2 * ks][ct][0], b1 = c.of[2 * ks][ct][1], b2 = c.of[2 * ks + 1][ct][0], b3 = c.of[2 * ks + 1][ct][1];
      const v8u bm = {b0.x, b0.y, b1.x, b1.y, b2.x, b2.y, b3.x, b3.y};
      y[ct] = __builtin_amdgcn_smfmac_f32_16x16x64_bf16(__builtin_bit_cast(bf16x8, am[ks]), __builtin_bit_cast(bf16x16, bm), y[ct], idx, 0, 0);
    }
  float sm[4];
#pragma unroll
  for (int ct = 0; ct < 4; ++ct) sm[ct] = (y[ct].x + y[ct].y) + (y[ct].z + y[ct].w);
  const float yv = add32(add16(sm[0], sm[1]), add16(sm[2], sm[3]));
  __builtin_amdgcn_raw_buffer_store_b64(granule(yv, dpp<XOR1>(yv), c.tag), c.rw, y_off, 0, AUX_SC1);

  if (w == NW - 1 && prod < DM / 16) {
    v4u v[4], u[4];
    bool got[4] = {false, false, false, false}, done = false;
    asm volatile("1:\n\ts_memrealtime vcc\n\ts_waitcnt lgkmcnt(0)\n\ts_sub_u32 vcc_lo, %0, vcc_lo\n\ts_cmp_lt_u32 vcc_lo, 0x100\n\ts_cbranch_scc1 1b"
                 :: "s"(due) : "scc", "vcc", "memory");
#pragma unroll 1
    for (unsigned spins = 0; !done && spins < SPIN_CAP; ++spins) {
      COMPILER_FENCE();
#pragma unroll
      for (int i = 0; i < 4; ++i)
        if (!got[i]) v[i] = __builtin_amdgcn_raw_buffer_load_b128(c.rw, p_off, i * 16 * PKT_BYTES, AUX_SC1);
#pragma unroll
      for (int i = 0; i < 4; ++i) { u[i] = v[i]; got[i] = untag(u[i], c.tag); }
      done = got[0] & got[1] & got[2] & got[3];
    }
    const f32x4 r = (__builtin_bit_cast(f32x4, u[0]) + __builtin_bit_cast(f32x4, u[1])) +
                    (__builtin_bit_cast(f32x4, u[2]) + __builtin_bit_cast(f32x4, u[3]));
    float z = add16(add32(r.x, r.z), add32(r.y, r.w));
    z += dpp<ROR4>(z);
    z += dpp<ROR8>(z);
    const __bf16 zb = (__bf16)z;
    __builtin_amdgcn_raw_buffer_store_b16(done ? __builtin_bit_cast(unsigned short, zb) : (unsigned short)0x7fc0u, rout, o_off, 0, 0);
    __builtin_amdgcn_raw_buffer_store_b32(c.tag, c.rw, e_off, 0, AUX_SC1);
  }
}

}

extern "C" void kernel_launch(void* const* d_in, const int* in_sizes, int n_in, void* d_out, int out_size, void* d_ws,
                              size_t ws_size, hipStream_t stream) {
  (void)in_sizes; (void)n_in; (void)out_size; (void)ws_size;
  mha_decode<<<256, NTHR, 0, stream>>>((const __bf16*)d_in[0], (const __bf16*)d_in[1], (const __bf16*)d_in[2],
                                       (const __bf16*)d_in[3], (const __bf16*)d_in[4], (const __bf16*)d_in[5],
                                       (const __bf16*)d_in[6], (const __bf16*)d_in[7], (const __bf16*)d_in[8],
                                       (const __bf16*)d_in[9], (__bf16*)d_out, (unsigned char*)d_ws);
}
```

```cpp
#include <hip/hip_runtime.h>
#include <hip/hip_bf16.h>
#include <utility>

namespace {

typedef unsigned v2u __attribute__((ext_vector_type(2)));
typedef unsigned v4u __attribute__((ext_vector_type(4)));
typedef unsigned v8u __attribute__((ext_vector_type(8)));
typedef float f32x2 __attribute__((ext_vector_type(2)));
typedef float f32x4 __attribute__((ext_vector_type(4)));
typedef __bf16 bf16x2 __attribute__((ext_vector_type(2)));
typedef __bf16 bf16x8 __attribute__((ext_vector_type(8)));
typedef __bf16 bf16x16 __attribute__((ext_vector_type(16)));
typedef short i16x4 __attribute__((ext_vector_type(4)));
typedef __amdgpu_buffer_rsrc_t rsrc_t;
#define LDS_AS __attribute__((address_space(3)))
#define CONST_AS __attribute__((address_space(4)))
#define COMPILER_FENCE() asm volatile("" ::: "memory")

constexpr int DM = 448, NH = 16, SK = 4096, DH = 256;
constexpr int NW = 8, NTHR = NW * 64;
constexpr int N_Q = 4, N_K = 64, N_V = 64, N_O = 32;
constexpr unsigned SPIN_CAP = 1u << 20;
constexpr int RSRC_W3 = 0x00020000, AUX_NT = 2, AUX_SC1 = 16, OOB = 0x40000000;
constexpr int WS_PKT = 4096, PKT_BYTES = DM * 4, WS_Q = WS_PKT + 256 * PKT_BYTES, WS_ML = WS_Q + 64 * 2048,
              WS_T = WS_ML + 64 * 128, WS_END = WS_T + 2048;
constexpr int A_STR = 68;
constexpr int L_RED = 0, L_T = L_RED + 16 * A_STR, L_TQ = L_T + NW * 1024, L_ML = L_TQ + NW * 1024, L_P = L_ML + 2 * NW,
              L_F = L_P + NW * 64, L_R = L_F + NW * 2, L_END = L_R + 4;

constexpr int WIN = 3;
constexpr int O_FIRST = 4;
constexpr int O_START = 16, O_EVERY = 3;
constexpr int X_AT = 2, QMM_AT = 6, QPUB_AT = 9, QLD_AT = 17, QCHK_AT = 21, T_AT[4] = {10, 38, 84, 100}, YPF_AT = 100;
constexpr int N_FIRST = N_Q + O_FIRST + (0 ? 0 : 1), N_LAST = 3;
constexpr int TAU = 433, ORIGIN0 = 255, MARGIN = 14, BAND = 58, GAIN_UP = 9, GAIN_DOWN = 4, LAG_FROM = 7, LAG_EVERY = 4,
              SLIP = 40, SLIP_MIN = 25, END_AFTER = 126, GATE_FROM = 24, GATE_AHEAD = 21, STAGGER = 24;

constexpr int due_at(int j) { return (j <= 82 ? j * TAU : 82 * TAU + (j - 82) * 433) / 16; }
enum Op : int { IQ, IK, IV, IO, IX, QPUB, QLD, MLPUB, VTCH, YPF, TPUB, TLD, PEERLD,
                WAITV, WAITB, WAITS, GATE, GATE0, LAG, TCHK, SETUP, QTR, QMM, QCHK, CK, SM1, SM2, CV, PEERCHK, TRW, TRR };
struct Sched { int n; int op[900]; int a[900]; };
constexpr Sched make_sched() {
  Sched s{};
  int vmk[256] = {}, vmi[256] = {}, nvm = 0;
  auto emit = [&](int op, int a) {
    s.op[s.n] = op; s.a[s.n] = a; ++s.n;
    if (op <= PEERLD) { vmk[nvm] = op; vmi[nvm] = a; ++nvm; }
  };
  int no = 0, nk = 0, nv = 0, nw = 0, nr = 0, sm = 0;
  int lk = 0, lv = 0, lo = 0;
  for (int j = 0; j < N_Q; ++j) emit(IQ, j);
  while (no < O_FIRST) emit(IO, no++);
  for (int j = 0; j < N_K + N_V; ++j) {
    if (j == 0 && 0) emit(SETUP, 0);
    if (j == 0 && 0) emit(GATE0, 0);
    if (j > 0 || 0 == 2) {
      int p = nvm, seen = 0;
      while (seen < WIN) { --p; if (vmk[p] <= IV || (0 && vmk[p] == IO && vmi[p] < O_FIRST)) ++seen; }
      if (j > LAG_FROM) {
        const int k = (j - LAG_FROM - 1) % LAG_EVERY + 1;
        int p2 = p, p0 = p;
        do --p2; while (vmk[p2] > IV);
        do ++p0; while (vmk[p0] > IV);
        emit(WAITV, nvm - 1 - p2);
        emit(WAITB, (nvm - 1 - p) | (k << 8));
        if (j > GATE_FROM) emit(GATE, due_at(j)); else emit(WAITS, (nvm - 1 - p0) | (k << 8));
        p = p2;
      } else emit(WAITV, nvm - 1 - p);
      if (vmk[p] == IK) lk = vmi[p] + 1;
      if (vmk[p] == IV) { lk = N_K; lv = vmi[p] + 1; }
      for (int t = 0; t < p; ++t) if (vmk[t] == IO) lo = vmi[t] + 1;
    }
    if (j < N_K) emit(IK, j); else emit(IV, j - N_K);
    if (j == 0 && !0) emit(SETUP, 0);
    if (j >= O_START && (j - O_START) % O_EVERY == 0 && no < N_O) emit(IO, no++);
    if (j >= LAG_FROM && (j - LAG_FROM) % LAG_EVERY == 0 && j + 1 < N_K + N_V) emit(LAG, j);
    if (j == X_AT) emit(IX, 0);
    if (j == YPF_AT) emit(YPF, 0);
    if (1 == 2 && j == 12) emit(VTCH, 0);
    for (int t = 0; t < 3; ++t) {
      if (j == T_AT[t]) emit(TPUB, t);
      if (j == T_AT[t] + 8) emit(TLD, t);
      if (j == T_AT[t] + 8 + 4) emit(TCHK, t);
    }
    if (j == QMM_AT - 1) emit(QTR, 0);
    if (j == QMM_AT) emit(QMM, 0);
    if (j == QPUB_AT) emit(QPUB, 0);
    if (j == QLD_AT) { emit(QLD, 0); emit(QLD, 1); }
    if (j == QCHK_AT) emit(QCHK, 0);
    else if (j > QCHK_AT && nk < N_K) { for (int t = 0; t < 3 && nk < lk; ++t) emit(CK, nk++); }
    else if (nk == N_K && sm < 2) { emit(sm == 0 ? SM1 : SM2, 0); ++sm; }
    else if (sm == 2) {
      for (int t = 0; t < 3 && nv < lv; ++t) {
        emit(CV, nv++);
        if (nv == 12) emit(MLPUB, 0);
        if (nv == N_V - 20) emit(PEERLD, 0);
        if (nv == N_V - 10) emit(PEERCHK, 0);
      }
    }
    if (nr < nw) emit(TRR, nr++);
    else if (4 * nw + 4 <= lo) emit(TRW, nw++);
  }
  if (no != N_O || nr != N_O / 4 || nv < N_V - 10 || nv > N_V - N_LAST) s.n = -1;
  else while (nv < N_V) emit(CV, nv++);
  return s;
}
constexpr Sched S = make_sched();
static_assert(S.n > 0, "the schedule must finish everything but the last V loads inside the stream");

constexpr int pair_off(int g) { return ((g & 1) << 12) | ((g & 2) << 14) | ((g & 12) << 15) | ((g & 16) << 16); }

__device__ __forceinline__ float dot2(unsigned a, unsigned b, float c) {
  return __builtin_amdgcn_fdot2_f32_bf16(__builtin_bit_cast(bf16x2, a), __builtin_bit_cast(bf16x2, b), c, false);
}
__device__ __forceinline__ unsigned cvtpk(float lo, float hi) {
  const bf16x2 r = {(__bf16)lo, (__bf16)hi};
  return __builtin_bit_cast(unsigned, r);
}
template <int CTRL> __device__ __forceinline__ float dpp(float x) {
  return __builtin_bit_cast(float, __builtin_amdgcn_mov_dpp(__builtin_bit_cast(int, x), CTRL, 0xf, 0xf, true));
}
constexpr int XOR1 = 0xB1, XOR2 = 0x4E, XOR7 = 0x141, XOR15 = 0x140, ROR4 = 0x124, ROR8 = 0x128;
__device__ __forceinline__ float sum8(float x) { x += dpp<XOR1>(x); x += dpp<XOR2>(x); x += dpp<XOR7>(x); return x; }
__device__ __forceinline__ float max8(float x) {
  x = fmaxf(x, dpp<XOR1>(x)); x = fmaxf(x, dpp<XOR2>(x)); x = fmaxf(x, dpp<XOR7>(x));
  return x;
}
__device__ __forceinline__ float add16(float a, float b) {
  const auto s = __builtin_amdgcn_permlane16_swap(__float_as_uint(a), __float_as_uint(b), false, false);
  return __uint_as_float(s[0]) + __uint_as_float(s[1]);
}
__device__ __forceinline__ float add32(float a, float b) {
  const auto s = __builtin_amdgcn_permlane32_swap(__float_as_uint(a), __float_as_uint(b), false, false);
  return __uint_as_float(s[0]) + __uint_as_float(s[1]);
}
template <int CTRL> __device__ __forceinline__ float tstep(float lo, float hi, bool sel) {
  const float a = lo + dpp<CTRL>(lo), b = hi + dpp<CTRL>(hi);
  return sel ? b : a;
}
__device__ __forceinline__ float wave_sum(float x) {
  x = sum8(x); x += dpp<XOR15>(x); x = add16(x, x);
  return add32(x, x);
}
__device__ __forceinline__ float wave_max(float x) {
  x = max8(x); x = fmaxf(x, dpp<XOR15>(x));
  const auto s = __builtin_amdgcn_permlane16_swap(__float_as_uint(x), __float_as_uint(x), false, false);
  x = fmaxf(__uint_as_float(s[0]), __uint_as_float(s[1]));
  const auto t = __builtin_amdgcn_permlane32_swap(__float_as_uint(x), __float_as_uint(x), false, false);
  return fmaxf(__uint_as_float(t[0]), __uint_as_float(t[1]));
}
__device__ __forceinline__ f32x4 mfma(v4u a, v4u b, f32x4 c) {
  return __builtin_amdgcn_mfma_f32_16x16x32_bf16(__builtin_bit_cast(bf16x8, a), __builtin_bit_cast(bf16x8, b), c, 0, 0, 0);
}
__device__ __forceinline__ v2u tr_read(const float* p) {
  return __builtin_bit_cast(v2u, __builtin_amdgcn_ds_read_tr16_b64_v4i16((LDS_AS i16x4*)p));
}
__device__ __forceinline__ void await_tags(v4u& g, rsrc_t r, int off, unsigned tag) {
  if (__builtin_expect(!__all((g.y == tag) & (g.w == tag)), 0)) {
    unsigned spins = 0;
    bool ok;
#pragma unroll 1
    do {
      COMPILER_FENCE();
      g = __builtin_amdgcn_raw_buffer_load_b128(r, off, 0, AUX_SC1);
      ok = __all((g.y == tag) & (g.w == tag));
    } while (!ok && ++spins < SPIN_CAP);
    if (!ok) g.x = 0x7fc00000u;
  }
}
__device__ __forceinline__ v2u granule(float a, float b, unsigned tag) {
  return v2u{(__float_as_uint(a) & ~255u) | (tag & 255u), (__float_as_uint(b) & ~255u) | ((tag >> 8) & 255u)};
}
__device__ __forceinline__ bool untag(v4u& g, unsigned tag) {
  const unsigned t0 = tag & 255u, t1 = (tag >> 8) & 255u;
  g ^= v4u{t0, t1, t0, t1};
  return __all(((g.x | g.y | g.z | g.w) & 255u) == 0u);
}
__device__ __forceinline__ f32x4 await16(v4u g, rsrc_t r, int off, unsigned tag) {
  if (__builtin_expect(!untag(g, tag), 0)) {
    unsigned spins = 0;
    bool ok;
#pragma unroll 1
    do {
      COMPILER_FENCE();
      g = __builtin_amdgcn_raw_buffer_load_b128(r, off, 0, AUX_SC1);
      ok = untag(g, tag);
    } while (!ok && ++spins < SPIN_CAP);
    if (!ok) g.x = 0x7fc00000u;
  }
  return __builtin_bit_cast(f32x4, g);
}
__device__ __forceinline__ void flag_set(float* lds, int k, int w) {
  asm volatile("s_waitcnt lgkmcnt(0)" ::: "memory");
  ((LDS_AS volatile unsigned*)lds)[L_F + k * NW + w] = 1u;
}
__device__ __forceinline__ void flag_wait(float* lds, int k, int lane) {
  while (!__all(((LDS_AS volatile unsigned*)lds)[L_F + k * NW + (lane & 7)] != 0u)) {}
  COMPILER_FENCE();
}
__device__ __forceinline__ void wait_until(unsigned t) {
  asm volatile("1:\n\ts_memrealtime vcc\n\ts_waitcnt lgkmcnt(0)\n\ts_sub_u32 vcc_lo, %0, vcc_lo\n\ts_cmp_lt_u32 vcc_lo, 0x100\n\ts_cbranch_scc1 1b"
               :: "s"(t) : "scc", "vcc", "memory");
}
__device__ __forceinline__ unsigned origin(float* lds, int j, unsigned none) {
  v2u v = *(LDS_AS volatile v2u*)&lds[L_R];
  asm volatile("" : "+v"(v));
  const unsigned org = __builtin_amdgcn_readfirstlane(v.x), x = __builtin_amdgcn_readfirstlane(v.y);
  return org ? org + ((min(j, (int)(x >> 8) & 255) - (int)(x & 255u)) * (int)(x >> 16) >> 4) : none;
}

struct Ctx {
  rsrc_t rq, rk, rv, ro, rx, rw;
  float* lds;
  int w, lane, vo_q, vo_kv, vo_o, vo_x, so_q, so_kv, so_o, orot, q_st, q_ld, ml_st, ml_ld, y_pf, boost, slow, t_st;
  unsigned tc, tagt, epa, org, gate;
  v2u tg;
  const CONST_AS unsigned *pv, *po;
  unsigned t0, epoch, tag, pf, cpf[2], sel[4];
  v4u qb[N_Q], kb[N_K], vb[N_V], ob[N_O], xv, qg[2], qr, pp, peer;
  v2u qf[4][2];
  v2u of[8][4][2];
  f32x4 acc;
  float xs[5], s0, s1, mw, scale;
};

template <int I> __device__ __forceinline__ void step(Ctx& c) {
  constexpr int op = S.op[I], a = S.a[I];
  float* const lds = c.lds;
  const int w = c.w, lane = c.lane;
  if constexpr (op == IQ) {
    c.qb[a] = __builtin_amdgcn_raw_buffer_load_b128(c.rq, c.vo_q, c.so_q + 8 * a * (NH * DH * 2), AUX_NT);
  } else if constexpr (op == IK) {
    c.kb[a] = __builtin_amdgcn_raw_buffer_load_b128(c.rk, c.vo_kv + (a & 1) * 1024, c.so_kv ^ pair_off(a >> 1), AUX_NT);
  } else if constexpr (op == IV) {
    c.vb[a] = __builtin_amdgcn_raw_buffer_load_b128(c.rv, c.vo_kv + (a & 1) * 1024, c.so_kv ^ pair_off(a >> 1), AUX_NT);
  } else if constexpr (op == IO) {
    c.ob[a] = __builtin_amdgcn_raw_buffer_load_b128(c.ro, c.vo_o, c.so_o + ((8 * a + 32 * c.orot) & 255) * (DM * 2), 0);
  } else if constexpr (op == IX) {
    c.xv = __builtin_amdgcn_raw_buffer_load_b128(c.rx, c.vo_x, 0, 0);
  } else if constexpr (op == WAITV) {
    __builtin_amdgcn_s_waitcnt((a & 15) | ((a >> 4) << 14) | 0x0F70);
  } else if constexpr (op == LAG) {
    const unsigned o = origin(lds, a + 2, c.t0 + ORIGIN0);
    const int late = (int)((unsigned)__builtin_amdgcn_s_memrealtime() - o) - due_at(a);
    c.gate = o - GATE_AHEAD + (w * STAGGER >> 3);
    c.boost = late * GAIN_UP >> 4;
    c.slow = (-late - BAND) * GAIN_DOWN >> 4;
  } else if constexpr (op == TPUB) {
    c.tc = (unsigned)__builtin_amdgcn_s_memrealtime();
    __builtin_amdgcn_raw_buffer_store_b64(v2u{c.tc, c.tagt}, c.rw, c.t_st, a * 512, AUX_SC1);
  } else if constexpr (op == TLD) {
    c.tg = __builtin_amdgcn_raw_buffer_load_b64(c.rw, (WS_T + lane * 8) | (w == NW - 1 ? 0 : OOB), a * 512, AUX_SC1);
  } else if constexpr (op == TCHK) {
    const bool ok = c.tg.y == c.tagt;
    const float d = wave_sum(ok ? (float)(int)(c.tg.x - c.tc) : (float)220), n = wave_sum(ok ? 1.f : 0.f);
    unsigned org = (c.tc + (int)(220 ? d * (1.f / 64) : d * __builtin_amdgcn_rcpf(fmaxf(n, 1.f))) - due_at(T_AT[a]) + MARGIN) | 1u;
    bool use = w == NW - 1 && n > 16.f;
    int ex = 0;
    if constexpr (a > 0) {
      const int drift = (int)(org - origin(lds, T_AT[a], org));
      use = use && drift > 25;
      ex = drift > (a == 1 ? 42 : 40) ? 40 : 0;
      org += -12;
    }
    *(LDS_AS volatile v2u*)&lds[L_R + (use ? 0 : 2)] = v2u{org | 1u, (unsigned)(T_AT[a] | ((T_AT[a] < N_K - 8 ? N_K + 2 : N_K + N_V) << 8) | (ex << 16))};
  } else if constexpr (op == GATE0) {
    wait_until(c.t0 + a);
  } else if constexpr (op == GATE) {
    wait_until(c.gate + a);
  } else if constexpr (op == WAITS) {
    asm volatile("s_cmp_lt_i32 %0, %1\n\ts_cbranch_scc1 1f\n\ts_waitcnt vmcnt(%2)\n1:"
                 :: "s"(c.slow), "i"(a >> 8), "i"(a & 255) : "scc", "memory");
  } else if constexpr (op == WAITB) {
    asm volatile("s_cmp_ge_i32 %0, %1\n\ts_cbranch_scc1 1f\n\ts_waitcnt vmcnt(%2)\n1:"
                 :: "s"(c.boost), "i"(a >> 8), "i"(a & 255) : "scc", "memory");
  } else if constexpr (op == SETUP) {
    const int row = lane & 15, j = row & 7;
    const bool act = ((lane >> 4) & 1) == (row >> 3);
    const unsigned two = (lane >> 5) ? 0x0302u : 0x0100u;
    const unsigned s = (j & 1) ? ((two << 16) | 0x0c0cu) : (0x0c0c0000u | two);
#pragma unroll
    for (int i = 0; i < 4; ++i) c.sel[i] = (act && (j >> 1) == i) ? s : 0x0c0c0c0cu;
    c.acc = f32x4{0.f, 0.f, 0.f, 0.f};
    ((unsigned*)lds)[L_F + (lane & 1) * NW + w] = 0u;
    ((unsigned*)lds)[L_R + (lane & 1)] = 0u;
    asm volatile("" :: "s"(c.cpf[0]), "s"(c.cpf[1]));
    c.tagt = c.epa * 0x9E3779B1u + 0x7F4A7C15u;
    __syncthreads();
    if (1 == 1) { c.cpf[0] = *c.pv; c.cpf[1] = *c.po; }
  } else if constexpr (op == QTR) {
#pragma unroll
    for (int i = 0; i < 4; ++i) *(v4u*)&lds[L_TQ + w * 1024 + i * 256 + lane * 4] = c.qb[i];
    COMPILER_FENCE();
    const float* base = &lds[L_TQ + w * 1024 + (lane >> 4) * 256 + ((lane >> 2) & 3) * 32 + (lane & 3) * 2];
#pragma unroll
    for (int ct = 0; ct < 4; ++ct)
#pragma unroll
      for (int h = 0; h < 2; ++h) c.qf[ct][h] = tr_read(base + h * 128 + ct * 8);
    COMPILER_FENCE();
  } else if constexpr (op == QMM) {
    f32x4 d[4];
#pragma unroll
    for (int ct = 0; ct < 4; ++ct)
      d[ct] = mfma(c.xv, v4u{c.qf[ct][0].x, c.qf[ct][0].y, c.qf[ct][1].x, c.qf[ct][1].y}, f32x4{0.f, 0.f, 0.f, 0.f});
    c.tag = c.epoch * 0x9E3779B1u + 0x7F4A7C15u;
    if (1 == 1) asm volatile("" :: "s"(c.cpf[0]), "s"(c.cpf[1]));
    asm volatile("" : "+s"(c.tag));
    const f32x4 lo = (lane & 16) ? d[1] : d[0], hi = (lane & 16) ? d[3] : d[2];
    *(f32x2*)&lds[L_RED + w * 128 + lane * 2] = (lane & 32) ? f32x2{hi.x, hi.y} : f32x2{lo.x, lo.y};
    flag_set(lds, 0, w);
  } else if constexpr (op == QPUB) {
    flag_wait(lds, 0, lane);
    f32x2 t = {0.f, 0.f};
#pragma unroll
    for (int i = 0; i < 7; ++i) t += *(const f32x2*)&lds[L_RED + i * 128 + lane * 2];
    const float nx = dpp<XOR1>(t.x), ny = dpp<XOR1>(t.y);
    __builtin_amdgcn_raw_buffer_store_b64(granule((lane & 1) ? ny : t.x, (lane & 1) ? t.y : nx, c.tag), c.rw, c.q_st, 0, AUX_SC1);
  } else if constexpr (op == QLD) {
    c.qg[a] = __builtin_amdgcn_raw_buffer_load_b128(c.rw, c.q_ld + a * 1024, 0, AUX_SC1);
  } else if constexpr (op == QCHK) {
    if (1 == 2) asm volatile("" :: "v"(c.pf));
    const f32x4 q = await16(c.qg[0], c.rw, c.q_ld, c.tag) + await16(c.qg[1], c.rw, c.q_ld + 1024, c.tag);
    *(v2u*)&lds[L_TQ + w * 1024 + lane * 2] = v2u{cvtpk(q.x, q.y), cvtpk(q.z, q.w)};
    COMPILER_FENCE();
    c.qr = *(const v4u*)&lds[L_TQ + w * 1024 + (lane & 31) * 4];
  } else if constexpr (op == CK) {
    const v4u k = c.kb[a], q = c.qr;
    float x = dot2(k.x, q.x, dot2(k.z, q.z, 0.f)) + dot2(k.y, q.y, dot2(k.w, q.w, 0.f));
    if constexpr ((a & 1) == 1) x = add16(c.xs[0], x);
    if constexpr ((a & 3) == 3) x = tstep<ROR8>(c.xs[1], x, lane & 8);
    if constexpr ((a & 7) == 7) x = tstep<XOR7>(c.xs[2], x, lane & 4);
    if constexpr ((a & 15) == 15) x = tstep<XOR2>(c.xs[3], x, lane & 2);
    if constexpr ((a & 31) == 31) (a == 31 ? c.s0 : c.s1) = tstep<XOR1>(c.xs[4], x, lane & 1) * (0.0625f * 1.4426950408889634f);
    else c.xs[__builtin_ctz(~a)] = x;
  } else if constexpr (op == SM1) {
    const float m = wave_max(fmaxf(c.s0, c.s1));
    const float p0 = __builtin_amdgcn_exp2f(c.s0 - m), p1 = __builtin_amdgcn_exp2f(c.s1 - m);
    __bf16* pw = (__bf16*)&lds[L_P + w * 64] + (__builtin_bitreverse32(lane) >> 26);
    pw[0] = (__bf16)p0; pw[64] = (__bf16)p1;
    COMPILER_FENCE();
    c.mw = m; c.s0 = p0 + p1;
    c.pp = *(const v4u*)&lds[L_P + w * 64];
  } else if constexpr (op == SM2) {
    const float l = wave_sum(c.s0);
    lds[L_ML + 2 * w + (lane & 1)] = (lane & 1) ? l : c.mw;
    flag_set(lds, 1, w);
  } else if constexpr (op == MLPUB) {
    flag_wait(lds, 1, lane);
    const f32x2 ml = *(const f32x2*)&lds[L_ML + 2 * (lane & 7)];
    const float M = max8(ml.x), L = sum8(ml.y * __builtin_amdgcn_exp2f(ml.x - M));
    const v4u g = {__float_as_uint(M), c.tag, __float_as_uint(L), c.tag};
    __builtin_amdgcn_raw_buffer_store_b128(g, c.rw, c.ml_st, 0, AUX_SC1);
  } else if constexpr (op == CV) {
    const unsigned pr = c.pp[a & 3];
    const v4u am = {__builtin_amdgcn_perm(0u, pr, c.sel[0]), __builtin_amdgcn_perm(0u, pr, c.sel[1]),
                    __builtin_amdgcn_perm(0u, pr, c.sel[2]), __builtin_amdgcn_perm(0u, pr, c.sel[3])};
    c.acc = mfma(am, c.vb[a], c.acc);
    if constexpr ((a & 3) == 3 && a + 1 < N_V) c.pp = *(const v4u*)&lds[L_P + w * 64 + (a + 1)];
  } else if constexpr (op == VTCH) {
    c.pf = __builtin_amdgcn_raw_buffer_load_b32(c.rv, lane == 0 && w == 0 ? 0 : OOB, c.so_kv, AUX_NT);
  } else if constexpr (op == YPF) {
    c.pf = __builtin_amdgcn_raw_buffer_load_b32(c.rw, c.y_pf, 0, AUX_SC1);
  } else if constexpr (op == PEERLD) {
    c.peer = __builtin_amdgcn_raw_buffer_load_b128(c.rw, c.ml_ld, 0, AUX_SC1);
  } else if constexpr (op == PEERCHK) {
    asm volatile("" :: "v"(c.pf));
    await_tags(c.peer, c.rw, c.ml_ld, c.tag);
    const float m = __uint_as_float(c.peer.x);
    float M = fmaxf(m, dpp<XOR1>(m)); M = fmaxf(M, dpp<XOR2>(M));
    float L = __uint_as_float(c.peer.z) * __builtin_amdgcn_exp2f(m - M); L += dpp<XOR1>(L); L += dpp<XOR2>(L);
    c.scale = __builtin_amdgcn_exp2f(c.mw - M) / L;
  } else if constexpr (op == TRW) {
#pragma unroll
    for (int i = 0; i < 4; ++i) *(v4u*)&lds[L_T + w * 1024 + i * 256 + lane * 4] = c.ob[4 * a + i];
    COMPILER_FENCE();
  } else if constexpr (op == TRR) {
    const float* base = &lds[L_T + w * 1024 + (lane >> 4) * 256 + ((lane >> 2) & 3) * 32 + (lane & 3) * 2];
#pragma unroll
    for (int ct = 0; ct < 4; ++ct)
#pragma unroll
      for (int h = 0; h < 2; ++h) c.of[a][ct][h] = tr_read(base + h * 128 + ct * 8);
    COMPILER_FENCE();
  }
  __builtin_amdgcn_sched_barrier(0);
}
template <int B, int... I> __device__ __forceinline__ void run_seq(Ctx& c, std::integer_sequence<int, I...>) { (step<B + I>(c), ...); }
template <int B, int E> __device__ __forceinline__ void run(Ctx& c) { run_seq<B>(c, std::make_integer_sequence<int, E - B>{}); }

__global__ void __launch_bounds__(NTHR, 2)
mha_decode(const __bf16* __restrict__ xA, const __bf16* __restrict__ kA, const __bf16* __restrict__ vA,
           const __bf16* __restrict__ wqA, const __bf16* __restrict__ woA,
           const __bf16* __restrict__ xB, const __bf16* __restrict__ kB, const __bf16* __restrict__ vB,
           const __bf16* __restrict__ wqB, const __bf16* __restrict__ woB, __bf16* out, unsigned char* ws) {
  __shared__ __attribute__((aligned(16))) float lds[L_END];
  Ctx c;
  const unsigned long long tstart = __builtin_amdgcn_s_memrealtime();
  __builtin_amdgcn_sched_barrier(0);
  const int bid = blockIdx.x, rk = bid >> 3, bh = (bid & 7) * 4 + (rk >> 3), br = bh >> 4, head = bh & 15;
  const int b = (rk >> 2) & 1, split = rk & 3, grp = br * 2 + b, prod = head * 4 + split;
  const int tid = threadIdx.x, lane = tid & 63, w = __builtin_amdgcn_readfirstlane(tid >> 6);
  const int off7 = w < 7 ? 0 : OOB;
  c.w = w; c.lane = lane; c.lds = lds;
  c.t0 = (unsigned)tstart;
  c.vo_q = ((lane >> 3) * (NH * DH * 2) + (lane & 7) * 16) | off7;
  c.so_q = ((224 * b + 32 * w) * NH * DH + head * DH + split * 64) * 2;
  c.vo_kv = lane * 16;
  c.so_kv = (b * NH + head) * (SK * DH * 2) + (((w & 1) << 11) | ((w & 2) << 12) | ((w & 4) << 14) | ((split & 1) << 14) | ((split & 2) << 18)) +
            (((head & 2) ? 0x68000 : 0) ^ ((head & 8) << 12) ^ ((((head >> 2) ^ b) & 1) << 20));
  c.vo_o = ((lane >> 3) * (DM * 2) + w * 128 + (lane & 7) * 16) | off7;
  c.so_o = head * DH * DM * 2;
  c.orot = rk & 7;
  int ep_off = br * 256;
  asm volatile("" : "+s"(c.so_q), "+s"(c.so_kv), "+s"(c.so_o), "+s"(c.orot), "+s"(ep_off), "+v"(c.vo_q), "+v"(c.vo_kv), "+v"(c.vo_o)
               : "s"(tstart));
  __builtin_amdgcn_sched_barrier(0);
  c.rq = __builtin_amdgcn_make_buffer_rsrc((void*)(br ? wqB : wqA), 0, DM * NH * DH * 2, RSRC_W3);
  c.ro = __builtin_amdgcn_make_buffer_rsrc((void*)(br ? woB : woA), 0, NH * DH * DM * 2, RSRC_W3);
  c.rk = __builtin_amdgcn_make_buffer_rsrc((void*)(br ? kB : kA), 0, 2 * NH * SK * DH * 2, RSRC_W3);
  run<0, 1>(c);
  c.epoch = *(const CONST_AS unsigned*)(ws + ep_off);
  c.epa = *(const CONST_AS unsigned*)ws;
  const unsigned long long pc = __builtin_amdgcn_s_getpc() & ~63ull;
  c.cpf[0] = *(const CONST_AS unsigned*)(pc + (rk * NW + w) * 64);
  c.cpf[1] = *(const CONST_AS unsigned*)(pc + (256 + min(rk * NW + w, 121)) * 64);
  run<1, N_FIRST>(c);
  int br2 = br;
  asm volatile("" : "+s"(br2));
  c.rx = __builtin_amdgcn_make_buffer_rsrc((void*)(br2 ? xB : xA), 0, 2 * DM * 2, RSRC_W3);
  c.vo_x = (((lane & 1) * DM + 224 * b + 32 * w + 8 * (lane >> 4)) * 2) | off7;
  c.rv = __builtin_amdgcn_make_buffer_rsrc((void*)(br2 ? vB : vA), 0, 2 * NH * SK * DH * 2, RSRC_W3);
  c.pv = (const CONST_AS unsigned*)((br2 ? vB : vA) + (size_t)(b * NH + head) * (SK * DH));
  c.po = (const CONST_AS unsigned*)out;
  c.rw = __builtin_amdgcn_make_buffer_rsrc((void*)ws, 0, WS_END, RSRC_W3);
  const rsrc_t rout = __builtin_amdgcn_make_buffer_rsrc((void*)out, 0, 4 * DM * 2, RSRC_W3);
  c.q_st = (WS_Q + bh * 4096 + (lane & 1) * 2048 + b * 1024 + split * 256 + (lane & ~1) * 4) | (w == 0 ? 0 : OOB);
  c.q_ld = WS_Q + bh * 4096 + b * 2048 + lane * 16;
  c.ml_st = (WS_ML + (grp * NH + head) * 128 + split * 16) | (tid == 0 ? 0 : OOB);
  c.ml_ld = WS_ML + (grp * NH + head) * 128 + (lane & 3) * 16;
  c.t_st = (WS_T + ((bid >> 5) * 8 + (bid & 7)) * 8) | (tid == 64 * 3 && ((rk + (rk >> 2)) & 3) == 0 ? 0 : OOB);
  c.y_pf = (WS_PKT + (grp * 64 + prod) * PKT_BYTES + lane * 128) | (tid < PKT_BYTES / 128 ? 0 : OOB);

  run<N_FIRST, S.n - N_LAST>(c);

  const int pk = WS_PKT + grp * 64 * PKT_BYTES;
  const int dq = 32 * (lane >> 5) + 2 * (lane & 15) + ((lane >> 4) & 1);
  int a_wr = (L_RED + w * A_STR + ((dq - 8 * c.orot) & 63)) * 4, a_rd = (L_RED + (lane & 15) * A_STR + 4 * (lane >> 4)) * 4;
  int idx = (lane & 8) ? 0xEEEE : 0x4444;
  int y_off = (pk + prod * PKT_BYTES + tid * 4) | ((lane & 1) ? OOB : 0);
  int p_off = pk + (lane >> 2) * PKT_BYTES + prod * 64 + (lane & 3) * 16;
  int o_off = ((b * (2 * DM) + br * DM + prod * 16 + (lane & 3) * 4 + (lane >> 4)) * 2) | ((lane & 12) ? OOB : 0);
  int e_off = (prod == 0 && b == 0 && lane == 0) ? ep_off : OOB;
  asm volatile("" : "+v"(a_wr), "+v"(a_rd), "+v"(idx), "+v"(y_off), "+v"(p_off), "+v"(o_off), "+v"(e_off));
  unsigned due = origin(lds, N_K + N_V - 1, 0u) + due_at(N_K + N_V - 1) + END_AFTER;
  asm volatile("" : "+s"(due));
  f32x4 y[4];
#pragma unroll
  for (int ct = 0; ct < 4; ++ct) { y[ct] = f32x4{0.f, 0.f, 0.f, 0.f}; asm volatile("" : "+v"(y[ct])); }

  run<S.n - N_LAST, S.n>(c);
  LDS_AS unsigned char* const lb = (LDS_AS unsigned char*)lds;
  *(LDS_AS unsigned*)(lb + a_wr) = cvtpk(c.acc.x * c.scale, c.acc.y * c.scale);
  *(LDS_AS unsigned*)(lb + a_wr + 8 * A_STR * 4) = cvtpk(c.acc.z * c.scale, c.acc.w * c.scale);
  __syncthreads();
  if (__builtin_expect(w == NW - 1, 0)) {
    if (prod < DM / 16) {
      unsigned cnt;
      asm volatile(
          "s_mov_b32 %0, 0\n"
          "1:\n\t"
          "s_memrealtime vcc\n\t"
          "s_waitcnt lgkmcnt(0)\n\t"
          "s_sub_u32 vcc_lo, %9, vcc_lo\n\t"
          "s_cmp_lt_u32 vcc_lo, 0x100\n\t"
          "s_cbranch_scc1 1b\n"
          "2:\n\t"
          "buffer_load_dwordx4 v[0:3], %1, %4, 0 offen sc1\n\t"
          "buffer_load_dwordx4 v[4:7], %1, %4, %10 offen sc1\n\t"
          "buffer_load_dwordx4 v[8:11], %1, %4, %11 offen sc1\n\t"
          "buffer_load_dwordx4 v[12:15], %1, %4, %12 offen sc1\n\t"
          "s_add_u32 %0, %0, 1\n\t"
          "s_waitcnt vmcnt(3)\n\t"
          "v_xor_b32 v0, %6, v0\n\tv_xor_b32 v1, %7, v1\n\tv_xor_b32 v2, %6, v2\n\tv_xor_b32 v3, %7, v3\n\t"
          "v_or3_b32 v16, v0, v1, v2\n\t"
          "s_waitcnt vmcnt(2)\n\t"
          "v_xor_b32 v4, %6, v4\n\tv_xor_b32 v5, %7, v5\n\tv_xor_b32 v6, %6, v6\n\tv_xor_b32 v7, %7, v7\n\t"
          "v_or3_b32 v16, v16, v3, v4\n\tv_or3_b32 v17, v5, v6, v7\n\t"
          "v_pk_add_f32 v[0:1], v[0:1], v[4:5]\n\tv_pk_add_f32 v[2:3], v[2:3], v[6:7]\n\t"
          "s_waitcnt vmcnt(1)\n\t"
          "v_xor_b32 v8, %6, v8\n\tv_xor_b32 v9, %7, v9\n\tv_xor_b32 v10, %6, v10\n\tv_xor_b32 v11, %7, v11\n\t"
          "v_or3_b32 v16, v16, v8, v9\n\tv_or3_b32 v17, v17, v10, v11\n\t"
          "s_waitcnt vmcnt(0)\n\t"
          "v_xor_b32 v12, %6, v12\n\tv_xor_b32 v13, %7, v13\n\tv_xor_b32 v14, %6, v14\n\tv_xor_b32 v15, %7, v15\n\t"
          "v_or3_b32 v16, v16, v12, v13\n\tv_or3_b32 v17, v17, v14, v15\n\t"
          "v_pk_add_f32 v[8:9], v[8:9], v[12:13]\n\tv_pk_add_f32 v[10:11], v[10:11], v[14:15]\n\t"
          "v_or_b32 v16, v16, v17\n\t"
          "v_pk_add_f32 v[0:1], v[0:1], v[8:9]\n\tv_pk_add_f32 v[2:3], v[2:3], v[10:11]\n\t"
          "v_and_b32 v16, 0xff, v16\n\t"
          "v_cmp_ne_u32 vcc, 0, v16\n\t"
          "v_permlane32_swap_b32 v0, v2\n\tv_permlane32_swap_b32 v1, v3\n\t"
          "s_nop 0\n\t"
          "v_add_f32 v0, v0, v2\n\tv_add_f32 v1, v1, v3\n\t"
          "s_nop 1\n\t"
          "v_permlane16_swap_b32 v0, v1\n\t"
          "s_nop 0\n\t"
          "v_add_f32 v0, v0, v1\n\t"
          "s_nop 1\n\t"
          "v_add_f32_dpp v0, v0, v0 row_ror:4 row_mask:0xf bank_mask:0xf\n\t"
          "s_nop 1\n\t"
          "v_add_f32_dpp v0, v0, v0 row_ror:8 row_mask:0xf bank_mask:0xf\n\t"
          "s_cmp_lt_u32 %0, %13\n\t"
          "v_cvt_pk_bf16_f32 v0, v0, v0\n\t"
          "s_cbranch_vccz 4f\n\t"
          "s_cbranch_scc1 2b\n\t"
          "v_mov_b32 v0, 0x7fc0\n"
          "4:\n\t"
          "buffer_store_short v0, %2, %5, 0 offen\n\t"
          "v_mov_b32 v1, %8\n\t"
          "buffer_store_dword v1, %3, %4, 0 offen sc1"
          : "=&s"(cnt)
          : "v"(p_off), "v"(o_off), "v"(e_off), "s"(c.rw), "s"(rout), "s"(c.tag & 255u), "s"((c.tag >> 8) & 255u), "s"(c.tag), "s"(due),
            "s"(16 * PKT_BYTES), "s"(32 * PKT_BYTES), "s"(48 * PKT_BYTES), "s"(SPIN_CAP)
          : "scc", "vcc", "memory", "v0", "v1", "v2", "v3", "v4", "v5", "v6", "v7", "v8", "v9", "v10", "v11", "v12", "v13", "v14", "v15", "v16", "v17");
    }
    __builtin_amdgcn_endpgm();
  }
  v4u am[4];
#pragma unroll
  for (int ks = 0; ks < 4; ++ks) am[ks] = *(LDS_AS v4u*)(lb + a_rd + 64 * ks);
  __builtin_amdgcn_sched_barrier(0);
#pragma unroll
  for (int ks = 0; ks < 4; ++ks)
#pragma unroll
    for (int ct = 0; ct < 4; ++ct) {
      const v2u b0 = c.of[2 * ks][ct][0], b1 = c.of[2 * ks][ct][1], b2 = c.of[2 * ks + 1][ct][0], b3 = c.of[2 * ks + 1][ct][1];
      const v8u bm = {b0.x, b0.y, b1.x, b1.y, b2.x, b2.y, b3.x, b3.y};
      y[ct] = __builtin_amdgcn_smfmac_f32_16x16x64_bf16(__builtin_bit_cast(bf16x8, am[ks]), __builtin_bit_cast(bf16x16, bm), y[ct], idx, 0, 0);
    }
  float sm[4];
#pragma unroll
  for (int ct = 0; ct < 4; ++ct) sm[ct] = (y[ct].x + y[ct].y) + (y[ct].z + y[ct].w);
  const float yv = add32(add16(sm[0], sm[1]), add16(sm[2], sm[3]));
  __builtin_amdgcn_raw_buffer_store_b64(granule(yv, dpp<XOR1>(yv), c.tag), c.rw, y_off, 0, AUX_SC1);
}

}

extern "C" void kernel_launch(void* const* d_in, const int* in_sizes, int n_in, void* d_out, int out_size, void* d_ws,
                              size_t ws_size, hipStream_t stream) {
  (void)in_sizes; (void)n_in; (void)out_size; (void)ws_size;
  mha_decode<<<256, NTHR, 0, stream>>>((const __bf16*)d_in[0], (const __bf16*)d_in[1], (const __bf16*)d_in[2],
                                       (const __bf16*)d_in[3], (const __bf16*)d_in[4], (const __bf16*)d_in[5],
                                       (const __bf16*)d_in[6], (const __bf16*)d_in[7], (const __bf16*)d_in[8],
                                       (const __bf16*)d_in[9], (__bf16*)d_out, (unsigned char*)d_ws);
}
```

```cpp
#include <hip/hip_runtime.h>
#include <hip/hip_bf16.h>
#include <utility>

namespace {

typedef unsigned v2u __attribute__((ext_vector_type(2)));
typedef unsigned v4u __attribute__((ext_vector_type(4)));
typedef unsigned v8u __attribute__((ext_vector_type(8)));
typedef float f32x2 __attribute__((ext_vector_type(2)));
typedef float f32x4 __attribute__((ext_vector_type(4)));
typedef __bf16 bf16x2 __attribute__((ext_vector_type(2)));
typedef __bf16 bf16x8 __attribute__((ext_vector_type(8)));
typedef __bf16 bf16x16 __attribute__((ext_vector_type(16)));
typedef short i16x4 __attribute__((ext_vector_type(4)));
typedef __amdgpu_buffer_rsrc_t rsrc_t;
#define LDS_AS __attribute__((address_space(3)))
#define CONST_AS __attribute__((address_space(4)))
#define COMPILER_FENCE() asm volatile("" ::: "memory")

constexpr int DM = 448, NH = 16, SK = 4096, DH = 256;
constexpr int NW = 8, NTHR = NW * 64;
constexpr int N_Q = 4, N_K = 64, N_V = 64, N_O = 32;
constexpr unsigned SPIN_CAP = 1u << 20;
constexpr int RSRC_W3 = 0x00020000, AUX_NT = 2, AUX_SC1 = 16, OOB = 0x40000000;
constexpr int WS_PKT = 4096, PKT_BYTES = DM * 4, WS_Q = WS_PKT + 256 * PKT_BYTES, WS_ML = WS_Q + 64 * 2048,
              WS_T = WS_ML + 64 * 128, WS_END = WS_T + 3 * 512;
constexpr int A_STR = 68;
constexpr int L_RED = 0, L_T = L_RED + 16 * A_STR, L_TQ = L_T + NW * 1024, L_ML = L_TQ + NW * 1024, L_P = L_ML + 2 * NW,
              L_F = L_P + NW * 64, L_R = L_F + NW * 2, L_END = L_R + 4;

constexpr int WIN = 3;
constexpr int O_FIRST = 4;
constexpr int O_START = 16, O_EVERY = 3;
constexpr int X_AT = 2, QMM_AT = 6, QPUB_AT = 9, QLD_AT = 17, QCHK_AT = 21, YPF_AT = 100;
constexpr int N_FIRST = N_Q + O_FIRST + 1, N_LAST = 3;
constexpr int TAU = 432, ORIGIN0 = 255, MARGIN = 14, GATE_FROM = 23, GATE_AHEAD = 21, STAGGER = 24, END_AFTER = 122;
constexpr int LAG_FROM = 7, LAG_EVERY = 4, GAIN_UP = 9, BAND = 58, GAIN_DOWN = 4;
constexpr int T_AT[3] = {10, 38, 84}, T_DELAY = 6, T_KEEP[3] = {0, 28, 24}, T_OVER = 12, T_SLOW[3] = {0, 42, 40},
              T_EXTRA[3] = {0, 18, 40}, T_PACE = 40;
constexpr int due_at(int j) { return j * TAU / 16; }

enum Op : int { IQ, IK, IV, IO, IX, QPUB, QLD, MLPUB, YPF, TPUB, TLD, PEERLD,
                WAITV, WAITB, WAITS, GATE, LAG, TCHK, SETUP, QTR, QMM, QCHK, CK, SM1, SM2, CV, PEERCHK, TRW, TRR };
struct Sched { int n; int op[900]; int a[900]; };
constexpr Sched make_sched() {
  Sched s{};
  int vmk[256] = {}, vmi[256] = {}, nvm = 0;
  auto emit = [&](int op, int a) {
    s.op[s.n] = op; s.a[s.n] = a; ++s.n;
    if (op <= PEERLD) { vmk[nvm] = op; vmi[nvm] = a; ++nvm; }
  };
  int no = 0, nk = 0, nv = 0, nw = 0, nr = 0, sm = 0;
  int lk = 0, lv = 0, lo = 0;
  for (int j = 0; j < N_Q; ++j) emit(IQ, j);
  while (no < O_FIRST) emit(IO, no++);
  for (int j = 0; j < N_K + N_V; ++j) {
    if (j > 0) {
      int p = nvm, seen = 0;
      while (seen < WIN) { --p; if (vmk[p] <= IV) ++seen; }
      if (j > LAG_FROM) {
        const int k = (j - LAG_FROM - 1) % LAG_EVERY + 1;
        int p2 = p, p0 = p;
        do --p2; while (vmk[p2] > IV);
        do ++p0; while (vmk[p0] > IV);
        emit(WAITV, nvm - 1 - p2);
        emit(WAITB, (nvm - 1 - p) | (k << 8));
        if (j > GATE_FROM) emit(GATE, due_at(j)); else emit(WAITS, (nvm - 1 - p0) | (k << 8));
        p = p2;
      } else emit(WAITV, nvm - 1 - p);
      if (vmk[p] == IK) lk = vmi[p] + 1;
      if (vmk[p] == IV) { lk = N_K; lv = vmi[p] + 1; }
      for (int t = 0; t < p; ++t) if (vmk[t] == IO) lo = vmi[t] + 1;
    }
    if (j < N_K) emit(IK, j); else emit(IV, j - N_K);
    if (j == 0) emit(SETUP, 0);
    if (j >= O_START && (j - O_START) % O_EVERY == 0 && no < N_O) emit(IO, no++);
    if (j >= LAG_FROM && (j - LAG_FROM) % LAG_EVERY == 0 && j + 1 < N_K + N_V) emit(LAG, j);
    if (j == X_AT) emit(IX, 0);
    if (j == YPF_AT) emit(YPF, 0);
    for (int t = 0; t < 3; ++t) {
      if (j == T_AT[t]) emit(TPUB, t);
      if (j == T_AT[t] + T_DELAY) emit(TLD, t);
      if (j == T_AT[t] + T_DELAY + 4) emit(TCHK, t);
    }
    if (j == QMM_AT - 1) emit(QTR, 0);
    if (j == QMM_AT) emit(QMM, 0);
    if (j == QPUB_AT) emit(QPUB, 0);
    if (j == QLD_AT) { emit(QLD, 0); emit(QLD, 1); }
    if (j == QCHK_AT) emit(QCHK, 0);
    else if (j > QCHK_AT && nk < N_K) { for (int t = 0; t < 3 && nk < lk; ++t) emit(CK, nk++); }
    else if (nk == N_K && sm < 2) { emit(sm == 0 ? SM1 : SM2, 0); ++sm; }
    else if (sm == 2) {
      for (int t = 0; t < 3 && nv < lv; ++t) {
        emit(CV, nv++);
        if (nv == 12) emit(MLPUB, 0);
        if (nv == N_V - 20) emit(PEERLD, 0);
        if (nv == N_V - 10) emit(PEERCHK, 0);
      }
    }
    if (nr < nw) emit(TRR, nr++);
    else if (4 * nw + 4 <= lo) emit(TRW, nw++);
  }
  if (no != N_O || nr != N_O / 4 || nv < N_V - 10 || nv > N_V - N_LAST) s.n = -1;
  else while (nv < N_V) emit(CV, nv++);
  return s;
}
constexpr Sched S = make_sched();
static_assert(S.n > 0, "the schedule must finish everything but the last V loads inside the stream");

constexpr int pair_off(int g) { return ((g & 1) << 12) | ((g & 2) << 14) | ((g & 12) << 15) | ((g & 16) << 16); }

__device__ __forceinline__ float dot2(unsigned a, unsigned b, float c) {
  return __builtin_amdgcn_fdot2_f32_bf16(__builtin_bit_cast(bf16x2, a), __builtin_bit_cast(bf16x2, b), c, false);
}
__device__ __forceinline__ unsigned cvtpk(float lo, float hi) {
  const bf16x2 r = {(__bf16)lo, (__bf16)hi};
  return __builtin_bit_cast(unsigned, r);
}
template <int CTRL> __device__ __forceinline__ float dpp(float x) {
  return __builtin_bit_cast(float, __builtin_amdgcn_mov_dpp(__builtin_bit_cast(int, x), CTRL, 0xf, 0xf, true));
}
constexpr int XOR1 = 0xB1, XOR2 = 0x4E, XOR7 = 0x141, XOR15 = 0x140, ROR4 = 0x124, ROR8 = 0x128;
__device__ __forceinline__ float sum8(float x) { x += dpp<XOR1>(x); x += dpp<XOR2>(x); x += dpp<XOR7>(x); return x; }
__device__ __forceinline__ float max8(float x) {
  x = fmaxf(x, dpp<XOR1>(x)); x = fmaxf(x, dpp<XOR2>(x)); x = fmaxf(x, dpp<XOR7>(x));
  return x;
}
__device__ __forceinline__ float add16(float a, float b) {
  const auto s = __builtin_amdgcn_permlane16_swap(__float_as_uint(a), __float_as_uint(b), false, false);
  return __uint_as_float(s[0]) + __uint_as_float(s[1]);
}
__device__ __forceinline__ float add32(float a, float b) {
  const auto s = __builtin_amdgcn_permlane32_swap(__float_as_uint(a), __float_as_uint(b), false, false);
  return __uint_as_float(s[0]) + __uint_as_float(s[1]);
}
template <int CTRL> __device__ __forceinline__ float tstep(float lo, float hi, bool sel) {
  const float a = lo + dpp<CTRL>(lo), b = hi + dpp<CTRL>(hi);
  return sel ? b : a;
}
__device__ __forceinline__ float wave_sum(float x) {
  x = sum8(x); x += dpp<XOR15>(x); x = add16(x, x);
  return add32(x, x);
}
__device__ __forceinline__ float wave_max(float x) {
  x = max8(x); x = fmaxf(x, dpp<XOR15>(x));
  const auto s = __builtin_amdgcn_permlane16_swap(__float_as_uint(x), __float_as_uint(x), false, false);
  x = fmaxf(__uint_as_float(s[0]), __uint_as_float(s[1]));
  const auto t = __builtin_amdgcn_permlane32_swap(__float_as_uint(x), __float_as_uint(x), false, false);
  return fmaxf(__uint_as_float(t[0]), __uint_as_float(t[1]));
}
__device__ __forceinline__ f32x4 mfma(v4u a, v4u b, f32x4 c) {
  return __builtin_amdgcn_mfma_f32_16x16x32_bf16(__builtin_bit_cast(bf16x8, a), __builtin_bit_cast(bf16x8, b), c, 0, 0, 0);
}
__device__ __forceinline__ v2u tr_read(const float* p) {
  return __builtin_bit_cast(v2u, __builtin_amdgcn_ds_read_tr16_b64_v4i16((LDS_AS i16x4*)p));
}
__device__ __forceinline__ void await_tags(v4u& g, rsrc_t r, int off, unsigned tag) {
  if (__builtin_expect(!__all((g.y == tag) & (g.w == tag)), 0)) {
    unsigned spins = 0;
    bool ok;
#pragma unroll 1
    do {
      COMPILER_FENCE();
      g = __builtin_amdgcn_raw_buffer_load_b128(r, off, 0, AUX_SC1);
      ok = __all((g.y == tag) & (g.w == tag));
    } while (!ok && ++spins < SPIN_CAP);
    if (!ok) g.x = 0x7fc00000u;
  }
}
__device__ __forceinline__ v2u granule(float a, float b, unsigned tag) {
  return v2u{(__float_as_uint(a) & ~255u) | (tag & 255u), (__float_as_uint(b) & ~255u) | ((tag >> 8) & 255u)};
}
__device__ __forceinline__ bool untag(v4u& g, unsigned tag) {
  const unsigned t0 = tag & 255u, t1 = (tag >> 8) & 255u;
  g ^= v4u{t0, t1, t0, t1};
  return __all(((g.x | g.y | g.z | g.w) & 255u) == 0u);
}
__device__ __forceinline__ f32x4 await16(v4u g, rsrc_t r, int off, unsigned tag) {
  if (__builtin_expect(!untag(g, tag), 0)) {
    unsigned spins = 0;
    bool ok;
#pragma unroll 1
    do {
      COMPILER_FENCE();
      g = __builtin_amdgcn_raw_buffer_load_b128(r, off, 0, AUX_SC1);
      ok = untag(g, tag);
    } while (!ok && ++spins < SPIN_CAP);
    if (!ok) g.x = 0x7fc00000u;
  }
  return __builtin_bit_cast(f32x4, g);
}
__device__ __forceinline__ void flag_set(float* lds, int k, int w) {
  asm volatile("s_waitcnt lgkmcnt(0)" ::: "memory");
  ((LDS_AS volatile unsigned*)lds)[L_F + k * NW + w] = 1u;
}
__device__ __forceinline__ void flag_wait(float* lds, int k, int lane) {
  while (!__all(((LDS_AS volatile unsigned*)lds)[L_F + k * NW + (lane & 7)] != 0u)) {}
  COMPILER_FENCE();
}
__device__ __forceinline__ void wait_until(unsigned t) {
  asm volatile("1:\n\ts_memrealtime vcc\n\ts_waitcnt lgkmcnt(0)\n\ts_sub_u32 vcc_lo, %0, vcc_lo\n\ts_cmp_lt_u32 vcc_lo, 0x100\n\ts_cbranch_scc1 1b"
               :: "s"(t) : "scc", "vcc", "memory");
}
__device__ __forceinline__ unsigned origin(float* lds, int j, unsigned none) {
  v2u v = *(LDS_AS volatile v2u*)&lds[L_R];
  asm volatile("" : "+v"(v));
  const unsigned org = __builtin_amdgcn_readfirstlane(v.x), x = __builtin_amdgcn_readfirstlane(v.y);
  return org ? org + ((min(j, (int)(x >> 8) & 255) - (int)(x & 255u)) * (int)(x >> 16) >> 4) : none;
}

struct Ctx {
  rsrc_t rq, rk, rv, ro, rx, rw;
  float* lds;
  int w, lane, vo_q, vo_kv, vo_o, vo_x, so_q, so_kv, so_o, orot, q_st, q_ld, ml_st, ml_ld, y_pf, boost, slow, t_st;
  unsigned tc, tagt, epa, gate;
  v2u tg;
  const CONST_AS unsigned *pv, *po;
  unsigned t0, epoch, tag, pf, cpf[3], sel[4];
  v4u qb[N_Q], kb[N_K], vb[N_V], ob[N_O], xv, qg[2], qr, pp, peer;
  v2u qf[4][2];
  v2u of[8][4][2];
  f32x4 acc;
  float xs[5], s0, s1, mw, scale;
};

template <int I> __device__ __forceinline__ void step(Ctx& c) {
  constexpr int op = S.op[I], a = S.a[I];
  float* const lds = c.lds;
  const int w = c.w, lane = c.lane;
  if constexpr (op == IQ) {
    c.qb[a] = __builtin_amdgcn_raw_buffer_load_b128(c.rq, c.vo_q, c.so_q + 8 * a * (NH * DH * 2), AUX_NT);
  } else if constexpr (op == IK) {
    c.kb[a] = __builtin_amdgcn_raw_buffer_load_b128(c.rk, c.vo_kv + (a & 1) * 1024, c.so_kv ^ pair_off(a >> 1), AUX_NT);
  } else if constexpr (op == IV) {
    c.vb[a] = __builtin_amdgcn_raw_buffer_load_b128(c.rv, c.vo_kv + (a & 1) * 1024, c.so_kv ^ pair_off(a >> 1), AUX_NT);
  } else if constexpr (op == IO) {
    c.ob[a] = __builtin_amdgcn_raw_buffer_load_b128(c.ro, c.vo_o, c.so_o + ((8 * a + 32 * c.orot) & 255) * (DM * 2), 0);
  } else if constexpr (op == IX) {
    c.xv = __builtin_amdgcn_raw_buffer_load_b128(c.rx, c.vo_x, 0, 0);
  } else if constexpr (op == WAITV) {
    __builtin_amdgcn_s_waitcnt((a & 15) | ((a >> 4) << 14) | 0x0F70);
  } else if constexpr (op == LAG) {
    const unsigned o = origin(lds, a + 2, c.t0 + ORIGIN0);
    const int late = (int)((unsigned)__builtin_amdgcn_s_memrealtime() - o) - due_at(a);
    c.gate = o - GATE_AHEAD + (w * STAGGER >> 3);
    c.boost = late * GAIN_UP >> 4;
    c.slow = (-late - BAND) * GAIN_DOWN >> 4;
  } else if constexpr (op == TPUB) {
    c.tc = (unsigned)__builtin_amdgcn_s_memrealtime();
    __builtin_amdgcn_raw_buffer_store_b64(v2u{c.tc, c.tagt}, c.rw, c.t_st, a * 512, AUX_SC1);
  } else if constexpr (op == TLD) {
    c.tg = __builtin_amdgcn_raw_buffer_load_b64(c.rw, (WS_T + lane * 8) | (w == NW - 1 ? 0 : OOB), a * 512, AUX_SC1);
  } else if constexpr (op == TCHK) {
    const bool ok = c.tg.y == c.tagt;
    const float d = wave_sum(ok ? (float)(int)(c.tg.x - c.tc) : (float)due_at(T_DELAY)), n = wave_sum(ok ? 1.f : 0.f);
    unsigned org = c.tc + (int)(d * (1.f / 64)) - due_at(T_AT[a]) + MARGIN;
    bool use = w == NW - 1 && n > 16.f;
    int pace = 0;
    if constexpr (a > 0) {
      const int drift = (int)(org - origin(lds, T_AT[a], org));
      use = use && drift > T_KEEP[a] + 2;
      pace = drift > T_SLOW[a] ? T_PACE : 0;
      org += min(2 * drift - 3 * T_KEEP[a], T_OVER) + (pace ? T_EXTRA[a] : 0);
    }
    constexpr int last = T_AT[a] < N_K ? N_K + 4 : N_K + N_V;
    *(LDS_AS volatile v2u*)&lds[L_R + (use ? 0 : 2)] = v2u{org | 1u, (unsigned)(T_AT[a] | (last << 8) | (pace << 16))};
  } else if constexpr (op == GATE) {
    wait_until(c.gate + a);
  } else if constexpr (op == WAITS) {
    asm volatile("s_cmp_lt_i32 %0, %1\n\ts_cbranch_scc1 1f\n\ts_waitcnt vmcnt(%2)\n1:"
                 :: "s"(c.slow), "i"(a >> 8), "i"(a & 255) : "scc", "memory");
  } else if constexpr (op == WAITB) {
    asm volatile("s_cmp_ge_i32 %0, %1\n\ts_cbranch_scc1 1f\n\ts_waitcnt vmcnt(%2)\n1:"
                 :: "s"(c.boost), "i"(a >> 8), "i"(a & 255) : "scc", "memory");
  } else if constexpr (op == SETUP) {
    const int row = lane & 15, j = row & 7;
    const bool act = ((lane >> 4) & 1) == (row >> 3);
    const unsigned two = (lane >> 5) ? 0x0302u : 0x0100u;
    const unsigned s = (j & 1) ? ((two << 16) | 0x0c0cu) : (0x0c0c0000u | two);
#pragma unroll
    for (int i = 0; i < 4; ++i) c.sel[i] = (act && (j >> 1) == i) ? s : 0x0c0c0c0cu;
    c.acc = f32x4{0.f, 0.f, 0.f, 0.f};
    ((unsigned*)lds)[L_F + (lane & 1) * NW + w] = 0u;
    ((unsigned*)lds)[L_R + (lane & 1)] = 0u;
    asm volatile("" :: "s"(c.cpf[0]), "s"(c.cpf[1]), "s"(c.cpf[2]));
    c.tagt = c.epa * 0x9E3779B1u + 0x7F4A7C15u;
    __syncthreads();
    c.cpf[0] = *c.pv; c.cpf[1] = *c.po;
  } else if constexpr (op == QTR) {
#pragma unroll
    for (int i = 0; i < 4; ++i) *(v4u*)&lds[L_TQ + w * 1024 + i * 256 + lane * 4] = c.qb[i];
    COMPILER_FENCE();
    const float* base = &lds[L_TQ + w * 1024 + (lane >> 4) * 256 + ((lane >> 2) & 3) * 32 + (lane & 3) * 2];
#pragma unroll
    for (int ct = 0; ct < 4; ++ct)
#pragma unroll
      for (int h = 0; h < 2; ++h) c.qf[ct][h] = tr_read(base + h * 128 + ct * 8);
    COMPILER_FENCE();
  } else if constexpr (op == QMM) {
    f32x4 d[4];
#pragma unroll
    for (int ct = 0; ct < 4; ++ct)
      d[ct] = mfma(c.xv, v4u{c.qf[ct][0].x, c.qf[ct][0].y, c.qf[ct][1].x, c.qf[ct][1].y}, f32x4{0.f, 0.f, 0.f, 0.f});
    c.tag = c.epoch * 0x9E3779B1u + 0x7F4A7C15u;
    asm volatile("" :: "s"(c.cpf[0]), "s"(c.cpf[1]));
    asm volatile("" : "+s"(c.tag));
    const f32x4 lo = (lane & 16) ? d[1] : d[0], hi = (lane & 16) ? d[3] : d[2];
    *(f32x2*)&lds[L_RED + w * 128 + lane * 2] = (lane & 32) ? f32x2{hi.x, hi.y} : f32x2{lo.x, lo.y};
    flag_set(lds, 0, w);
  } else if constexpr (op == QPUB) {
    flag_wait(lds, 0, lane);
    f32x2 t = {0.f, 0.f};
#pragma unroll
    for (int i = 0; i < 7; ++i) t += *(const f32x2*)&lds[L_RED + i * 128 + lane * 2];
    const float nx = dpp<XOR1>(t.x), ny = dpp<XOR1>(t.y);
    __builtin_amdgcn_raw_buffer_store_b64(granule((lane & 1) ? ny : t.x, (lane & 1) ? t.y : nx, c.tag), c.rw, c.q_st, 0, AUX_SC1);
  } else if constexpr (op == QLD) {
    c.qg[a] = __builtin_amdgcn_raw_buffer_load_b128(c.rw, c.q_ld + a * 1024, 0, AUX_SC1);
  } else if constexpr (op == QCHK) {
    const f32x4 q = await16(c.qg[0], c.rw, c.q_ld, c.tag) + await16(c.qg[1], c.rw, c.q_ld + 1024, c.tag);
    *(v2u*)&lds[L_TQ + w * 1024 + lane * 2] = v2u{cvtpk(q.x, q.y), cvtpk(q.z, q.w)};
    COMPILER_FENCE();
    c.qr = *(const v4u*)&lds[L_TQ + w * 1024 + (lane & 31) * 4];
  } else if constexpr (op == CK) {
    const v4u k = c.kb[a], q = c.qr;
    float x = dot2(k.x, q.x, dot2(k.z, q.z, 0.f)) + dot2(k.y, q.y, dot2(k.w, q.w, 0.f));
    if constexpr ((a & 1) == 1) x = add16(c.xs[0], x);
    if constexpr ((a & 3) == 3) x = tstep<ROR8>(c.xs[1], x, lane & 8);
    if constexpr ((a & 7) == 7) x = tstep<XOR7>(c.xs[2], x, lane & 4);
    if constexpr ((a & 15) == 15) x = tstep<XOR2>(c.xs[3], x, lane & 2);
    if constexpr ((a & 31) == 31) (a == 31 ? c.s0 : c.s1) = tstep<XOR1>(c.xs[4], x, lane & 1) * (0.0625f * 1.4426950408889634f);
    else c.xs[__builtin_ctz(~a)] = x;
  } else if constexpr (op == SM1) {
    const float m = wave_max(fmaxf(c.s0, c.s1));
    const float p0 = __builtin_amdgcn_exp2f(c.s0 - m), p1 = __builtin_amdgcn_exp2f(c.s1 - m);
    __bf16* pw = (__bf16*)&lds[L_P + w * 64] + (__builtin_bitreverse32(lane) >> 26);
    pw[0] = (__bf16)p0; pw[64] = (__bf16)p1;
    COMPILER_FENCE();
    c.mw = m; c.s0 = p0 + p1;
    c.pp = *(const v4u*)&lds[L_P + w * 64];
  } else if constexpr (op == SM2) {
    const float l = wave_sum(c.s0);
    lds[L_ML + 2 * w + (lane & 1)] = (lane & 1) ? l : c.mw;
    flag_set(lds, 1, w);
  } else if constexpr (op == MLPUB) {
    flag_wait(lds, 1, lane);
    const f32x2 ml = *(const f32x2*)&lds[L_ML + 2 * (lane & 7)];
    const float M = max8(ml.x), L = sum8(ml.y * __builtin_amdgcn_exp2f(ml.x - M));
    const v4u g = {__float_as_uint(M), c.tag, __float_as_uint(L), c.tag};
    __builtin_amdgcn_raw_buffer_store_b128(g, c.rw, c.ml_st, 0, AUX_SC1);
  } else if constexpr (op == CV) {
    const unsigned pr = c.pp[a & 3];
    const v4u am = {__builtin_amdgcn_perm(0u, pr, c.sel[0]), __builtin_amdgcn_perm(0u, pr, c.sel[1]),
                    __builtin_amdgcn_perm(0u, pr, c.sel[2]), __builtin_amdgcn_perm(0u, pr, c.sel[3])};
    c.acc = mfma(am, c.vb[a], c.acc);
    if constexpr ((a & 3) == 3 && a + 1 < N_V) c.pp = *(const v4u*)&lds[L_P + w * 64 + (a + 1)];
  } else if constexpr (op == YPF) {
    c.pf = __builtin_amdgcn_raw_buffer_load_b32(c.rw, c.y_pf, 0, AUX_SC1);
  } else if constexpr (op == PEERLD) {
    c.peer = __builtin_amdgcn_raw_buffer_load_b128(c.rw, c.ml_ld, 0, AUX_SC1);
  } else if constexpr (op == PEERCHK) {
    asm volatile("" :: "v"(c.pf));
    await_tags(c.peer, c.rw, c.ml_ld, c.tag);
    const float m = __uint_as_float(c.peer.x);
    float M = fmaxf(m, dpp<XOR1>(m)); M = fmaxf(M, dpp<XOR2>(M));
    float L = __uint_as_float(c.peer.z) * __builtin_amdgcn_exp2f(m - M); L += dpp<XOR1>(L); L += dpp<XOR2>(L);
    c.scale = __builtin_amdgcn_exp2f(c.mw - M) / L;
  } else if constexpr (op == TRW) {
#pragma unroll
    for (int i = 0; i < 4; ++i) *(v4u*)&lds[L_T + w * 1024 + i * 256 + lane * 4] = c.ob[4 * a + i];
    COMPILER_FENCE();
  } else if constexpr (op == TRR) {
    const float* base = &lds[L_T + w * 1024 + (lane >> 4) * 256 + ((lane >> 2) & 3) * 32 + (lane & 3) * 2];
#pragma unroll
    for (int ct = 0; ct < 4; ++ct)
#pragma unroll
      for (int h = 0; h < 2; ++h) c.of[a][ct][h] = tr_read(base + h * 128 + ct * 8);
    COMPILER_FENCE();
  }
  __builtin_amdgcn_sched_barrier(0);
}
template <int B, int... I> __device__ __forceinline__ void run_seq(Ctx& c, std::integer_sequence<int, I...>) { (step<B + I>(c), ...); }
template <int B, int E> __device__ __forceinline__ void run(Ctx& c) { run_seq<B>(c, std::make_integer_sequence<int, E - B>{}); }

__global__ void __launch_bounds__(NTHR, 2)
mha_decode(const __bf16* __restrict__ xA, const __bf16* __restrict__ kA, const __bf16* __restrict__ vA,
           const __bf16* __restrict__ wqA, const __bf16* __restrict__ woA,
           const __bf16* __restrict__ xB, const __bf16* __restrict__ kB, const __bf16* __restrict__ vB,
           const __bf16* __restrict__ wqB, const __bf16* __restrict__ woB, __bf16* out, unsigned char* ws) {
  __shared__ __attribute__((aligned(16))) float lds[L_END];
  Ctx c;
  const unsigned long long tstart = __builtin_amdgcn_s_memrealtime();
  __builtin_amdgcn_sched_barrier(0);
  const int bid = blockIdx.x, rk = bid >> 3, bh = (bid & 7) * 4 + (rk >> 3), br = bh >> 4, head = bh & 15;
  const int b = (rk >> 2) & 1, split = rk & 3, grp = br * 2 + b, prod = head * 4 + split;
  const int tid = threadIdx.x, lane = tid & 63, w = __builtin_amdgcn_readfirstlane(tid >> 6);
  const int off7 = w < 7 ? 0 : OOB;
  c.w = w; c.lane = lane; c.lds = lds;
  c.t0 = (unsigned)tstart;
  c.vo_q = ((lane >> 3) * (NH * DH * 2) + (lane & 7) * 16) | off7;
  c.so_q = ((224 * b + 32 * w) * NH * DH + head * DH + split * 64) * 2;
  c.vo_kv = lane * 16;
  c.so_kv = (b * NH + head) * (SK * DH * 2) + (((w & 1) << 11) | ((w & 2) << 12) | ((w & 4) << 14) | ((split & 1) << 14) | ((split & 2) << 18)) +
            (((head & 2) ? 0x68000 : 0) ^ ((head & 8) << 12) ^ ((((head >> 2) ^ b) & 1) << 20));
  c.vo_o = ((lane >> 3) * (DM * 2) + w * 128 + (lane & 7) * 16) | off7;
  c.so_o = head * DH * DM * 2;
  c.orot = rk & 7;
  int ep_off = br * 256;
  asm volatile("" : "+s"(c.so_q), "+s"(c.so_kv), "+s"(c.so_o), "+s"(c.orot), "+s"(ep_off), "+v"(c.vo_q), "+v"(c.vo_kv), "+v"(c.vo_o)
               : "s"(tstart));
  __builtin_amdgcn_sched_barrier(0);
  c.rq = __builtin_amdgcn_make_buffer_rsrc((void*)(br ? wqB : wqA), 0, DM * NH * DH * 2, RSRC_W3);
  c.ro = __builtin_amdgcn_make_buffer_rsrc((void*)(br ? woB : woA), 0, NH * DH * DM * 2, RSRC_W3);
  c.rk = __builtin_amdgcn_make_buffer_rsrc((void*)(br ? kB : kA), 0, 2 * NH * SK * DH * 2, RSRC_W3);
  run<0, 1>(c);
  c.epoch = *(const CONST_AS unsigned*)(ws + ep_off);
  c.epa = *(const CONST_AS unsigned*)ws;
  const unsigned long long pc = __builtin_amdgcn_s_getpc() & ~63ull;
  c.cpf[0] = *(const CONST_AS unsigned*)(pc + (rk * NW + w) * 64);
  c.cpf[1] = *(const CONST_AS unsigned*)(pc + (256 + min(rk * NW + w, 121)) * 64);
  const int touch = (b * NH + head) * (SK * DH) + (split * NW + w) * 32768;
  c.cpf[2] = *(const CONST_AS unsigned*)((br ? kB : kA) + touch);
  run<1, N_FIRST>(c);
  int br2 = br;
  asm volatile("" : "+s"(br2));
  c.rx = __builtin_amdgcn_make_buffer_rsrc((void*)(br2 ? xB : xA), 0, 2 * DM * 2, RSRC_W3);
  c.vo_x = (((lane & 1) * DM + 224 * b + 32 * w + 8 * (lane >> 4)) * 2) | off7;
  c.rv = __builtin_amdgcn_make_buffer_rsrc((void*)(br2 ? vB : vA), 0, 2 * NH * SK * DH * 2, RSRC_W3);
  c.pv = (const CONST_AS unsigned*)((br2 ? vB : vA) + touch);
  c.po = (const CONST_AS unsigned*)out;
  c.rw = __builtin_amdgcn_make_buffer_rsrc((void*)ws, 0, WS_END, RSRC_W3);
  const rsrc_t rout = __builtin_amdgcn_make_buffer_rsrc((void*)out, 0, 4 * DM * 2, RSRC_W3);
  c.q_st = (WS_Q + bh * 4096 + (lane & 1) * 2048 + b * 1024 + split * 256 + (lane & ~1) * 4) | (w == 0 ? 0 : OOB);
  c.q_ld = WS_Q + bh * 4096 + b * 2048 + lane * 16;
  c.ml_st = (WS_ML + (grp * NH + head) * 128 + split * 16) | (tid == 0 ? 0 : OOB);
  c.ml_ld = WS_ML + (grp * NH + head) * 128 + (lane & 3) * 16;
  c.t_st = (WS_T + ((bid >> 5) * 8 + (bid & 7)) * 8) | (tid == 64 * 3 && ((rk + (rk >> 2)) & 3) == 0 ? 0 : OOB);
  c.y_pf = (WS_PKT + (grp * 64 + prod) * PKT_BYTES + lane * 128) | (tid < PKT_BYTES / 128 ? 0 : OOB);

  run<N_FIRST, S.n - N_LAST>(c);

  const int pk = WS_PKT + grp * 64 * PKT_BYTES;
  const int dq = 32 * (lane >> 5) + 2 * (lane & 15) + ((lane >> 4) & 1);
  int a_wr = (L_RED + w * A_STR + ((dq - 8 * c.orot) & 63)) * 4, a_rd = (L_RED + (lane & 15) * A_STR + 4 * (lane >> 4)) * 4;
  int idx = (lane & 8) ? 0xEEEE : 0x4444;
  int y_off = (pk + prod * PKT_BYTES + tid * 4) | ((lane & 1) ? OOB : 0);
  int p_off = pk + (lane >> 2) * PKT_BYTES + prod * 64 + (lane & 3) * 16;
  int o_off = ((b * (2 * DM) + br * DM + prod * 16 + (lane & 3) * 4 + (lane >> 4)) * 2) | ((lane & 12) ? OOB : 0);
  int e_off = (prod == 0 && b == 0 && lane == 0) ? ep_off : OOB;
  asm volatile("" : "+v"(a_wr), "+v"(a_rd), "+v"(idx), "+v"(y_off), "+v"(p_off), "+v"(o_off), "+v"(e_off));
  unsigned due = origin(lds, N_K + N_V - 1, 0u) + due_at(N_K + N_V - 1) + END_AFTER;
  asm volatile("" : "+s"(due));
  f32x4 y[4];
#pragma unroll
  for (int ct = 0; ct < 4; ++ct) { y[ct] = f32x4{0.f, 0.f, 0.f, 0.f}; asm volatile("" : "+v"(y[ct])); }

  run<S.n - N_LAST, S.n>(c);
  LDS_AS unsigned char* const lb = (LDS_AS unsigned char*)lds;
  *(LDS_AS unsigned*)(lb + a_wr) = cvtpk(c.acc.x * c.scale, c.acc.y * c.scale);
  *(LDS_AS unsigned*)(lb + a_wr + 8 * A_STR * 4) = cvtpk(c.acc.z * c.scale, c.acc.w * c.scale);
  __syncthreads();
  if (__builtin_expect(w == NW - 1, 0)) {
    if (prod < DM / 16) {
      unsigned cnt;
      asm volatile(
          "s_mov_b32 %0, 0\n"
          "1:\n\t"
          "s_memrealtime vcc\n\t"
          "s_waitcnt lgkmcnt(0)\n\t"
          "s_sub_u32 vcc_lo, %9, vcc_lo\n\t"
          "s_cmp_lt_u32 vcc_lo, 0x100\n\t"
          "s_cbranch_scc1 1b\n"
          "2:\n\t"
          "buffer_load_dwordx4 v[0:3], %1, %4, 0 offen sc1\n\t"
          "buffer_load_dwordx4 v[4:7], %1, %4, %10 offen sc1\n\t"
          "buffer_load_dwordx4 v[8:11], %1, %4, %11 offen sc1\n\t"
          "buffer_load_dwordx4 v[12:15], %1, %4, %12 offen sc1\n\t"
          "s_add_u32 %0, %0, 1\n\t"
          "s_waitcnt vmcnt(3)\n\t"
          "v_xor_b32 v0, %6, v0\n\tv_xor_b32 v1, %7, v1\n\tv_xor_b32 v2, %6, v2\n\tv_xor_b32 v3, %7, v3\n\t"
          "v_or3_b32 v16, v0, v1, v2\n\t"
          "s_waitcnt vmcnt(2)\n\t"
          "v_xor_b32 v4, %6, v4\n\tv_xor_b32 v5, %7, v5\n\tv_xor_b32 v6, %6, v6\n\tv_xor_b32 v7, %7, v7\n\t"
          "v_or3_b32 v16, v16, v3, v4\n\tv_or3_b32 v17, v5, v6, v7\n\t"
          "v_pk_add_f32 v[0:1], v[0:1], v[4:5]\n\tv_pk_add_f32 v[2:3], v[2:3], v[6:7]\n\t"
          "s_waitcnt vmcnt(1)\n\t"
          "v_xor_b32 v8, %6, v8\n\tv_xor_b32 v9, %7, v9\n\tv_xor_b32 v10, %6, v10\n\tv_xor_b32 v11, %7, v11\n\t"
          "v_or3_b32 v16, v16, v8, v9\n\tv_or3_b32 v17, v17, v10, v11\n\t"
          "s_waitcnt vmcnt(0)\n\t"
          "v_xor_b32 v12, %6, v12\n\tv_xor_b32 v13, %7, v13\n\tv_xor_b32 v14, %6, v14\n\tv_xor_b32 v15, %7, v15\n\t"
          "v_or3_b32 v16, v16, v12, v13\n\tv_or3_b32 v17, v17, v14, v15\n\t"
          "v_pk_add_f32 v[8:9], v[8:9], v[12:13]\n\tv_pk_add_f32 v[10:11], v[10:11], v[14:15]\n\t"
          "v_or_b32 v16, v16, v17\n\t"
          "v_pk_add_f32 v[0:1], v[0:1], v[8:9]\n\tv_pk_add_f32 v[2:3], v[2:3], v[10:11]\n\t"
          "v_and_b32 v16, 0xff, v16\n\t"
          "v_cmp_ne_u32 vcc, 0, v16\n\t"
          "v_permlane32_swap_b32 v0, v2\n\tv_permlane32_swap_b32 v1, v3\n\t"
          "s_nop 0\n\t"
          "v_add_f32 v0, v0, v2\n\tv_add_f32 v1, v1, v3\n\t"
          "s_nop 1\n\t"
          "v_permlane16_swap_b32 v0, v1\n\t"
          "s_nop 0\n\t"
          "v_add_f32 v0, v0, v1\n\t"
          "s_nop 1\n\t"
          "v_add_f32_dpp v0, v0, v0 row_ror:4 row_mask:0xf bank_mask:0xf\n\t"
          "s_nop 1\n\t"
          "v_add_f32_dpp v0, v0, v0 row_ror:8 row_mask:0xf bank_mask:0xf\n\t"
          "s_cmp_lt_u32 %0, %13\n\t"
          "v_cvt_pk_bf16_f32 v0, v0, v0\n\t"
          "s_cbranch_vccz 4f\n\t"
          "s_cbranch_scc1 2b\n\t"
          "v_mov_b32 v0, 0x7fc0\n"
          "4:\n\t"
          "buffer_store_short v0, %2, %5, 0 offen\n\t"
          "v_mov_b32 v1, %8\n\t"
          "buffer_store_dword v1, %3, %4, 0 offen sc1"
          : "=&s"(cnt)
          : "v"(p_off), "v"(o_off), "v"(e_off), "s"(c.rw), "s"(rout), "s"(c.tag & 255u), "s"((c.tag >> 8) & 255u), "s"(c.tag), "s"(due),
            "s"(16 * PKT_BYTES), "s"(32 * PKT_BYTES), "s"(48 * PKT_BYTES), "s"(SPIN_CAP)
          : "scc", "vcc", "memory", "v0", "v1", "v2", "v3", "v4", "v5", "v6", "v7", "v8", "v9", "v10", "v11", "v12", "v13", "v14", "v15", "v16", "v17");
    }
    __builtin_amdgcn_endpgm();
  }
  v4u am[4];
#pragma unroll
  for (int ks = 0; ks < 4; ++ks) am[ks] = *(LDS_AS v4u*)(lb + a_rd + 64 * ks);
  __builtin_amdgcn_sched_barrier(0);
#pragma unroll
  for (int ks = 0; ks < 4; ++ks)
#pragma unroll
    for (int ct = 0; ct < 4; ++ct) {
      const v2u b0 = c.of[2 * ks][ct][0], b1 = c.of[2 * ks][ct][1], b2 = c.of[2 * ks + 1][ct][0], b3 = c.of[2 * ks + 1][ct][1];
      const v8u bm = {b0.x, b0.y, b1.x, b1.y, b2.x, b2.y, b3.x, b3.y};
      y[ct] = __builtin_amdgcn_smfmac_f32_16x16x64_bf16(__builtin_bit_cast(bf16x8, am[ks]), __builtin_bit_cast(bf16x16, bm), y[ct], idx, 0, 0);
    }
  float sm[4];
#pragma unroll
  for (int ct = 0; ct < 4; ++ct) sm[ct] = (y[ct].x + y[ct].y) + (y[ct].z + y[ct].w);
  const float yv = add32(add16(sm[0], sm[1]), add16(sm[2], sm[3]));
  __builtin_amdgcn_raw_buffer_store_b64(granule(yv, dpp<XOR1>(yv), c.tag), c.rw, y_off, 0, AUX_SC1);
}

}

extern "C" void kernel_launch(void* const* d_in, const int* in_sizes, int n_in, void* d_out, int out_size, void* d_ws,
                              size_t ws_size, hipStream_t stream) {
  (void)in_sizes; (void)n_in; (void)out_size; (void)ws_size;
  mha_decode<<<256, NTHR, 0, stream>>>((const __bf16*)d_in[0], (const __bf16*)d_in[1], (const __bf16*)d_in[2],
                                       (const __bf16*)d_in[3], (const __bf16*)d_in[4], (const __bf16*)d_in[5],
                                       (const __bf16*)d_in[6], (const __bf16*)d_in[7], (const __bf16*)d_in[8],
                                       (const __bf16*)d_in[9], (__bf16*)d_out, (unsigned char*)d_ws);
}
```

```cpp
#include <hip/hip_runtime.h>
#include <hip/hip_bf16.h>
#include <utility>

namespace {

typedef unsigned v2u __attribute__((ext_vector_type(2)));
typedef unsigned v4u __attribute__((ext_vector_type(4)));
typedef unsigned v8u __attribute__((ext_vector_type(8)));
typedef float f32x2 __attribute__((ext_vector_type(2)));
typedef float f32x4 __attribute__((ext_vector_type(4)));
typedef __bf16 bf16x2 __attribute__((ext_vector_type(2)));
typedef __bf16 bf16x8 __attribute__((ext_vector_type(8)));
typedef __bf16 bf16x16 __attribute__((ext_vector_type(16)));
typedef short i16x4 __attribute__((ext_vector_type(4)));
typedef __amdgpu_buffer_rsrc_t rsrc_t;
#define LDS_AS __attribute__((address_space(3)))
#define CONST_AS __attribute__((address_space(4)))
#define COMPILER_FENCE() asm volatile("" ::: "memory")

constexpr int DM = 448, NH = 16, SK = 4096, DH = 256;
constexpr int NW = 8, NTHR = NW * 64;
constexpr int N_Q = 4, N_K = 64, N_V = 64, N_O = 32;
constexpr unsigned SPIN_CAP = 1u << 20;
constexpr int RSRC_W3 = 0x00020000, AUX_NT = 2, AUX_SC1 = 16, OOB = 0x40000000;
constexpr int WS_PKT = 4096, PKT_BYTES = DM * 4, WS_Q = WS_PKT + 256 * PKT_BYTES, WS_ML = WS_Q + 64 * 2048,
              WS_T = WS_ML + 64 * 128, WS_END = WS_T + 3 * 512;
constexpr int A_STR = 68;
constexpr int L_RED = 0, L_T = L_RED + 16 * A_STR, L_TQ = L_T + NW * 1024, L_ML = L_TQ + NW * 1024, L_P = L_ML + 2 * NW,
              L_F = L_P + NW * 64, L_R = L_F + NW * 2, L_END = L_R + 4;

constexpr int WIN = 3;
constexpr int O_FIRST = 4;
constexpr int O_START = 16, O_EVERY = 3;
constexpr int X_AT = 2, QMM_AT = 6, QPUB_AT = 9, QLD_AT = 17, QCHK_AT = 21, YPF_AT = 100;
constexpr int N_FIRST = N_Q + O_FIRST + 1, N_LAST = 3;
constexpr int TAU = 431, ORIGIN0 = 255, MARGIN = 32, GATE_FROM = 23, GATE_AHEAD = 21, STAGGER = 24, END_AFTER = 126;
constexpr int LAG_FROM = 7, LAG_EVERY = 4, GAIN_UP = 9, BAND = 58, GAIN_DOWN = 4;
constexpr int T_AT[3] = {10, 38, 84}, T_DELAY = 6, T_KEEP[3] = {0, 14, 8}, T_OVER = 12, T_SLOW[3] = {0, 32, 36},
              T_EXTRA[3] = {0, 18, 40}, T_PACE = 42;
constexpr int due_at(int j) { return j * TAU / 16; }

enum Op : int { IQ, IK, IV, IO, IX, QPUB, QLD, MLPUB, YPF, TPUB, TLD, PEERLD,
                WAITV, WAITB, WAITS, GATE, LAG, TCHK, SETUP, QTR, QMM, QCHK, CK, SM1, SM2, CV, PEERCHK, TRW, TRR };
struct Sched { int n; int op[900]; int a[900]; };
constexpr Sched make_sched() {
  Sched s{};
  int vmk[256] = {}, vmi[256] = {}, nvm = 0;
  auto emit = [&](int op, int a) {
    s.op[s.n] = op; s.a[s.n] = a; ++s.n;
    if (op <= PEERLD) { vmk[nvm] = op; vmi[nvm] = a; ++nvm; }
  };
  int no = 0, nk = 0, nv = 0, nw = 0, nr = 0, sm = 0;
  int lk = 0, lv = 0, lo = 0;
  for (int j = 0; j < N_Q; ++j) emit(IQ, j);
  while (no < O_FIRST) emit(IO, no++);
  for (int j = 0; j < N_K + N_V; ++j) {
    if (j > 0) {
      int p = nvm, seen = 0;
      while (seen < WIN) { --p; if (vmk[p] <= IV) ++seen; }
      if (j > LAG_FROM) {
        const int k = (j - LAG_FROM - 1) % LAG_EVERY + 1;
        int p2 = p, p0 = p;
        do --p2; while (vmk[p2] > IV);
        do ++p0; while (vmk[p0] > IV);
        emit(WAITV, nvm - 1 - p2);
        emit(WAITB, (nvm - 1 - p) | (k << 8));
        if (j > GATE_FROM) emit(GATE, due_at(j)); else emit(WAITS, (nvm - 1 - p0) | (k << 8));
        p = p2;
      } else emit(WAITV, nvm - 1 - p);
      if (vmk[p] == IK) lk = vmi[p] + 1;
      if (vmk[p] == IV) { lk = N_K; lv = vmi[p] + 1; }
      for (int t = 0; t < p; ++t) if (vmk[t] == IO) lo = vmi[t] + 1;
    }
    if (j < N_K) emit(IK, j); else emit(IV, j - N_K);
    if (j == 0) emit(SETUP, 0);
    if (j >= O_START && (j - O_START) % O_EVERY == 0 && no < N_O) emit(IO, no++);
    if (j >= LAG_FROM && (j - LAG_FROM) % LAG_EVERY == 0 && j + 1 < N_K + N_V) emit(LAG, j);
    if (j == X_AT) emit(IX, 0);
    if (j == YPF_AT) emit(YPF, 0);
    for (int t = 0; t < 3; ++t) {
      if (j == T_AT[t]) emit(TPUB, t);
      if (j == T_AT[t] + T_DELAY) emit(TLD, t);
      if (j == T_AT[t] + T_DELAY + 4) emit(TCHK, t);
    }
    if (j == QMM_AT - 1) emit(QTR, 0);
    if (j == QMM_AT) emit(QMM, 0);
    if (j == QPUB_AT) emit(QPUB, 0);
    if (j == QLD_AT) { emit(QLD, 0); emit(QLD, 1); }
    if (j == QCHK_AT) emit(QCHK, 0);
    else if (j > QCHK_AT && nk < N_K) { for (int t = 0; t < 3 && nk < lk; ++t) emit(CK, nk++); }
    else if (nk == N_K && sm < 2) { emit(sm == 0 ? SM1 : SM2, 0); ++sm; }
    else if (sm == 2) {
      for (int t = 0; t < 3 && nv < lv; ++t) {
        emit(CV, nv++);
        if (nv == 12) emit(MLPUB, 0);
        if (nv == N_V - 20) emit(PEERLD, 0);
        if (nv == N_V - 10) emit(PEERCHK, 0);
      }
    }
    if (nr < nw) emit(TRR, nr++);
    else if (4 * nw + 4 <= lo) emit(TRW, nw++);
  }
  if (no != N_O || nr != N_O / 4 || nv < N_V - 10 || nv > N_V - N_LAST) s.n = -1;
  else while (nv < N_V) emit(CV, nv++);
  return s;
}
constexpr Sched S = make_sched();
static_assert(S.n > 0, "the schedule must finish everything but the last V loads inside the stream");

constexpr int pair_off(int g) { return ((g & 1) << 12) | ((g & 2) << 14) | ((g & 12) << 15) | ((g & 16) << 16); }

__device__ __forceinline__ float dot2(unsigned a, unsigned b, float c) {
  return __builtin_amdgcn_fdot2_f32_bf16(__builtin_bit_cast(bf16x2, a), __builtin_bit_cast(bf16x2, b), c, false);
}
__device__ __forceinline__ unsigned cvtpk(float lo, float hi) {
  const bf16x2 r = {(__bf16)lo, (__bf16)hi};
  return __builtin_bit_cast(unsigned, r);
}
template <int CTRL> __device__ __forceinline__ float dpp(float x) {
  return __builtin_bit_cast(float, __builtin_amdgcn_mov_dpp(__builtin_bit_cast(int, x), CTRL, 0xf, 0xf, true));
}
constexpr int XOR1 = 0xB1, XOR2 = 0x4E, XOR7 = 0x141, XOR15 = 0x140, ROR4 = 0x124, ROR8 = 0x128;
__device__ __forceinline__ float sum8(float x) { x += dpp<XOR1>(x); x += dpp<XOR2>(x); x += dpp<XOR7>(x); return x; }
__device__ __forceinline__ float max8(float x) {
  x = fmaxf(x, dpp<XOR1>(x)); x = fmaxf(x, dpp<XOR2>(x)); x = fmaxf(x, dpp<XOR7>(x));
  return x;
}
__device__ __forceinline__ float add16(float a, float b) {
  const auto s = __builtin_amdgcn_permlane16_swap(__float_as_uint(a), __float_as_uint(b), false, false);
  return __uint_as_float(s[0]) + __uint_as_float(s[1]);
}
__device__ __forceinline__ float add32(float a, float b) {
  const auto s = __builtin_amdgcn_permlane32_swap(__float_as_uint(a), __float_as_uint(b), false, false);
  return __uint_as_float(s[0]) + __uint_as_float(s[1]);
}
template <int CTRL> __device__ __forceinline__ float tstep(float lo, float hi, bool sel) {
  const float a = lo + dpp<CTRL>(lo), b = hi + dpp<CTRL>(hi);
  return sel ? b : a;
}
__device__ __forceinline__ float wave_sum(float x) {
  x = sum8(x); x += dpp<XOR15>(x); x = add16(x, x);
  return add32(x, x);
}
__device__ __forceinline__ float wave_max(float x) {
  x = max8(x); x = fmaxf(x, dpp<XOR15>(x));
  const auto s = __builtin_amdgcn_permlane16_swap(__float_as_uint(x), __float_as_uint(x), false, false);
  x = fmaxf(__uint_as_float(s[0]), __uint_as_float(s[1]));
  const auto t = __builtin_amdgcn_permlane32_swap(__float_as_uint(x), __float_as_uint(x), false, false);
  return fmaxf(__uint_as_float(t[0]), __uint_as_float(t[1]));
}
__device__ __forceinline__ f32x4 mfma(v4u a, v4u b, f32x4 c) {
  return __builtin_amdgcn_mfma_f32_16x16x32_bf16(__builtin_bit_cast(bf16x8, a), __builtin_bit_cast(bf16x8, b), c, 0, 0, 0);
}
__device__ __forceinline__ v2u tr_read(const float* p) {
  return __builtin_bit_cast(v2u, __builtin_amdgcn_ds_read_tr16_b64_v4i16((LDS_AS i16x4*)p));
}
__device__ __forceinline__ void await_tags(v4u& g, rsrc_t r, int off, unsigned tag) {
  if (__builtin_expect(!__all((g.y == tag) & (g.w == tag)), 0)) {
    unsigned spins = 0;
    bool ok;
#pragma unroll 1
    do {
      COMPILER_FENCE();
      g = __builtin_amdgcn_raw_buffer_load_b128(r, off, 0, AUX_SC1);
      ok = __all((g.y == tag) & (g.w == tag));
    } while (!ok && ++spins < SPIN_CAP);
    if (!ok) g.x = 0x7fc00000u;
  }
}
__device__ __forceinline__ v2u granule(float a, float b, unsigned tag) {
  return v2u{(__float_as_uint(a) & ~255u) | (tag & 255u), (__float_as_uint(b) & ~255u) | ((tag >> 8) & 255u)};
}
__device__ __forceinline__ bool untag(v4u& g, unsigned tag) {
  const unsigned t0 = tag & 255u, t1 = (tag >> 8) & 255u;
  g ^= v4u{t0, t1, t0, t1};
  return __all(((g.x | g.y | g.z | g.w) & 255u) == 0u);
}
__device__ __forceinline__ f32x4 await16(v4u g, rsrc_t r, int off, unsigned tag) {
  if (__builtin_expect(!untag(g, tag), 0)) {
    unsigned spins = 0;
    bool ok;
#pragma unroll 1
    do {
      COMPILER_FENCE();
      g = __builtin_amdgcn_raw_buffer_load_b128(r, off, 0, AUX_SC1);
      ok = untag(g, tag);
    } while (!ok && ++spins < SPIN_CAP);
    if (!ok) g.x = 0x7fc00000u;
  }
  return __builtin_bit_cast(f32x4, g);
}
__device__ __forceinline__ void flag_set(float* lds, int k, int w) {
  asm volatile("s_waitcnt lgkmcnt(0)" ::: "memory");
  ((LDS_AS volatile unsigned*)lds)[L_F + k * NW + w] = 1u;
}
__device__ __forceinline__ void flag_wait(float* lds, int k, int lane) {
  while (!__all(((LDS_AS volatile unsigned*)lds)[L_F + k * NW + (lane & 7)] != 0u)) {}
  COMPILER_FENCE();
}
__device__ __forceinline__ void wait_until(unsigned t) {
  asm volatile("1:\n\ts_memrealtime vcc\n\ts_waitcnt lgkmcnt(0)\n\ts_sub_u32 vcc_lo, %0, vcc_lo\n\ts_cmp_lt_u32 vcc_lo, 0x100\n\ts_cbranch_scc1 1b"
               :: "s"(t) : "scc", "vcc", "memory");
}
__device__ __forceinline__ unsigned origin(float* lds, int j, unsigned none) {
  v2u v = *(LDS_AS volatile v2u*)&lds[L_R];
  asm volatile("" : "+v"(v));
  const unsigned org = __builtin_amdgcn_readfirstlane(v.x), x = __builtin_amdgcn_readfirstlane(v.y);
  return org ? org + ((min(j, (int)(x >> 8) & 255) - (int)(x & 255u)) * (int)(x >> 16) >> 4) : none;
}

struct Ctx {
  rsrc_t rq, rk, rv, ro, rx, rw;
  float* lds;
  int w, lane, vo_q, vo_kv, vo_o, vo_x, so_q, so_kv, so_o, orot, q_st, q_ld, ml_st, ml_ld, y_pf, boost, slow, t_st;
  unsigned tc, tagt, epa, gate;
  v2u tg;
  const CONST_AS unsigned *pv, *po;
  unsigned t0, epoch, tag, pf, cpf[3], sel[4];
  v4u qb[N_Q], kb[N_K], vb[N_V], ob[N_O], xv, qg[2], qr, pp, peer;
  v2u qf[4][2];
  v2u of[8][4][2];
  f32x4 acc;
  float xs[5], s0, s1, mw, scale;
};

template <int I> __device__ __forceinline__ void step(Ctx& c) {
  constexpr int op = S.op[I], a = S.a[I];
  float* const lds = c.lds;
  const int w = c.w, lane = c.lane;
  if constexpr (op == IQ) {
    c.qb[a] = __builtin_amdgcn_raw_buffer_load_b128(c.rq, c.vo_q, c.so_q + 8 * a * (NH * DH * 2), AUX_NT);
  } else if constexpr (op == IK) {
    c.kb[a] = __builtin_amdgcn_raw_buffer_load_b128(c.rk, c.vo_kv + (a & 1) * 1024, c.so_kv ^ pair_off(a >> 1), AUX_NT);
  } else if constexpr (op == IV) {
    c.vb[a] = __builtin_amdgcn_raw_buffer_load_b128(c.rv, c.vo_kv + (a & 1) * 1024, c.so_kv ^ pair_off(a >> 1), AUX_NT);
  } else if constexpr (op == IO) {
    c.ob[a] = __builtin_amdgcn_raw_buffer_load_b128(c.ro, c.vo_o, c.so_o + ((8 * a + 32 * c.orot) & 255) * (DM * 2), 0);
  } else if constexpr (op == IX) {
    c.xv = __builtin_amdgcn_raw_buffer_load_b128(c.rx, c.vo_x, 0, 0);
  } else if constexpr (op == WAITV) {
    __builtin_amdgcn_s_waitcnt((a & 15) | ((a >> 4) << 14) | 0x0F70);
  } else if constexpr (op == LAG) {
    const unsigned o = origin(lds, a + 2, c.t0 + ORIGIN0);
    const int late = (int)((unsigned)__builtin_amdgcn_s_memrealtime() - o) - due_at(a);
    c.gate = o - GATE_AHEAD + (w * STAGGER >> 3);
    c.boost = late * GAIN_UP >> 4;
    c.slow = (-late - BAND) * GAIN_DOWN >> 4;
  } else if constexpr (op == TPUB) {
    c.tc = (unsigned)__builtin_amdgcn_s_memrealtime();
    __builtin_amdgcn_raw_buffer_store_b64(v2u{c.tc, c.tagt}, c.rw, c.t_st, a * 512, AUX_SC1);
  } else if constexpr (op == TLD) {
    c.tg = __builtin_amdgcn_raw_buffer_load_b64(c.rw, (WS_T + lane * 8) | (w == NW - 1 ? 0 : OOB), a * 512, AUX_SC1);
  } else if constexpr (op == TCHK) {
    const bool ok = c.tg.y == c.tagt;
    const float d = wave_sum(ok ? (float)(int)(c.tg.x - c.tc) : (float)due_at(T_DELAY)), n = wave_sum(ok ? 1.f : 0.f);
    unsigned org = c.tc + (int)(d * (1.f / 64)) - due_at(T_AT[a]) + MARGIN;
    bool use = w == NW - 1 && n > 16.f;
    int pace = 0;
    if constexpr (a > 0) {
      const unsigned old = origin(lds, T_AT[a], org);
      const int debt = (int)(org - old) - (MARGIN - GATE_AHEAD + 3 * STAGGER / 8);
      use = use && debt > T_KEEP[a];
      pace = debt > T_SLOW[a] ? T_PACE : 0;
      org = old + min(3 * (debt - T_KEEP[a]), debt + T_OVER) + (pace ? T_EXTRA[a] : 0);
    }
    constexpr int last = T_AT[a] < N_K ? N_K + 4 : N_K + N_V;
    *(LDS_AS volatile v2u*)&lds[L_R + (use ? 0 : 2)] = v2u{org | 1u, (unsigned)(T_AT[a] | (last << 8) | (pace << 16))};
  } else if constexpr (op == GATE) {
    wait_until(c.gate + a);
  } else if constexpr (op == WAITS) {
    asm volatile("s_cmp_lt_i32 %0, %1\n\ts_cbranch_scc1 1f\n\ts_waitcnt vmcnt(%2)\n1:"
                 :: "s"(c.slow), "i"(a >> 8), "i"(a & 255) : "scc", "memory");
  } else if constexpr (op == WAITB) {
    asm volatile("s_cmp_ge_i32 %0, %1\n\ts_cbranch_scc1 1f\n\ts_waitcnt vmcnt(%2)\n1:"
                 :: "s"(c.boost), "i"(a >> 8), "i"(a & 255) : "scc", "memory");
  } else if constexpr (op == SETUP) {
    const int row = lane & 15, j = row & 7;
    const bool act = ((lane >> 4) & 1) == (row >> 3);
    const unsigned two = (lane >> 5) ? 0x0302u : 0x0100u;
    const unsigned s = (j & 1) ? ((two << 16) | 0x0c0cu) : (0x0c0c0000u | two);
#pragma unroll
    for (int i = 0; i < 4; ++i) c.sel[i] = (act && (j >> 1) == i) ? s : 0x0c0c0c0cu;
    c.acc = f32x4{0.f, 0.f, 0.f, 0.f};
    ((unsigned*)lds)[L_F + (lane & 1) * NW + w] = 0u;
    ((unsigned*)lds)[L_R + (lane & 1)] = 0u;
    asm volatile("" :: "s"(c.cpf[0]), "s"(c.cpf[1]), "s"(c.cpf[2]));
    c.tagt = c.epa * 0x9E3779B1u + 0x7F4A7C15u;
    __syncthreads();
    c.cpf[0] = *c.pv; c.cpf[1] = *c.po;
  } else if constexpr (op == QTR) {
#pragma unroll
    for (int i = 0; i < 4; ++i) *(v4u*)&lds[L_TQ + w * 1024 + i * 256 + lane * 4] = c.qb[i];
    COMPILER_FENCE();
    const float* base = &lds[L_TQ + w * 1024 + (lane >> 4) * 256 + ((lane >> 2) & 3) * 32 + (lane & 3) * 2];
#pragma unroll
    for (int ct = 0; ct < 4; ++ct)
#pragma unroll
      for (int h = 0; h < 2; ++h) c.qf[ct][h] = tr_read(base + h * 128 + ct * 8);
    COMPILER_FENCE();
  } else if constexpr (op == QMM) {
    f32x4 d[4];
#pragma unroll
    for (int ct = 0; ct < 4; ++ct)
      d[ct] = mfma(c.xv, v4u{c.qf[ct][0].x, c.qf[ct][0].y, c.qf[ct][1].x, c.qf[ct][1].y}, f32x4{0.f, 0.f, 0.f, 0.f});
    c.tag = c.epoch * 0x9E3779B1u + 0x7F4A7C15u;
    asm volatile("" :: "s"(c.cpf[0]), "s"(c.cpf[1]));
    asm volatile("" : "+s"(c.tag));
    const f32x4 lo = (lane & 16) ? d[1] : d[0], hi = (lane & 16) ? d[3] : d[2];
    *(f32x2*)&lds[L_RED + w * 128 + lane * 2] = (lane & 32) ? f32x2{hi.x, hi.y} : f32x2{lo.x, lo.y};
    flag_set(lds, 0, w);
  } else if constexpr (op == QPUB) {
    flag_wait(lds, 0, lane);
    f32x2 t = {0.f, 0.f};
#pragma unroll
    for (int i = 0; i < 7; ++i) t += *(const f32x2*)&lds[L_RED + i * 128 + lane * 2];
    const float nx = dpp<XOR1>(t.x), ny = dpp<XOR1>(t.y);
    __builtin_amdgcn_raw_buffer_store_b64(granule((lane & 1) ? ny : t.x, (lane & 1) ? t.y : nx, c.tag), c.rw, c.q_st, 0, AUX_SC1);
  } else if constexpr (op == QLD) {
    c.qg[a] = __builtin_amdgcn_raw_buffer_load_b128(c.rw, c.q_ld + a * 1024, 0, AUX_SC1);
  } else if constexpr (op == QCHK) {
    const f32x4 q = await16(c.qg[0], c.rw, c.q_ld, c.tag) + await16(c.qg[1], c.rw, c.q_ld + 1024, c.tag);
    *(v2u*)&lds[L_TQ + w * 1024 + lane * 2] = v2u{cvtpk(q.x, q.y), cvtpk(q.z, q.w)};
    COMPILER_FENCE();
    c.qr = *(const v4u*)&lds[L_TQ + w * 1024 + (lane & 31) * 4];
  } else if constexpr (op == CK) {
    const v4u k = c.kb[a], q = c.qr;
    float x = dot2(k.x, q.x, dot2(k.z, q.z, 0.f)) + dot2(k.y, q.y, dot2(k.w, q.w, 0.f));
    if constexpr ((a & 1) == 1) x = add16(c.xs[0], x);
    if constexpr ((a & 3) == 3) x = tstep<ROR8>(c.xs[1], x, lane & 8);
    if constexpr ((a & 7) == 7) x = tstep<XOR7>(c.xs[2], x, lane & 4);
    if constexpr ((a & 15) == 15) x = tstep<XOR2>(c.xs[3], x, lane & 2);
    if constexpr ((a & 31) == 31) (a == 31 ? c.s0 : c.s1) = tstep<XOR1>(c.xs[4], x, lane & 1) * (0.0625f * 1.4426950408889634f);
    else c.xs[__builtin_ctz(~a)] = x;
  } else if constexpr (op == SM1) {
    const float m = wave_max(fmaxf(c.s0, c.s1));
    const float p0 = __builtin_amdgcn_exp2f(c.s0 - m), p1 = __builtin_amdgcn_exp2f(c.s1 - m);
    __bf16* pw = (__bf16*)&lds[L_P + w * 64] + (__builtin_bitreverse32(lane) >> 26);
    pw[0] = (__bf16)p0; pw[64] = (__bf16)p1;
    COMPILER_FENCE();
    c.mw = m; c.s0 = p0 + p1;
    c.pp = *(const v4u*)&lds[L_P + w * 64];
  } else if constexpr (op == SM2) {
    const float l = wave_sum(c.s0);
    lds[L_ML + 2 * w + (lane & 1)] = (lane & 1) ? l : c.mw;
    flag_set(lds, 1, w);
  } else if constexpr (op == MLPUB) {
    flag_wait(lds, 1, lane);
    const f32x2 ml = *(const f32x2*)&lds[L_ML + 2 * (lane & 7)];
    const float M = max8(ml.x), L = sum8(ml.y * __builtin_amdgcn_exp2f(ml.x - M));
    const v4u g = {__float_as_uint(M), c.tag, __float_as_uint(L), c.tag};
    __builtin_amdgcn_raw_buffer_store_b128(g, c.rw, c.ml_st, 0, AUX_SC1);
  } else if constexpr (op == CV) {
    const unsigned pr = c.pp[a & 3];
    const v4u am = {__builtin_amdgcn_perm(0u, pr, c.sel[0]), __builtin_amdgcn_perm(0u, pr, c.sel[1]),
                    __builtin_amdgcn_perm(0u, pr, c.sel[2]), __builtin_amdgcn_perm(0u, pr, c.sel[3])};
    c.acc = mfma(am, c.vb[a], c.acc);
    if constexpr ((a & 3) == 3 && a + 1 < N_V) c.pp = *(const v4u*)&lds[L_P + w * 64 + (a + 1)];
  } else if constexpr (op == YPF) {
    c.pf = __builtin_amdgcn_raw_buffer_load_b32(c.rw, c.y_pf, 0, AUX_SC1);
  } else if constexpr (op == PEERLD) {
    c.peer = __builtin_amdgcn_raw_buffer_load_b128(c.rw, c.ml_ld, 0, AUX_SC1);
  } else if constexpr (op == PEERCHK) {
    asm volatile("" :: "v"(c.pf));
    await_tags(c.peer, c.rw, c.ml_ld, c.tag);
    const float m = __uint_as_float(c.peer.x);
    float M = fmaxf(m, dpp<XOR1>(m)); M = fmaxf(M, dpp<XOR2>(M));
    float L = __uint_as_float(c.peer.z) * __builtin_amdgcn_exp2f(m - M); L += dpp<XOR1>(L); L += dpp<XOR2>(L);
    c.scale = __builtin_amdgcn_exp2f(c.mw - M) / L;
  } else if constexpr (op == TRW) {
#pragma unroll
    for (int i = 0; i < 4; ++i) *(v4u*)&lds[L_T + w * 1024 + i * 256 + lane * 4] = c.ob[4 * a + i];
    COMPILER_FENCE();
  } else if constexpr (op == TRR) {
    const float* base = &lds[L_T + w * 1024 + (lane >> 4) * 256 + ((lane >> 2) & 3) * 32 + (lane & 3) * 2];
#pragma unroll
    for (int ct = 0; ct < 4; ++ct)
#pragma unroll
      for (int h = 0; h < 2; ++h) c.of[a][ct][h] = tr_read(base + h * 128 + ct * 8);
    COMPILER_FENCE();
  }
  __builtin_amdgcn_sched_barrier(0);
}
template <int B, int... I> __device__ __forceinline__ void run_seq(Ctx& c, std::integer_sequence<int, I...>) { (step<B + I>(c), ...); }
template <int B, int E> __device__ __forceinline__ void run(Ctx& c) { run_seq<B>(c, std::make_integer_sequence<int, E - B>{}); }

__global__ void __launch_bounds__(NTHR, 2)
mha_decode(const __bf16* __restrict__ xA, const __bf16* __restrict__ kA, const __bf16* __restrict__ vA,
           const __bf16* __restrict__ wqA, const __bf16* __restrict__ woA,
           const __bf16* __restrict__ xB, const __bf16* __restrict__ kB, const __bf16* __restrict__ vB,
           const __bf16* __restrict__ wqB, const __bf16* __restrict__ woB, __bf16* out, unsigned char* ws) {
  __shared__ __attribute__((aligned(16))) float lds[L_END];
  Ctx c;
  const unsigned long long tstart = __builtin_amdgcn_s_memrealtime();
  __builtin_amdgcn_sched_barrier(0);
  const int bid = blockIdx.x, rk = bid >> 3, bh = (bid & 7) * 4 + (rk >> 3), br = bh >> 4, head = bh & 15;
  const int b = (rk >> 2) & 1, split = rk & 3, grp = br * 2 + b, prod = head * 4 + split;
  const int tid = threadIdx.x, lane = tid & 63, w = __builtin_amdgcn_readfirstlane(tid >> 6);
  const int off7 = w < 7 ? 0 : OOB;
  c.w = w; c.lane = lane; c.lds = lds;
  c.t0 = (unsigned)tstart;
  c.vo_q = ((lane >> 3) * (NH * DH * 2) + (lane & 7) * 16) | off7;
  c.so_q = ((224 * b + 32 * w) * NH * DH + head * DH + split * 64) * 2;
  c.vo_kv = lane * 16;
  c.so_kv = (b * NH + head) * (SK * DH * 2) + (((w & 1) << 11) | ((w & 2) << 12) | ((w & 4) << 14) | ((split & 1) << 14) | ((split & 2) << 18)) +
            (((head & 2) ? 0x68000 : 0) ^ ((head & 8) << 12) ^ ((((head >> 2) ^ b) & 1) << 20));
  c.vo_o = ((lane >> 3) * (DM * 2) + w * 128 + (lane & 7) * 16) | off7;
  c.so_o = head * DH * DM * 2;
  c.orot = rk & 7;
  int ep_off = br * 256;
  asm volatile("" : "+s"(c.so_q), "+s"(c.so_kv), "+s"(c.so_o), "+s"(c.orot), "+s"(ep_off), "+v"(c.vo_q), "+v"(c.vo_kv), "+v"(c.vo_o)
               : "s"(tstart));
  __builtin_amdgcn_sched_barrier(0);
  c.rq = __builtin_amdgcn_make_buffer_rsrc((void*)(br ? wqB : wqA), 0, DM * NH * DH * 2, RSRC_W3);
  c.ro = __builtin_amdgcn_make_buffer_rsrc((void*)(br ? woB : woA), 0, NH * DH * DM * 2, RSRC_W3);
  c.rk = __builtin_amdgcn_make_buffer_rsrc((void*)(br ? kB : kA), 0, 2 * NH * SK * DH * 2, RSRC_W3);
  run<0, 1>(c);
  c.epoch = *(const CONST_AS unsigned*)(ws + ep_off);
  c.epa = *(const CONST_AS unsigned*)ws;
  const unsigned long long pc = __builtin_amdgcn_s_getpc() & ~63ull;
  c.cpf[0] = *(const CONST_AS unsigned*)(pc + (rk * NW + w) * 64);
  c.cpf[1] = *(const CONST_AS unsigned*)(pc + (256 + min(rk * NW + w, 121)) * 64);
  const int touch = (b * NH + head) * (SK * DH) + (split * NW + w) * 32768;
  c.cpf[2] = *(const CONST_AS unsigned*)((br ? kB : kA) + touch);
  run<1, N_FIRST>(c);
  int br2 = br;
  asm volatile("" : "+s"(br2));
  c.rx = __builtin_amdgcn_make_buffer_rsrc((void*)(br2 ? xB : xA), 0, 2 * DM * 2, RSRC_W3);
  c.vo_x = (((lane & 1) * DM + 224 * b + 32 * w + 8 * (lane >> 4)) * 2) | off7;
  c.rv = __builtin_amdgcn_make_buffer_rsrc((void*)(br2 ? vB : vA), 0, 2 * NH * SK * DH * 2, RSRC_W3);
  c.pv = (const CONST_AS unsigned*)((br2 ? vB : vA) + touch);
  c.po = (const CONST_AS unsigned*)out;
  c.rw = __builtin_amdgcn_make_buffer_rsrc((void*)ws, 0, WS_END, RSRC_W3);
  const rsrc_t rout = __builtin_amdgcn_make_buffer_rsrc((void*)out, 0, 4 * DM * 2, RSRC_W3);
  c.q_st = (WS_Q + bh * 4096 + (lane & 1) * 2048 + b * 1024 + split * 256 + (lane & ~1) * 4) | (w == 0 ? 0 : OOB);
  c.q_ld = WS_Q + bh * 4096 + b * 2048 + lane * 16;
  c.ml_st = (WS_ML + (grp * NH + head) * 128 + split * 16) | (tid == 0 ? 0 : OOB);
  c.ml_ld = WS_ML + (grp * NH + head) * 128 + (lane & 3) * 16;
  c.t_st = (WS_T + ((bid >> 5) * 8 + (bid & 7)) * 8) | (tid == 64 * 3 && ((rk + (rk >> 2)) & 3) == 0 ? 0 : OOB);
  c.y_pf = (WS_PKT + (grp * 64 + prod) * PKT_BYTES + lane * 128) | (tid < PKT_BYTES / 128 ? 0 : OOB);

  run<N_FIRST, S.n - N_LAST>(c);

  const int pk = WS_PKT + grp * 64 * PKT_BYTES;
  const int dq = 32 * (lane >> 5) + 2 * (lane & 15) + ((lane >> 4) & 1);
  int a_wr = (L_RED + w * A_STR + ((dq - 8 * c.orot) & 63)) * 4, a_rd = (L_RED + (lane & 15) * A_STR + 4 * (lane >> 4)) * 4;
  int idx = (lane & 8) ? 0xEEEE : 0x4444;
  int y_off = (pk + prod * PKT_BYTES + tid * 4) | ((lane & 1) ? OOB : 0);
  int p_off = pk + (lane >> 2) * PKT_BYTES + prod * 64 + (lane & 3) * 16;
  int o_off = ((b * (2 * DM) + br * DM + prod * 16 + (lane & 3) * 4 + (lane >> 4)) * 2) | ((lane & 12) ? OOB : 0);
  int e_off = (prod == 0 && b == 0 && lane == 0) ? ep_off : OOB;
  asm volatile("" : "+v"(a_wr), "+v"(a_rd), "+v"(idx), "+v"(y_off), "+v"(p_off), "+v"(o_off), "+v"(e_off));
  unsigned due = origin(lds, N_K + N_V - 1, 0u) + due_at(N_K + N_V - 1) + END_AFTER;
  asm volatile("" : "+s"(due));
  f32x4 y[4];
#pragma unroll
  for (int ct = 0; ct < 4; ++ct) { y[ct] = f32x4{0.f, 0.f, 0.f, 0.f}; asm volatile("" : "+v"(y[ct])); }

  run<S.n - N_LAST, S.n>(c);
  LDS_AS unsigned char* const lb = (LDS_AS unsigned char*)lds;
  *(LDS_AS unsigned*)(lb + a_wr) = cvtpk(c.acc.x * c.scale, c.acc.y * c.scale);
  *(LDS_AS unsigned*)(lb + a_wr + 8 * A_STR * 4) = cvtpk(c.acc.z * c.scale, c.acc.w * c.scale);
  __syncthreads();
  if (__builtin_expect(w == NW - 1, 0)) {
    if (prod < DM / 16) {
      unsigned cnt;
      asm volatile(
          "s_mov_b32 %0, 0\n"
          "1:\n\t"
          "s_memrealtime vcc\n\t"
          "s_waitcnt lgkmcnt(0)\n\t"
          "s_sub_u32 vcc_lo, %9, vcc_lo\n\t"
          "s_cmp_lt_u32 vcc_lo, 0x100\n\t"
          "s_cbranch_scc1 1b\n"
          "2:\n\t"
          "buffer_load_dwordx4 v[0:3], %1, %4, 0 offen sc1\n\t"
          "buffer_load_dwordx4 v[4:7], %1, %4, %10 offen sc1\n\t"
          "buffer_load_dwordx4 v[8:11], %1, %4, %11 offen sc1\n\t"
          "buffer_load_dwordx4 v[12:15], %1, %4, %12 offen sc1\n\t"
          "s_add_u32 %0, %0, 1\n\t"
          "s_waitcnt vmcnt(3)\n\t"
          "v_xor_b32 v0, %6, v0\n\tv_xor_b32 v1, %7, v1\n\tv_xor_b32 v2, %6, v2\n\tv_xor_b32 v3, %7, v3\n\t"
          "v_or3_b32 v16, v0, v1, v2\n\t"
          "s_waitcnt vmcnt(2)\n\t"
          "v_xor_b32 v4, %6, v4\n\tv_xor_b32 v5, %7, v5\n\tv_xor_b32 v6, %6, v6\n\tv_xor_b32 v7, %7, v7\n\t"
          "v_or3_b32 v16, v16, v3, v4\n\tv_or3_b32 v17, v5, v6, v7\n\t"
          "v_pk_add_f32 v[0:1], v[0:1], v[4:5]\n\tv_pk_add_f32 v[2:3], v[2:3], v[6:7]\n\t"
          "s_waitcnt vmcnt(1)\n\t"
          "v_xor_b32 v8, %6, v8\n\tv_xor_b32 v9, %7, v9\n\tv_xor_b32 v10, %6, v10\n\tv_xor_b32 v11, %7, v11\n\t"
          "v_or3_b32 v16, v16, v8, v9\n\tv_or3_b32 v17, v17, v10, v11\n\t"
          "s_waitcnt vmcnt(0)\n\t"
          "v_xor_b32 v12, %6, v12\n\tv_xor_b32 v13, %7, v13\n\tv_xor_b32 v14, %6, v14\n\tv_xor_b32 v15, %7, v15\n\t"
          "v_or3_b32 v16, v16, v12, v13\n\tv_or3_b32 v17, v17, v14, v15\n\t"
          "v_pk_add_f32 v[8:9], v[8:9], v[12:13]\n\tv_pk_add_f32 v[10:11], v[10:11], v[14:15]\n\t"
          "v_or_b32 v16, v16, v17\n\t"
          "v_pk_add_f32 v[0:1], v[0:1], v[8:9]\n\tv_pk_add_f32 v[2:3], v[2:3], v[10:11]\n\t"
          "v_and_b32 v16, 0xff, v16\n\t"
          "v_cmp_ne_u32 vcc, 0, v16\n\t"
          "v_permlane32_swap_b32 v0, v2\n\tv_permlane32_swap_b32 v1, v3\n\t"
          "s_nop 0\n\t"
          "v_add_f32 v0, v0, v2\n\tv_add_f32 v1, v1, v3\n\t"
          "s_nop 1\n\t"
          "v_permlane16_swap_b32 v0, v1\n\t"
          "s_nop 0\n\t"
          "v_add_f32 v0, v0, v1\n\t"
          "s_nop 1\n\t"
          "v_add_f32_dpp v0, v0, v0 row_ror:4 row_mask:0xf bank_mask:0xf\n\t"
          "s_nop 1\n\t"
          "v_add_f32_dpp v0, v0, v0 row_ror:8 row_mask:0xf bank_mask:0xf\n\t"
          "s_cmp_lt_u32 %0, %13\n\t"
          "v_cvt_pk_bf16_f32 v0, v0, v0\n\t"
          "s_cbranch_vccz 4f\n\t"
          "s_cbranch_scc1 2b\n\t"
          "v_mov_b32 v0, 0x7fc0\n"
          "4:\n\t"
          "buffer_store_short v0, %2, %5, 0 offen\n\t"
          "v_mov_b32 v1, %8\n\t"
          "buffer_store_dword v1, %3, %4, 0 offen sc1"
          : "=&s"(cnt)
          : "v"(p_off), "v"(o_off), "v"(e_off), "s"(c.rw), "s"(rout), "s"(c.tag & 255u), "s"((c.tag >> 8) & 255u), "s"(c.tag), "s"(due),
            "s"(16 * PKT_BYTES), "s"(32 * PKT_BYTES), "s"(48 * PKT_BYTES), "s"(SPIN_CAP)
          : "scc", "vcc", "memory", "v0", "v1", "v2", "v3", "v4", "v5", "v6", "v7", "v8", "v9", "v10", "v11", "v12", "v13", "v14", "v15", "v16", "v17");
    }
    __builtin_amdgcn_endpgm();
  }
  v4u am[4];
#pragma unroll
  for (int ks = 0; ks < 4; ++ks) am[ks] = *(LDS_AS v4u*)(lb + a_rd + 64 * ks);
  __builtin_amdgcn_sched_barrier(0);
#pragma unroll
  for (int ks = 0; ks < 4; ++ks)
#pragma unroll
    for (int ct = 0; ct < 4; ++ct) {
      const v2u b0 = c.of[2 * ks][ct][0], b1 = c.of[2 * ks][ct][1], b2 = c.of[2 * ks + 1][ct][0], b3 = c.of[2 * ks + 1][ct][1];
      const v8u bm = {b0.x, b0.y, b1.x, b1.y, b2.x, b2.y, b3.x, b3.y};
      y[ct] = __builtin_amdgcn_smfmac_f32_16x16x64_bf16(__builtin_bit_cast(bf16x8, am[ks]), __builtin_bit_cast(bf16x16, bm), y[ct], idx, 0, 0);
    }
  float sm[4];
#pragma unroll
  for (int ct = 0; ct < 4; ++ct) sm[ct] = (y[ct].x + y[ct].y) + (y[ct].z + y[ct].w);
  const float yv = add32(add16(sm[0], sm[1]), add16(sm[2], sm[3]));
  __builtin_amdgcn_raw_buffer_store_b64(granule(yv, dpp<XOR1>(yv), c.tag), c.rw, y_off, 0, AUX_SC1);
}

}

extern "C" void kernel_launch(void* const* d_in, const int* in_sizes, int n_in, void* d_out, int out_size, void* d_ws,
                              size_t ws_size, hipStream_t stream) {
  (void)in_sizes; (void)n_in; (void)out_size; (void)ws_size;
  mha_decode<<<256, NTHR, 0, stream>>>((const __bf16*)d_in[0], (const __bf16*)d_in[1], (const __bf16*)d_in[2],
                                       (const __bf16*)d_in[3], (const __bf16*)d_in[4], (const __bf16*)d_in[5],
                                       (const __bf16*)d_in[6], (const __bf16*)d_in[7], (const __bf16*)d_in[8],
                                       (const __bf16*)d_in[9], (__bf16*)d_out, (unsigned char*)d_ws);
}
```

```cpp
#include <hip/hip_runtime.h>
#include <hip/hip_bf16.h>
#include <utility>

namespace {

typedef unsigned v2u __attribute__((ext_vector_type(2)));
typedef unsigned v4u __attribute__((ext_vector_type(4)));
typedef unsigned v8u __attribute__((ext_vector_type(8)));
typedef float f32x2 __attribute__((ext_vector_type(2)));
typedef float f32x4 __attribute__((ext_vector_type(4)));
typedef __bf16 bf16x2 __attribute__((ext_vector_type(2)));
typedef __bf16 bf16x8 __attribute__((ext_vector_type(8)));
typedef __bf16 bf16x16 __attribute__((ext_vector_type(16)));
typedef short i16x4 __attribute__((ext_vector_type(4)));
typedef __amdgpu_buffer_rsrc_t rsrc_t;
#define LDS_AS __attribute__((address_space(3)))
#define CONST_AS __attribute__((address_space(4)))
#define COMPILER_FENCE() asm volatile("" ::: "memory")

constexpr int DM = 448, NH = 16, SK = 4096, DH = 256;
constexpr int NW = 8, NTHR = NW * 64;
constexpr int N_Q = 4, N_K = 64, N_V = 64, N_O = 32;
constexpr unsigned SPIN_CAP = 1u << 20;
constexpr int RSRC_W3 = 0x00020000, AUX_NT = 2, AUX_SC1 = 16, OOB = 0x40000000;
constexpr int WS_PKT = 4096, PKT_BYTES = DM * 4, WS_Q = WS_PKT + 256 * PKT_BYTES, WS_ML = WS_Q + 64 * 2048,
              WS_T = WS_ML + 64 * 128, WS_END = WS_T + 3 * 512;
constexpr int A_STR = 68;
constexpr int L_RED = 0, L_T = L_RED + 16 * A_STR, L_TQ = L_T + NW * 1024, L_ML = L_TQ + NW * 1024, L_P = L_ML + 2 * NW,
              L_F = L_P + NW * 64, L_R = L_F + NW * 2, L_END = L_R + 4;

constexpr int WIN = 3;
constexpr int O_FIRST = 4;
constexpr int O_START = 16, O_EVERY = 3;
constexpr int X_AT = 2, QMM_AT = 6, QPUB_AT = 9, QLD_AT = 17, QCHK_AT = 21, YPF_AT = 100;
constexpr int N_FIRST = N_Q + O_FIRST + 1, N_LAST = 3;
constexpr int TAU = 431, ORIGIN0 = 245, MARGIN = 30, GATE_FROM = 23, GATE_AHEAD = 21, STAGGER = 24, END_AFTER = 126;
constexpr int LAG_FROM = 7, LAG_EVERY = 4, GAIN_UP = 9;
constexpr int T_AT[3] = {10, 38, 84}, T_DELAY[3] = {8, 6, 6}, T_KEEP[3] = {0, 14, 8}, T_OVER = 12, T_SLOW[3] = {0, 32, 36},
              T_EXTRA[3] = {0, 18, 40}, T_PACE = 42;
constexpr int due_at(int j) { return j * TAU / 16; }

enum Op : int { IQ, IK, IV, IO, IX, QPUB, QLD, MLPUB, YPF, TPUB, TLD, PEERLD,
                WAITV, WAITB, GATE, LAG, TCHK, SETUP, QTR, QMM, QCHK, CK, SM1, SM2, CV, PEERCHK, TRW, TRR };
struct Sched { int n; int op[900]; int a[900]; };
constexpr Sched make_sched() {
  Sched s{};
  int vmk[256] = {}, vmi[256] = {}, nvm = 0;
  auto emit = [&](int op, int a) {
    s.op[s.n] = op; s.a[s.n] = a; ++s.n;
    if (op <= PEERLD) { vmk[nvm] = op; vmi[nvm] = a; ++nvm; }
  };
  int no = 0, nk = 0, nv = 0, nw = 0, nr = 0, sm = 0;
  int lk = 0, lv = 0, lo = 0;
  for (int j = 0; j < N_Q; ++j) emit(IQ, j);
  while (no < O_FIRST) emit(IO, no++);
  for (int j = 0; j < N_K + N_V; ++j) {
    if (j > 0) {
      int p = nvm, seen = 0;
      while (seen < WIN) { --p; if (vmk[p] <= IV) ++seen; }
      if (j > LAG_FROM) {
        const int k = (j - LAG_FROM - 1) % LAG_EVERY + 1;
        int p2 = p;
        do --p2; while (vmk[p2] > IV);
        emit(WAITV, nvm - 1 - p2);
        emit(WAITB, (nvm - 1 - p) | (k << 8));
        if (j > GATE_FROM) emit(GATE, due_at(j));
        p = p2;
      } else emit(WAITV, nvm - 1 - p);
      if (vmk[p] == IK) lk = vmi[p] + 1;
      if (vmk[p] == IV) { lk = N_K; lv = vmi[p] + 1; }
      for (int t = 0; t < p; ++t) if (vmk[t] == IO) lo = vmi[t] + 1;
    }
    if (j < N_K) emit(IK, j); else emit(IV, j - N_K);
    if (j == 0) emit(SETUP, 0);
    if (j >= O_START && (j - O_START) % O_EVERY == 0 && no < N_O) emit(IO, no++);
    if (j >= LAG_FROM && (j - LAG_FROM) % LAG_EVERY == 0 && j + 1 < N_K + N_V) emit(LAG, j);
    if (j == X_AT) emit(IX, 0);
    if (j == YPF_AT) emit(YPF, 0);
    for (int t = 0; t < 3; ++t) {
      if (j == T_AT[t]) emit(TPUB, t);
      if (j == T_AT[t] + T_DELAY[t]) emit(TLD, t);
      if (j == T_AT[t] + T_DELAY[t] + 4) emit(TCHK, t);
    }
    if (j == QMM_AT - 1) emit(QTR, 0);
    if (j == QMM_AT) emit(QMM, 0);
    if (j == QPUB_AT) emit(QPUB, 0);
    if (j == QLD_AT) { emit(QLD, 0); emit(QLD, 1); }
    if (j == QCHK_AT) emit(QCHK, 0);
    else if (j > QCHK_AT && nk < N_K) { for (int t = 0; t < 3 && nk < lk; ++t) emit(CK, nk++); }
    else if (nk == N_K && sm < 2) { emit(sm == 0 ? SM1 : SM2, 0); ++sm; }
    else if (sm == 2) {
      for (int t = 0; t < 3 && nv < lv; ++t) {
        emit(CV, nv++);
        if (nv == 12) emit(MLPUB, 0);
        if (nv == N_V - 20) emit(PEERLD, 0);
        if (nv == N_V - 10) emit(PEERCHK, 0);
      }
    }
    if (nr < nw) emit(TRR, nr++);
    else if (4 * nw + 4 <= lo) emit(TRW, nw++);
  }
  if (no != N_O || nr != N_O / 4 || nv < N_V - 10 || nv > N_V - N_LAST) s.n = -1;
  else while (nv < N_V) emit(CV, nv++);
  return s;
}
constexpr Sched S = make_sched();
static_assert(S.n > 0, "the schedule must finish everything but the last V loads inside the stream");

constexpr int pair_off(int g) { return ((g & 1) << 12) | ((g & 2) << 14) | ((g & 12) << 15) | ((g & 16) << 16); }

__device__ __forceinline__ float dot2(unsigned a, unsigned b, float c) {
  return __builtin_amdgcn_fdot2_f32_bf16(__builtin_bit_cast(bf16x2, a), __builtin_bit_cast(bf16x2, b), c, false);
}
__device__ __forceinline__ unsigned cvtpk(float lo, float hi) {
  const bf16x2 r = {(__bf16)lo, (__bf16)hi};
  return __builtin_bit_cast(unsigned, r);
}
template <int CTRL> __device__ __forceinline__ float dpp(float x) {
  return __builtin_bit_cast(float, __builtin_amdgcn_mov_dpp(__builtin_bit_cast(int, x), CTRL, 0xf, 0xf, true));
}
constexpr int XOR1 = 0xB1, XOR2 = 0x4E, XOR7 = 0x141, XOR15 = 0x140, ROR8 = 0x128;
__device__ __forceinline__ float sum8(float x) { x += dpp<XOR1>(x); x += dpp<XOR2>(x); x += dpp<XOR7>(x); return x; }
__device__ __forceinline__ float max8(float x) {
  x = fmaxf(x, dpp<XOR1>(x)); x = fmaxf(x, dpp<XOR2>(x)); x = fmaxf(x, dpp<XOR7>(x));
  return x;
}
__device__ __forceinline__ float add16(float a, float b) {
  const auto s = __builtin_amdgcn_permlane16_swap(__float_as_uint(a), __float_as_uint(b), false, false);
  return __uint_as_float(s[0]) + __uint_as_float(s[1]);
}
__device__ __forceinline__ float add32(float a, float b) {
  const auto s = __builtin_amdgcn_permlane32_swap(__float_as_uint(a), __float_as_uint(b), false, false);
  return __uint_as_float(s[0]) + __uint_as_float(s[1]);
}
template <int CTRL> __device__ __forceinline__ float tstep(float lo, float hi, bool sel) {
  const float a = lo + dpp<CTRL>(lo), b = hi + dpp<CTRL>(hi);
  return sel ? b : a;
}
__device__ __forceinline__ float wave_sum(float x) {
  x = sum8(x); x += dpp<XOR15>(x); x = add16(x, x);
  return add32(x, x);
}
__device__ __forceinline__ float wave_max(float x) {
  x = max8(x); x = fmaxf(x, dpp<XOR15>(x));
  const auto s = __builtin_amdgcn_permlane16_swap(__float_as_uint(x), __float_as_uint(x), false, false);
  x = fmaxf(__uint_as_float(s[0]), __uint_as_float(s[1]));
  const auto t = __builtin_amdgcn_permlane32_swap(__float_as_uint(x), __float_as_uint(x), false, false);
  return fmaxf(__uint_as_float(t[0]), __uint_as_float(t[1]));
}
__device__ __forceinline__ f32x4 mfma(v4u a, v4u b, f32x4 c) {
  return __builtin_amdgcn_mfma_f32_16x16x32_bf16(__builtin_bit_cast(bf16x8, a), __builtin_bit_cast(bf16x8, b), c, 0, 0, 0);
}
__device__ __forceinline__ v2u tr_read(const float* p) {
  return __builtin_bit_cast(v2u, __builtin_amdgcn_ds_read_tr16_b64_v4i16((LDS_AS i16x4*)p));
}
__device__ __forceinline__ void await_tags(v4u& g, rsrc_t r, int off, unsigned tag) {
  if (__builtin_expect(!__all((g.y == tag) & (g.w == tag)), 0)) {
    unsigned spins = 0;
    bool ok;
#pragma unroll 1
    do {
      COMPILER_FENCE();
      g = __builtin_amdgcn_raw_buffer_load_b128(r, off, 0, AUX_SC1);
      ok = __all((g.y == tag) & (g.w == tag));
    } while (!ok && ++spins < SPIN_CAP);
    if (!ok) g.x = 0x7fc00000u;
  }
}
__device__ __forceinline__ v2u granule(float a, float b, unsigned tag) {
  return v2u{(__float_as_uint(a) & ~255u) | (tag & 255u), (__float_as_uint(b) & ~255u) | ((tag >> 8) & 255u)};
}
__device__ __forceinline__ bool untag(v4u& g, unsigned tag) {
  const unsigned t0 = tag & 255u, t1 = (tag >> 8) & 255u;
  g ^= v4u{t0, t1, t0, t1};
  return __all(((g.x | g.y | g.z | g.w) & 255u) == 0u);
}
__device__ __forceinline__ f32x4 await16(v4u g, rsrc_t r, int off, unsigned tag) {
  if (__builtin_expect(!untag(g, tag), 0)) {
    unsigned spins = 0;
    bool ok;
#pragma unroll 1
    do {
      COMPILER_FENCE();
      g = __builtin_amdgcn_raw_buffer_load_b128(r, off, 0, AUX_SC1);
      ok = untag(g, tag);
    } while (!ok && ++spins < SPIN_CAP);
    if (!ok) g.x = 0x7fc00000u;
  }
  return __builtin_bit_cast(f32x4, g);
}
__device__ __forceinline__ void flag_set(float* lds, int k, int w) {
  asm volatile("s_waitcnt lgkmcnt(0)" ::: "memory");
  ((LDS_AS volatile unsigned*)lds)[L_F + k * NW + w] = 1u;
}
__device__ __forceinline__ void flag_wait(float* lds, int k, int lane) {
  while (!__all(((LDS_AS volatile unsigned*)lds)[L_F + k * NW + (lane & 7)] != 0u)) {}
  COMPILER_FENCE();
}
__device__ __forceinline__ void wait_until(unsigned t) {
  asm volatile("1:\n\ts_memrealtime vcc\n\ts_waitcnt lgkmcnt(0)\n\ts_sub_u32 vcc_lo, %0, vcc_lo\n\ts_cmp_lt_u32 vcc_lo, 0x100\n\ts_cbranch_scc1 1b"
               :: "s"(t) : "scc", "vcc", "memory");
}
__device__ __forceinline__ unsigned origin(float* lds, int j, unsigned none) {
  v2u v = *(LDS_AS volatile v2u*)&lds[L_R];
  asm volatile("" : "+v"(v));
  const unsigned org = __builtin_amdgcn_readfirstlane(v.x), x = __builtin_amdgcn_readfirstlane(v.y);
  return org ? org + ((min(j, (int)(x >> 8) & 255) - (int)(x & 255u)) * (int)(x >> 16) >> 4) : none;
}

struct Ctx {
  rsrc_t rq, rk, rv, ro, rx, rw;
  float* lds;
  int w, lane, vo_q, vo_kv, vo_o, vo_x, so_q, so_kv, so_o, orot, q_st, q_ld, ml_st, ml_ld, y_pf, boost, t_st;
  unsigned tc, tagt, epa, gate;
  v2u tg;
  const CONST_AS unsigned *pv, *po;
  unsigned t0, epoch, tag, pf, cpf[3], sel[4];
  v4u qb[N_Q], kb[N_K], vb[N_V], ob[N_O], xv, qg[2], qr, pp, peer;
  v2u qf[4][2];
  v2u of[8][4][2];
  f32x4 acc;
  float xs[5], s0, s1, mw, scale;
};

template <int I> __device__ __forceinline__ void step(Ctx& c) {
  constexpr int op = S.op[I], a = S.a[I];
  float* const lds = c.lds;
  const int w = c.w, lane = c.lane;
  if constexpr (op == IQ) {
    c.qb[a] = __builtin_amdgcn_raw_buffer_load_b128(c.rq, c.vo_q, c.so_q + 8 * a * (NH * DH * 2), AUX_NT);
  } else if constexpr (op == IK) {
    c.kb[a] = __builtin_amdgcn_raw_buffer_load_b128(c.rk, c.vo_kv + (a & 1) * 1024, c.so_kv ^ pair_off(a >> 1), AUX_NT);
  } else if constexpr (op == IV) {
    c.vb[a] = __builtin_amdgcn_raw_buffer_load_b128(c.rv, c.vo_kv + (a & 1) * 1024, c.so_kv ^ pair_off(a >> 1), AUX_NT);
  } else if constexpr (op == IO) {
    c.ob[a] = __builtin_amdgcn_raw_buffer_load_b128(c.ro, c.vo_o, c.so_o + ((8 * a + 32 * c.orot) & 255) * (DM * 2), 0);
  } else if constexpr (op == IX) {
    c.xv = __builtin_amdgcn_raw_buffer_load_b128(c.rx, c.vo_x, 0, 0);
  } else if constexpr (op == WAITV) {
    __builtin_amdgcn_s_waitcnt((a & 15) | ((a >> 4) << 14) | 0x0F70);
  } else if constexpr (op == LAG) {
    const unsigned none = c.t0 - 65536, o = origin(lds, a + 2, none);
    const int late = (int)((unsigned)__builtin_amdgcn_s_memrealtime() - (o == none ? c.t0 + ORIGIN0 : o)) - due_at(a);
    c.gate = o - GATE_AHEAD + (w * STAGGER >> 3);
    c.boost = late * GAIN_UP >> 4;
  } else if constexpr (op == TPUB) {
    c.tc = (unsigned)__builtin_amdgcn_s_memrealtime();
    __builtin_amdgcn_raw_buffer_store_b64(v2u{c.tc, c.tagt}, c.rw, c.t_st, a * 512, AUX_SC1);
  } else if constexpr (op == TLD) {
    c.tg = __builtin_amdgcn_raw_buffer_load_b64(c.rw, (WS_T + lane * 8) | (w == NW - 1 ? 0 : OOB), a * 512, AUX_SC1);
  } else if constexpr (op == TCHK) {
    const bool ok = c.tg.y == c.tagt;
    const float d = wave_sum(ok ? (float)(int)(c.tg.x - c.tc) : (float)due_at(T_DELAY[a])), n = wave_sum(ok ? 1.f : 0.f);
    unsigned org = c.tc + (int)(d * (1.f / 64)) - due_at(T_AT[a]) + MARGIN;
    bool use = w == NW - 1 && n > 16.f;
    int pace = 0;
    if constexpr (a > 0) {
      const unsigned old = origin(lds, T_AT[a], org);
      const int debt = (int)(org - old) - (MARGIN - GATE_AHEAD + 3 * STAGGER / 8);
      use = use && debt > T_KEEP[a];
      pace = debt > T_SLOW[a] ? T_PACE : 0;
      org = old + min(3 * (debt - T_KEEP[a]), debt + T_OVER) + (pace ? T_EXTRA[a] : 0);
    }
    constexpr int last = T_AT[a] < N_K ? N_K + 4 : N_K + N_V;
    *(LDS_AS volatile v2u*)&lds[L_R + (use ? 0 : 2)] = v2u{org | 1u, (unsigned)(T_AT[a] | (last << 8) | (pace << 16))};
  } else if constexpr (op == GATE) {
    wait_until(c.gate + a);
  } else if constexpr (op == WAITB) {
    asm volatile("s_cmp_ge_i32 %0, %1\n\ts_cbranch_scc1 1f\n\ts_waitcnt vmcnt(%2)\n1:"
                 :: "s"(c.boost), "i"(a >> 8), "i"(a & 255) : "scc", "memory");
  } else if constexpr (op == SETUP) {
    const int row = lane & 15, j = row & 7;
    const bool act = ((lane >> 4) & 1) == (row >> 3);
    const unsigned two = (lane >> 5) ? 0x0302u : 0x0100u;
    const unsigned s = (j & 1) ? ((two << 16) | 0x0c0cu) : (0x0c0c0000u | two);
#pragma unroll
    for (int i = 0; i < 4; ++i) c.sel[i] = (act && (j >> 1) == i) ? s : 0x0c0c0c0cu;
    c.acc = f32x4{0.f, 0.f, 0.f, 0.f};
    ((unsigned*)lds)[L_F + (lane & 1) * NW + w] = 0u;
    ((unsigned*)lds)[L_R + (lane & 1)] = 0u;
    asm volatile("" :: "s"(c.cpf[0]), "s"(c.cpf[1]), "s"(c.cpf[2]));
    c.tagt = c.epa * 0x9E3779B1u + 0x7F4A7C15u;
    __syncthreads();
    c.cpf[0] = *c.pv; c.cpf[1] = *c.po;
  } else if constexpr (op == QTR) {
#pragma unroll
    for (int i = 0; i < 4; ++i) *(v4u*)&lds[L_TQ + w * 1024 + i * 256 + lane * 4] = c.qb[i];
    COMPILER_FENCE();
    const float* base = &lds[L_TQ + w * 1024 + (lane >> 4) * 256 + ((lane >> 2) & 3) * 32 + (lane & 3) * 2];
#pragma unroll
    for (int ct = 0; ct < 4; ++ct)
#pragma unroll
      for (int h = 0; h < 2; ++h) c.qf[ct][h] = tr_read(base + h * 128 + ct * 8);
    COMPILER_FENCE();
  } else if constexpr (op == QMM) {
    f32x4 d[4];
#pragma unroll
    for (int ct = 0; ct < 4; ++ct)
      d[ct] = mfma(c.xv, v4u{c.qf[ct][0].x, c.qf[ct][0].y, c.qf[ct][1].x, c.qf[ct][1].y}, f32x4{0.f, 0.f, 0.f, 0.f});
    c.tag = c.epoch * 0x9E3779B1u + 0x7F4A7C15u;
    asm volatile("" :: "s"(c.cpf[0]), "s"(c.cpf[1]));
    asm volatile("" : "+s"(c.tag));
    const f32x4 lo = (lane & 16) ? d[1] : d[0], hi = (lane & 16) ? d[3] : d[2];
    *(f32x2*)&lds[L_RED + w * 128 + lane * 2] = (lane & 32) ? f32x2{hi.x, hi.y} : f32x2{lo.x, lo.y};
    flag_set(lds, 0, w);
  } else if constexpr (op == QPUB) {
    flag_wait(lds, 0, lane);
    f32x2 t = {0.f, 0.f};
#pragma unroll
    for (int i = 0; i < 7; ++i) t += *(const f32x2*)&lds[L_RED + i * 128 + lane * 2];
    const float nx = dpp<XOR1>(t.x), ny = dpp<XOR1>(t.y);
    __builtin_amdgcn_raw_buffer_store_b64(granule((lane & 1) ? ny : t.x, (lane & 1) ? t.y : nx, c.tag), c.rw, c.q_st, 0, AUX_SC1);
  } else if constexpr (op == QLD) {
    c.qg[a] = __builtin_amdgcn_raw_buffer_load_b128(c.rw, c.q_ld + a * 1024, 0, AUX_SC1);
  } else if constexpr (op == QCHK) {
    const f32x4 q = await16(c.qg[0], c.rw, c.q_ld, c.tag) + await16(c.qg[1], c.rw, c.q_ld + 1024, c.tag);
    *(v2u*)&lds[L_TQ + w * 1024 + lane * 2] = v2u{cvtpk(q.x, q.y), cvtpk(q.z, q.w)};
    COMPILER_FENCE();
    c.qr = *(const v4u*)&lds[L_TQ + w * 1024 + (lane & 31) * 4];
  } else if constexpr (op == CK) {
    const v4u k = c.kb[a], q = c.qr;
    float x = dot2(k.x, q.x, dot2(k.z, q.z, 0.f)) + dot2(k.y, q.y, dot2(k.w, q.w, 0.f));
    if constexpr ((a & 1) == 1) x = add16(c.xs[0], x);
    if constexpr ((a & 3) == 3) x = tstep<ROR8>(c.xs[1], x, lane & 8);
    if constexpr ((a & 7) == 7) x = tstep<XOR7>(c.xs[2], x, lane & 4);
    if constexpr ((a & 15) == 15) x = tstep<XOR2>(c.xs[3], x, lane & 2);
    if constexpr ((a & 31) == 31) (a == 31 ? c.s0 : c.s1) = tstep<XOR1>(c.xs[4], x, lane & 1) * (0.0625f * 1.4426950408889634f);
    else c.xs[__builtin_ctz(~a)] = x;
  } else if constexpr (op == SM1) {
    const float m = wave_max(fmaxf(c.s0, c.s1));
    const float p0 = __builtin_amdgcn_exp2f(c.s0 - m), p1 = __builtin_amdgcn_exp2f(c.s1 - m);
    __bf16* pw = (__bf16*)&lds[L_P + w * 64] + (__builtin_bitreverse32(lane) >> 26);
    pw[0] = (__bf16)p0; pw[64] = (__bf16)p1;
    COMPILER_FENCE();
    c.mw = m; c.s0 = p0 + p1;
    c.pp = *(const v4u*)&lds[L_P + w * 64];
  } else if constexpr (op == SM2) {
    const float l = wave_sum(c.s0);
    lds[L_ML + 2 * w + (lane & 1)] = (lane & 1) ? l : c.mw;
    flag_set(lds, 1, w);
  } else if constexpr (op == MLPUB) {
    flag_wait(lds, 1, lane);
    const f32x2 ml = *(const f32x2*)&lds[L_ML + 2 * (lane & 7)];
    const float M = max8(ml.x), L = sum8(ml.y * __builtin_amdgcn_exp2f(ml.x - M));
    const v4u g = {__float_as_uint(M), c.tag, __float_as_uint(L), c.tag};
    __builtin_amdgcn_raw_buffer_store_b128(g, c.rw, c.ml_st, 0, AUX_SC1);
  } else if constexpr (op == CV) {
    const unsigned pr = c.pp[a & 3];
    const v4u am = {__builtin_amdgcn_perm(0u, pr, c.sel[0]), __builtin_amdgcn_perm(0u, pr, c.sel[1]),
                    __builtin_amdgcn_perm(0u, pr, c.sel[2]), __builtin_amdgcn_perm(0u, pr, c.sel[3])};
    c.acc = mfma(am, c.vb[a], c.acc);
    if constexpr ((a & 3) == 3 && a + 1 < N_V) c.pp = *(const v4u*)&lds[L_P + w * 64 + (a + 1)];
  } else if constexpr (op == YPF) {
    c.pf = __builtin_amdgcn_raw_buffer_load_b32(c.rw, c.y_pf, 0, AUX_SC1);
  } else if constexpr (op == PEERLD) {
    c.peer = __builtin_amdgcn_raw_buffer_load_b128(c.rw, c.ml_ld, 0, AUX_SC1);
  } else if constexpr (op == PEERCHK) {
    asm volatile("" :: "v"(c.pf));
    await_tags(c.peer, c.rw, c.ml_ld, c.tag);
    const float m = __uint_as_float(c.peer.x);
    float M = fmaxf(m, dpp<XOR1>(m)); M = fmaxf(M, dpp<XOR2>(M));
    float L = __uint_as_float(c.peer.z) * __builtin_amdgcn_exp2f(m - M); L += dpp<XOR1>(L); L += dpp<XOR2>(L);
    c.scale = __builtin_amdgcn_exp2f(c.mw - M) / L;
  } else if constexpr (op == TRW) {
#pragma unroll
    for (int i = 0; i < 4; ++i) *(v4u*)&lds[L_T + w * 1024 + i * 256 + lane * 4] = c.ob[4 * a + i];
    COMPILER_FENCE();
  } else if constexpr (op == TRR) {
    const float* base = &lds[L_T + w * 1024 + (lane >> 4) * 256 + ((lane >> 2) & 3) * 32 + (lane & 3) * 2];
#pragma unroll
    for (int ct = 0; ct < 4; ++ct)
#pragma unroll
      for (int h = 0; h < 2; ++h) c.of[a][ct][h] = tr_read(base + h * 128 + ct * 8);
    COMPILER_FENCE();
  }
  __builtin_amdgcn_sched_barrier(0);
}
template <int B, int... I> __device__ __forceinline__ void run_seq(Ctx& c, std::integer_sequence<int, I...>) { (step<B + I>(c), ...); }
template <int B, int E> __device__ __forceinline__ void run(Ctx& c) { run_seq<B>(c, std::make_integer_sequence<int, E - B>{}); }

__global__ void __launch_bounds__(NTHR, 2)
mha_decode(const __bf16* __restrict__ xA, const __bf16* __restrict__ kA, const __bf16* __restrict__ vA,
           const __bf16* __restrict__ wqA, const __bf16* __restrict__ woA,
           const __bf16* __restrict__ xB, const __bf16* __restrict__ kB, const __bf16* __restrict__ vB,
           const __bf16* __restrict__ wqB, const __bf16* __restrict__ woB, __bf16* out, unsigned char* ws) {
  __shared__ __attribute__((aligned(16))) float lds[L_END];
  Ctx c;
  const unsigned long long tstart = __builtin_amdgcn_s_memrealtime();
  __builtin_amdgcn_sched_barrier(0);
  const int bid = blockIdx.x, rk = bid >> 3, bh = (bid & 7) * 4 + (rk >> 3), br = bh >> 4, head = bh & 15;
  const int b = (rk >> 2) & 1, split = rk & 3, grp = br * 2 + b, prod = head * 4 + split;
  const int tid = threadIdx.x, lane = tid & 63, w = __builtin_amdgcn_readfirstlane(tid >> 6);
  const int off7 = w < 7 ? 0 : OOB;
  c.w = w; c.lane = lane; c.lds = lds;
  c.t0 = (unsigned)tstart;
  c.vo_q = ((lane >> 3) * (NH * DH * 2) + (lane & 7) * 16) | off7;
  c.so_q = ((224 * b + 32 * w) * NH * DH + head * DH + split * 64) * 2;
  c.vo_kv = lane * 16;
  c.so_kv = (b * NH + head) * (SK * DH * 2) + (((w & 1) << 11) | ((w & 2) << 12) | ((w & 4) << 14) | ((split & 1) << 14) | ((split & 2) << 18)) +
            (((head & 2) ? 0x68000 : 0) ^ ((head & 8) << 12) ^ ((((head >> 2) ^ b) & 1) << 20));
  c.vo_o = ((lane >> 3) * (DM * 2) + w * 128 + (lane & 7) * 16) | off7;
  c.so_o = head * DH * DM * 2;
  c.orot = rk & 7;
  int ep_off = br * 256;
  asm volatile("" : "+s"(c.so_q), "+s"(c.so_kv), "+s"(c.so_o), "+s"(c.orot), "+s"(ep_off), "+v"(c.vo_q), "+v"(c.vo_kv), "+v"(c.vo_o)
               : "s"(tstart));
  __builtin_amdgcn_sched_barrier(0);
  c.rq = __builtin_amdgcn_make_buffer_rsrc((void*)(br ? wqB : wqA), 0, DM * NH * DH * 2, RSRC_W3);
  c.ro = __builtin_amdgcn_make_buffer_rsrc((void*)(br ? woB : woA), 0, NH * DH * DM * 2, RSRC_W3);
  c.rk = __builtin_amdgcn_make_buffer_rsrc((void*)(br ? kB : kA), 0, 2 * NH * SK * DH * 2, RSRC_W3);
  run<0, 1>(c);
  c.epoch = *(const CONST_AS unsigned*)(ws + ep_off);
  c.epa = *(const CONST_AS unsigned*)ws;
  const unsigned long long pc = __builtin_amdgcn_s_getpc() & ~63ull;
  c.cpf[0] = *(const CONST_AS unsigned*)(pc + (rk * NW + w) * 64);
  c.cpf[1] = *(const CONST_AS unsigned*)(pc + (256 + min(rk * NW + w, 121)) * 64);
  const int touch = (b * NH + head) * (SK * DH) + (split * NW + w) * 32768;
  c.cpf[2] = *(const CONST_AS unsigned*)((br ? kB : kA) + touch);
  run<1, N_FIRST>(c);
  int br2 = br;
  asm volatile("" : "+s"(br2));
  c.rx = __builtin_amdgcn_make_buffer_rsrc((void*)(br2 ? xB : xA), 0, 2 * DM * 2, RSRC_W3);
  c.vo_x = (((lane & 1) * DM + 224 * b + 32 * w + 8 * (lane >> 4)) * 2) | off7;
  c.rv = __builtin_amdgcn_make_buffer_rsrc((void*)(br2 ? vB : vA), 0, 2 * NH * SK * DH * 2, RSRC_W3);
  c.pv = (const CONST_AS unsigned*)((br2 ? vB : vA) + touch);
  c.po = prod < DM / 16 ? (const CONST_AS unsigned*)(out + b * (2 * DM) + br * DM + prod * 16) : c.pv;
  c.rw = __builtin_amdgcn_make_buffer_rsrc((void*)ws, 0, WS_END, RSRC_W3);
  const rsrc_t rout = __builtin_amdgcn_make_buffer_rsrc((void*)out, 0, 4 * DM * 2, RSRC_W3);
  c.q_st = (WS_Q + bh * 4096 + (lane & 1) * 2048 + b * 1024 + split * 256 + (lane & ~1) * 4) | (w == 0 ? 0 : OOB);
  c.q_ld = WS_Q + bh * 4096 + b * 2048 + lane * 16;
  c.ml_st = (WS_ML + (grp * NH + head) * 128 + split * 16) | (tid == 0 ? 0 : OOB);
  c.ml_ld = WS_ML + (grp * NH + head) * 128 + (lane & 3) * 16;
  c.t_st = (WS_T + ((bid >> 5) * 8 + (bid & 7)) * 8) | (tid == 64 * 3 && ((rk + (rk >> 2)) & 3) == 0 ? 0 : OOB);
  c.y_pf = (WS_PKT + (grp * 64 + prod) * PKT_BYTES + lane * 128) | (tid < PKT_BYTES / 128 ? 0 : OOB);

  run<N_FIRST, S.n - N_LAST>(c);

  const int pk = WS_PKT + grp * 64 * PKT_BYTES;
  const int dq = 32 * (lane >> 5) + 2 * (lane & 15) + ((lane >> 4) & 1);
  int a_wr = (L_RED + w * A_STR + ((dq - 8 * c.orot) & 63)) * 4, a_rd = (L_RED + (lane & 15) * A_STR + 4 * (lane >> 4)) * 4;
  int idx = (lane & 8) ? 0xEEEE : 0x4444;
  int y_off = (pk + prod * PKT_BYTES + tid * 4) | ((lane & 1) ? OOB : 0);
  int p_off = pk + (lane >> 2) * PKT_BYTES + prod * 64 + (lane & 3) * 16;
  int o_off = ((b * (2 * DM) + br * DM + prod * 16 + (lane & 3) * 4 + (lane >> 4)) * 2) | ((lane & 12) ? OOB : 0);
  int e_off = (prod == 0 && b == 0 && lane == 0) ? ep_off : OOB;
  asm volatile("" : "+v"(a_wr), "+v"(a_rd), "+v"(idx), "+v"(y_off), "+v"(p_off), "+v"(o_off), "+v"(e_off));
  unsigned due = origin(lds, N_K + N_V - 1, 0u) + due_at(N_K + N_V - 1) + END_AFTER;
  asm volatile("" : "+s"(due));
  f32x4 y[4];
#pragma unroll
  for (int ct = 0; ct < 4; ++ct) { y[ct] = f32x4{0.f, 0.f, 0.f, 0.f}; asm volatile("" : "+v"(y[ct])); }

  run<S.n - N_LAST, S.n>(c);
  LDS_AS unsigned char* const lb = (LDS_AS unsigned char*)lds;
  *(LDS_AS unsigned*)(lb + a_wr) = cvtpk(c.acc.x * c.scale, c.acc.y * c.scale);
  *(LDS_AS unsigned*)(lb + a_wr + 8 * A_STR * 4) = cvtpk(c.acc.z * c.scale, c.acc.w * c.scale);
  __syncthreads();
  if (__builtin_expect(w == NW - 1, 0)) {
    if (prod < DM / 16) {
      unsigned cnt;
      asm volatile(
          "s_mov_b32 %0, 0\n"
          "1:\n\t"
          "s_memrealtime vcc\n\t"
          "s_waitcnt lgkmcnt(0)\n\t"
          "s_sub_u32 vcc_lo, %9, vcc_lo\n\t"
          "s_cmp_lt_u32 vcc_lo, 0x100\n\t"
          "s_cbranch_scc1 1b\n"
          "2:\n\t"
          "buffer_load_dwordx4 v[0:3], %1, %4, 0 offen sc1\n\t"
          "buffer_load_dwordx4 v[4:7], %1, %4, %10 offen sc1\n\t"
          "buffer_load_dwordx4 v[8:11], %1, %4, %11 offen sc1\n\t"
          "buffer_load_dwordx4 v[12:15], %1, %4, %12 offen sc1\n\t"
          "s_add_u32 %0, %0, 1\n\t"
          "s_waitcnt vmcnt(3)\n\t"
          "v_xor_b32 v0, %6, v0\n\tv_xor_b32 v1, %7, v1\n\tv_xor_b32 v2, %6, v2\n\tv_xor_b32 v3, %7, v3\n\t"
          "v_or3_b32 v16, v0, v1, v2\n\t"
          "s_waitcnt vmcnt(2)\n\t"
          "v_xor_b32 v4, %6, v4\n\tv_xor_b32 v5, %7, v5\n\tv_xor_b32 v6, %6, v6\n\tv_xor_b32 v7, %7, v7\n\t"
          "v_or3_b32 v16, v16, v3, v4\n\tv_or3_b32 v17, v5, v6, v7\n\t"
          "v_pk_add_f32 v[0:1], v[0:1], v[4:5]\n\tv_pk_add_f32 v[2:3], v[2:3], v[6:7]\n\t"
          "s_waitcnt vmcnt(1)\n\t"
          "v_xor_b32 v8, %6, v8\n\tv_xor_b32 v9, %7, v9\n\tv_xor_b32 v10, %6, v10\n\tv_xor_b32 v11, %7, v11\n\t"
          "v_or3_b32 v16, v16, v8, v9\n\tv_or3_b32 v17, v17, v10, v11\n\t"
          "s_waitcnt vmcnt(0)\n\t"
          "v_xor_b32 v12, %6, v12\n\tv_xor_b32 v13, %7, v13\n\tv_xor_b32 v14, %6, v14\n\tv_xor_b32 v15, %7, v15\n\t"
          "v_or3_b32 v16, v16, v12, v13\n\tv_or3_b32 v17, v17, v14, v15\n\t"
          "v_pk_add_f32 v[8:9], v[8:9], v[12:13]\n\tv_pk_add_f32 v[10:11], v[10:11], v[14:15]\n\t"
          "v_or_b32 v16, v16, v17\n\t"
          "v_pk_add_f32 v[0:1], v[0:1], v[8:9]\n\tv_pk_add_f32 v[2:3], v[2:3], v[10:11]\n\t"
          "v_and_b32 v16, 0xff, v16\n\t"
          "v_cmp_ne_u32 vcc, 0, v16\n\t"
          "v_permlane32_swap_b32 v0, v2\n\tv_permlane32_swap_b32 v1, v3\n\t"
          "s_nop 0\n\t"
          "v_add_f32 v0, v0, v2\n\tv_add_f32 v1, v1, v3\n\t"
          "s_nop 1\n\t"
          "v_permlane16_swap_b32 v0, v1\n\t"
          "s_nop 0\n\t"
          "v_add_f32 v0, v0, v1\n\t"
          "s_nop 1\n\t"
          "v_add_f32_dpp v0, v0, v0 row_ror:4 row_mask:0xf bank_mask:0xf\n\t"
          "s_nop 1\n\t"
          "v_add_f32_dpp v0, v0, v0 row_ror:8 row_mask:0xf bank_mask:0xf\n\t"
          "s_cmp_lt_u32 %0, %13\n\t"
          "v_cvt_pk_bf16_f32 v0, v0, v0\n\t"
          "s_cbranch_vccz 4f\n\t"
          "s_cbranch_scc1 2b\n\t"
          "v_mov_b32 v0, 0x7fc0\n"
          "4:\n\t"
          "buffer_store_short v0, %2, %5, 0 offen\n\t"
          "v_mov_b32 v1, %8\n\t"
          "buffer_store_dword v1, %3, %4, 0 offen sc1"
          : "=&s"(cnt)
          : "v"(p_off), "v"(o_off), "v"(e_off), "s"(c.rw), "s"(rout), "s"(c.tag & 255u), "s"((c.tag >> 8) & 255u), "s"(c.tag), "s"(due),
            "s"(16 * PKT_BYTES), "s"(32 * PKT_BYTES), "s"(48 * PKT_BYTES), "s"(SPIN_CAP)
          : "scc", "vcc", "memory", "v0", "v1", "v2", "v3", "v4", "v5", "v6", "v7", "v8", "v9", "v10", "v11", "v12", "v13", "v14", "v15", "v16", "v17");
    }
    __builtin_amdgcn_endpgm();
  }
  v4u am[4];
#pragma unroll
  for (int ks = 0; ks < 4; ++ks) am[ks] = *(LDS_AS v4u*)(lb + a_rd + 64 * ks);
  __builtin_amdgcn_sched_barrier(0);
#pragma unroll
  for (int ks = 0; ks < 4; ++ks)
#pragma unroll
    for (int ct = 0; ct < 4; ++ct) {
      const v2u b0 = c.of[2 * ks][ct][0], b1 = c.of[2 * ks][ct][1], b2 = c.of[2 * ks + 1][ct][0], b3 = c.of[2 * ks + 1][ct][1];
      const v8u bm = {b0.x, b0.y, b1.x, b1.y, b2.x, b2.y, b3.x, b3.y};
      y[ct] = __builtin_amdgcn_smfmac_f32_16x16x64_bf16(__builtin_bit_cast(bf16x8, am[ks]), __builtin_bit_cast(bf16x16, bm), y[ct], idx, 0, 0);
    }
  float sm[4];
#pragma unroll
  for (int ct = 0; ct < 4; ++ct) sm[ct] = (y[ct].x + y[ct].y) + (y[ct].z + y[ct].w);
  const float yv = add32(add16(sm[0], sm[1]), add16(sm[2], sm[3]));
  __builtin_amdgcn_raw_buffer_store_b64(granule(yv, dpp<XOR1>(yv), c.tag), c.rw, y_off, 0, AUX_SC1);
}

}

extern "C" void kernel_launch(void* const* d_in, const int* in_sizes, int n_in, void* d_out, int out_size, void* d_ws,
                              size_t ws_size, hipStream_t stream) {
  (void)in_sizes; (void)n_in; (void)out_size; (void)ws_size;
  mha_decode<<<256, NTHR, 0, stream>>>((const __bf16*)d_in[0], (const __bf16*)d_in[1], (const __bf16*)d_in[2],
                                       (const __bf16*)d_in[3], (const __bf16*)d_in[4], (const __bf16*)d_in[5],
                                       (const __bf16*)d_in[6], (const __bf16*)d_in[7], (const __bf16*)d_in[8],
                                       (const __bf16*)d_in[9], (__bf16*)d_out, (unsigned char*)d_ws);
}
```

```cpp
#include <hip/hip_runtime.h>
#include <hip/hip_bf16.h>
#include <utility>

namespace {

typedef unsigned v2u __attribute__((ext_vector_type(2)));
typedef unsigned v4u __attribute__((ext_vector_type(4)));
typedef unsigned v8u __attribute__((ext_vector_type(8)));
typedef float f32x2 __attribute__((ext_vector_type(2)));
typedef float f32x4 __attribute__((ext_vector_type(4)));
typedef __bf16 bf16x2 __attribute__((ext_vector_type(2)));
typedef __bf16 bf16x8 __attribute__((ext_vector_type(8)));
typedef __bf16 bf16x16 __attribute__((ext_vector_type(16)));
typedef short i16x4 __attribute__((ext_vector_type(4)));
typedef __amdgpu_buffer_rsrc_t rsrc_t;
#define LDS_AS __attribute__((address_space(3)))
#define CONST_AS __attribute__((address_space(4)))
#define COMPILER_FENCE() asm volatile("" ::: "memory")

constexpr int DM = 448, NH = 16, SK = 4096, DH = 256;
constexpr int NW = 8, NTHR = NW * 64;
constexpr int N_Q = 4, N_K = 64, N_V = 64, N_O = 32;
constexpr unsigned SPIN_CAP = 1u << 20;
constexpr int RSRC_W3 = 0x00020000, AUX_NT = 2, AUX_SC1 = 16, OOB = 0x40000000;
constexpr int WS_PKT = 4096, PKT_BYTES = DM * 4, WS_Q = WS_PKT + 256 * PKT_BYTES, WS_ML = WS_Q + 64 * 2048,
              WS_T = WS_ML + 64 * 128, WS_END = WS_T + 3 * 512;
constexpr int A_STR = 68;
constexpr int L_RED = 0, L_T = L_RED + 16 * A_STR, L_TQ = L_T + NW * 1024, L_ML = L_TQ + NW * 1024, L_P = L_ML + 2 * NW,
              L_F = L_P + NW * 64, L_R = L_F + NW * 2, L_END = L_R + 4;

constexpr int WIN = 3;
constexpr int O_FIRST = 4;
constexpr int O_START = 16, O_EVERY = 3;
constexpr int X_AT = 2, QMM_AT = 6, QPUB_AT = 9, QLD_AT = 17, QCHK_AT = 21, YPF_AT = 100;
constexpr int N_FIRST = N_Q + O_FIRST + 1, N_LAST = 3;
constexpr int TAU = 431, ORIGIN0 = 255, MARGIN = 32, GATE_FROM = 23, GATE_AHEAD = 21, STAGGER = 24, END_AFTER = 118;
constexpr int LAG_FROM = 7, LAG_EVERY = 4, GAIN_UP = 9, BAND = 58, GAIN_DOWN = 4;
constexpr int T_AT[3] = {10, 38, 84}, T_DELAY[3] = {8, 6, 6}, T_KEEP[3] = {0, 14, 8}, T_OVER = 12, T_SLOW[3] = {0, 32, 36},
              T_EXTRA[3] = {0, 18, 40}, T_PACE = 43;
constexpr int due_at(int j) { return j * TAU / 16; }

enum Op : int { IQ, IK, IV, IO, IX, QPUB, QLD, MLPUB, YPF, TPUB, TLD, PEERLD,
                WAITV, WAITB, WAITS, GATE, LAG, TCHK, SETUP, QTR, QMM, QCHK, CK, SM1, SM2, CV, PEERCHK, TRW, TRR };
struct Sched { int n; int op[900]; int a[900]; };
constexpr Sched make_sched() {
  Sched s{};
  int vmk[256] = {}, vmi[256] = {}, nvm = 0;
  auto emit = [&](int op, int a) {
    s.op[s.n] = op; s.a[s.n] = a; ++s.n;
    if (op <= PEERLD) { vmk[nvm] = op; vmi[nvm] = a; ++nvm; }
  };
  int no = 0, nk = 0, nv = 0, nw = 0, nr = 0, sm = 0;
  int lk = 0, lv = 0, lo = 0;
  for (int j = 0; j < N_Q; ++j) emit(IQ, j);
  while (no < O_FIRST) emit(IO, no++);
  for (int j = 0; j < N_K + N_V; ++j) {
    if (j > 0) {
      int p = nvm, seen = 0;
      while (seen < WIN) { --p; if (vmk[p] <= IV) ++seen; }
      if (j > LAG_FROM) {
        const int k = (j - LAG_FROM - 1) % LAG_EVERY + 1;
        int p2 = p, p0 = p;
        do --p2; while (vmk[p2] > IV);
        do ++p0; while (vmk[p0] > IV);
        emit(WAITV, nvm - 1 - p2);
        emit(WAITB, (nvm - 1 - p) | (k << 8));
        if (j > GATE_FROM) emit(GATE, due_at(j)); else emit(WAITS, (nvm - 1 - p0) | (k << 8));
        p = p2;
      } else emit(WAITV, nvm - 1 - p);
      if (vmk[p] == IK) lk = vmi[p] + 1;
      if (vmk[p] == IV) { lk = N_K; lv = vmi[p] + 1; }
      for (int t = 0; t < p; ++t) if (vmk[t] == IO) lo = vmi[t] + 1;
    }
    if (j < N_K) emit(IK, j); else emit(IV, j - N_K);
    if (j == 0) emit(SETUP, 0);
    if (j >= O_START && (j - O_START) % O_EVERY == 0 && no < N_O) emit(IO, no++);
    if (j >= LAG_FROM && (j - LAG_FROM) % LAG_EVERY == 0 && j + 1 < N_K + N_V) emit(LAG, j);
    if (j == X_AT) emit(IX, 0);
    if (j == YPF_AT) emit(YPF, 0);
    for (int t = 0; t < 3; ++t) {
      if (j == T_AT[t]) emit(TPUB, t);
      if (j == T_AT[t] + T_DELAY[t]) emit(TLD, t);
      if (j == T_AT[t] + T_DELAY[t] + 4) emit(TCHK, t);
    }
    if (j == QMM_AT - 1) emit(QTR, 0);
    if (j == QMM_AT) emit(QMM, 0);
    if (j == QPUB_AT) emit(QPUB, 0);
    if (j == QLD_AT) { emit(QLD, 0); emit(QLD, 1); }
    if (j == QCHK_AT) emit(QCHK, 0);
    else if (j > QCHK_AT && nk < N_K) { for (int t = 0; t < 3 && nk < lk; ++t) emit(CK, nk++); }
    else if (nk == N_K && sm < 2) { emit(sm == 0 ? SM1 : SM2, 0); ++sm; }
    else if (sm == 2) {
      for (int t = 0; t < 3 && nv < lv; ++t) {
        emit(CV, nv++);
        if (nv == 12) emit(MLPUB, 0);
        if (nv == N_V - 20) emit(PEERLD, 0);
        if (nv == N_V - 10) emit(PEERCHK, 0);
      }
    }
    if (nr < nw) emit(TRR, nr++);
    else if (4 * nw + 4 <= lo) emit(TRW, nw++);
  }
  if (no != N_O || nr != N_O / 4 || nv < N_V - 10 || nv > N_V - N_LAST) s.n = -1;
  else while (nv < N_V) emit(CV, nv++);
  return s;
}
constexpr Sched S = make_sched();
static_assert(S.n > 0, "the schedule must finish everything but the last V loads inside the stream");

constexpr int pair_off(int g) { return ((g & 1) << 12) | ((g & 2) << 14) | ((g & 12) << 15) | ((g & 16) << 16); }

__device__ __forceinline__ float dot2(unsigned a, unsigned b, float c) {
  return __builtin_amdgcn_fdot2_f32_bf16(__builtin_bit_cast(bf16x2, a), __builtin_bit_cast(bf16x2, b), c, false);
}
__device__ __forceinline__ unsigned cvtpk(float lo, float hi) {
  const bf16x2 r = {(__bf16)lo, (__bf16)hi};
  return __builtin_bit_cast(unsigned, r);
}
template <int CTRL> __device__ __forceinline__ float dpp(float x) {
  return __builtin_bit_cast(float, __builtin_amdgcn_mov_dpp(__builtin_bit_cast(int, x), CTRL, 0xf, 0xf, true));
}
constexpr int XOR1 = 0xB1, XOR2 = 0x4E, XOR7 = 0x141, XOR15 = 0x140, ROR8 = 0x128;
__device__ __forceinline__ float sum8(float x) { x += dpp<XOR1>(x); x += dpp<XOR2>(x); x += dpp<XOR7>(x); return x; }
__device__ __forceinline__ float max8(float x) {
  x = fmaxf(x, dpp<XOR1>(x)); x = fmaxf(x, dpp<XOR2>(x)); x = fmaxf(x, dpp<XOR7>(x));
  return x;
}
__device__ __forceinline__ float add16(float a, float b) {
  const auto s = __builtin_amdgcn_permlane16_swap(__float_as_uint(a), __float_as_uint(b), false, false);
  return __uint_as_float(s[0]) + __uint_as_float(s[1]);
}
__device__ __forceinline__ float add32(float a, float b) {
  const auto s = __builtin_amdgcn_permlane32_swap(__float_as_uint(a), __float_as_uint(b), false, false);
  return __uint_as_float(s[0]) + __uint_as_float(s[1]);
}
template <int CTRL> __device__ __forceinline__ float tstep(float lo, float hi, bool sel) {
  const float a = lo + dpp<CTRL>(lo), b = hi + dpp<CTRL>(hi);
  return sel ? b : a;
}
__device__ __forceinline__ float wave_sum(float x) {
  x = sum8(x); x += dpp<XOR15>(x); x = add16(x, x);
  return add32(x, x);
}
__device__ __forceinline__ float wave_max(float x) {
  x = max8(x); x = fmaxf(x, dpp<XOR15>(x));
  const auto s = __builtin_amdgcn_permlane16_swap(__float_as_uint(x), __float_as_uint(x), false, false);
  x = fmaxf(__uint_as_float(s[0]), __uint_as_float(s[1]));
  const auto t = __builtin_amdgcn_permlane32_swap(__float_as_uint(x), __float_as_uint(x), false, false);
  return fmaxf(__uint_as_float(t[0]), __uint_as_float(t[1]));
}
__device__ __forceinline__ f32x4 mfma(v4u a, v4u b, f32x4 c) {
  return __builtin_amdgcn_mfma_f32_16x16x32_bf16(__builtin_bit_cast(bf16x8, a), __builtin_bit_cast(bf16x8, b), c, 0, 0, 0);
}
__device__ __forceinline__ v2u tr_read(const float* p) {
  return __builtin_bit_cast(v2u, __builtin_amdgcn_ds_read_tr16_b64_v4i16((LDS_AS i16x4*)p));
}
__device__ __forceinline__ void await_tags(v4u& g, rsrc_t r, int off, unsigned tag) {
  if (__builtin_expect(!__all((g.y == tag) & (g.w == tag)), 0)) {
    unsigned spins = 0;
    bool ok;
#pragma unroll 1
    do {
      COMPILER_FENCE();
      g = __builtin_amdgcn_raw_buffer_load_b128(r, off, 0, AUX_SC1);
      ok = __all((g.y == tag) & (g.w == tag));
    } while (!ok && ++spins < SPIN_CAP);
    if (!ok) g.x = 0x7fc00000u;
  }
}
__device__ __forceinline__ v2u granule(float a, float b, unsigned tag) {
  return v2u{(__float_as_uint(a) & ~255u) | (tag & 255u), (__float_as_uint(b) & ~255u) | ((tag >> 8) & 255u)};
}
__device__ __forceinline__ bool untag(v4u& g, unsigned tag) {
  const unsigned t0 = tag & 255u, t1 = (tag >> 8) & 255u;
  g ^= v4u{t0, t1, t0, t1};
  return __all(((g.x | g.y | g.z | g.w) & 255u) == 0u);
}
__device__ __forceinline__ f32x4 await16(v4u g, rsrc_t r, int off, unsigned tag) {
  if (__builtin_expect(!untag(g, tag), 0)) {
    unsigned spins = 0;
    bool ok;
#pragma unroll 1
    do {
      COMPILER_FENCE();
      g = __builtin_amdgcn_raw_buffer_load_b128(r, off, 0, AUX_SC1);
      ok = untag(g, tag);
    } while (!ok && ++spins < SPIN_CAP);
    if (!ok) g.x = 0x7fc00000u;
  }
  return __builtin_bit_cast(f32x4, g);
}
__device__ __forceinline__ void flag_set(float* lds, int k, int w) {
  asm volatile("s_waitcnt lgkmcnt(0)" ::: "memory");
  ((LDS_AS volatile unsigned*)lds)[L_F + k * NW + w] = 1u;
}
__device__ __forceinline__ void flag_wait(float* lds, int k, int lane) {
  while (!__all(((LDS_AS volatile unsigned*)lds)[L_F + k * NW + (lane & 7)] != 0u)) {}
  COMPILER_FENCE();
}
__device__ __forceinline__ void wait_until(unsigned t) {
  asm volatile("1:\n\ts_memrealtime vcc\n\ts_waitcnt lgkmcnt(0)\n\ts_sub_u32 vcc_lo, %0, vcc_lo\n\ts_cmp_lt_u32 vcc_lo, 0x100\n\ts_cbranch_scc1 1b"
               :: "s"(t) : "scc", "vcc", "memory");
}
__device__ __forceinline__ unsigned origin(float* lds, int j, unsigned none) {
  v2u v = *(LDS_AS volatile v2u*)&lds[L_R];
  asm volatile("" : "+v"(v));
  const unsigned org = __builtin_amdgcn_readfirstlane(v.x), x = __builtin_amdgcn_readfirstlane(v.y);
  return org ? org + ((min(j, (int)(x >> 8) & 255) - (int)(x & 255u)) * (int)(x >> 16) >> 4) : none;
}

struct Ctx {
  rsrc_t rq, rk, rv, ro, rx, rw;
  float* lds;
  int w, lane, vo_q, vo_kv, vo_o, vo_x, so_q, so_kv, so_o, orot, q_st, q_ld, ml_st, ml_ld, y_pf, boost, slow, t_st;
  unsigned tc, tagt, epa, gate;
  v2u tg;
  const CONST_AS unsigned *pv, *po;
  unsigned t0, epoch, tag, pf, cpf[3], sel[4];
  v4u qb[N_Q], kb[N_K], vb[N_V], ob[N_O], xv, qg[2], qr, pp, peer;
  v2u qf[4][2];
  v2u of[8][4][2];
  f32x4 acc;
  float xs[5], s0, s1, mw, scale;
};

template <int I> __device__ __forceinline__ void step(Ctx& c) {
  constexpr int op = S.op[I], a = S.a[I];
  float* const lds = c.lds;
  const int w = c.w, lane = c.lane;
  if constexpr (op == IQ) {
    c.qb[a] = __builtin_amdgcn_raw_buffer_load_b128(c.rq, c.vo_q, c.so_q + 8 * a * (NH * DH * 2), AUX_NT);
  } else if constexpr (op == IK) {
    c.kb[a] = __builtin_amdgcn_raw_buffer_load_b128(c.rk, c.vo_kv + (a & 1) * 1024, c.so_kv ^ pair_off(a >> 1), AUX_NT);
  } else if constexpr (op == IV) {
    c.vb[a] = __builtin_amdgcn_raw_buffer_load_b128(c.rv, c.vo_kv + (a & 1) * 1024, c.so_kv ^ pair_off(a >> 1), AUX_NT);
  } else if constexpr (op == IO) {
    c.ob[a] = __builtin_amdgcn_raw_buffer_load_b128(c.ro, c.vo_o, c.so_o + (a < 4 || c.orot + a / 4 < 8 ? 8 * a : 8 * a - DH) * (DM * 2), 0);
  } else if constexpr (op == IX) {
    c.xv = __builtin_amdgcn_raw_buffer_load_b128(c.rx, c.vo_x, 0, 0);
  } else if constexpr (op == WAITV) {
    __builtin_amdgcn_s_waitcnt((a & 15) | ((a >> 4) << 14) | 0x0F70);
  } else if constexpr (op == LAG) {
    const unsigned none = c.t0 - 65536, o = origin(lds, a + 2, none);
    const int late = (int)((unsigned)__builtin_amdgcn_s_memrealtime() - (o == none ? c.t0 + ORIGIN0 : o)) - due_at(a);
    c.gate = o - GATE_AHEAD + (w * STAGGER >> 3);
    c.boost = late * GAIN_UP >> 4;
    c.slow = (-late - BAND) * GAIN_DOWN >> 4;
  } else if constexpr (op == TPUB) {
    c.tc = (unsigned)__builtin_amdgcn_s_memrealtime();
    __builtin_amdgcn_raw_buffer_store_b64(v2u{c.tc, c.tagt}, c.rw, c.t_st, a * 512, AUX_SC1);
  } else if constexpr (op == TLD) {
    c.tg = __builtin_amdgcn_raw_buffer_load_b64(c.rw, (WS_T + lane * 8) | (w == NW - 1 ? 0 : OOB), a * 512, AUX_SC1);
  } else if constexpr (op == TCHK) {
    const bool ok = c.tg.y == c.tagt;
    const float d = wave_sum(ok ? (float)(int)(c.tg.x - c.tc) : (float)due_at(T_DELAY[a])), n = wave_sum(ok ? 1.f : 0.f);
    unsigned org = c.tc + (int)(d * (1.f / 64)) - due_at(T_AT[a]) + MARGIN;
    bool use = w == NW - 1 && n > 16.f;
    int pace = 0;
    if constexpr (a > 0) {
      const unsigned old = origin(lds, T_AT[a], org);
      const int debt = (int)(org - old) - (MARGIN - GATE_AHEAD + 3 * STAGGER / 8);
      use = use && debt > T_KEEP[a];
      pace = debt > T_SLOW[a] ? T_PACE : 0;
      org = old + min(3 * (debt - T_KEEP[a]), debt + T_OVER) + (pace ? T_EXTRA[a] : 0);
    }
    constexpr int last = T_AT[a] < N_K ? N_K + 4 : N_K + N_V;
    *(LDS_AS volatile v2u*)&lds[L_R + (use ? 0 : 2)] = v2u{org | 1u, (unsigned)(T_AT[a] | (last << 8) | (pace << 16))};
  } else if constexpr (op == GATE) {
    wait_until(c.gate + a);
  } else if constexpr (op == WAITS) {
    asm volatile("s_cmp_lt_i32 %0, %1\n\ts_cbranch_scc1 1f\n\ts_waitcnt vmcnt(%2)\n1:"
                 :: "s"(c.slow), "i"(a >> 8), "i"(a & 255) : "scc", "memory");
  } else if constexpr (op == WAITB) {
    asm volatile("s_cmp_ge_i32 %0, %1\n\ts_cbranch_scc1 1f\n\ts_waitcnt vmcnt(%2)\n1:"
                 :: "s"(c.boost), "i"(a >> 8), "i"(a & 255) : "scc", "memory");
  } else if constexpr (op == SETUP) {
    const int row = lane & 15, j = row & 7;
    const bool act = ((lane >> 4) & 1) == (row >> 3);
    const unsigned two = (lane >> 5) ? 0x0302u : 0x0100u;
    const unsigned s = (j & 1) ? ((two << 16) | 0x0c0cu) : (0x0c0c0000u | two);
#pragma unroll
    for (int i = 0; i < 4; ++i) c.sel[i] = (act && (j >> 1) == i) ? s : 0x0c0c0c0cu;
    c.acc = f32x4{0.f, 0.f, 0.f, 0.f};
    ((unsigned*)lds)[L_F + (lane & 1) * NW + w] = 0u;
    ((unsigned*)lds)[L_R + (lane & 1)] = 0u;
    asm volatile("" :: "s"(c.cpf[0]), "s"(c.cpf[1]), "s"(c.cpf[2]));
    c.tagt = c.epa * 0x9E3779B1u + 0x7F4A7C15u;
    __syncthreads();
    c.cpf[0] = *c.pv; c.cpf[1] = *c.po;
  } else if constexpr (op == QTR) {
#pragma unroll
    for (int i = 0; i < 4; ++i) *(v4u*)&lds[L_TQ + w * 1024 + i * 256 + lane * 4] = c.qb[i];
    COMPILER_FENCE();
    const float* base = &lds[L_TQ + w * 1024 + (lane >> 4) * 256 + ((lane >> 2) & 3) * 32 + (lane & 3) * 2];
#pragma unroll
    for (int ct = 0; ct < 4; ++ct)
#pragma unroll
      for (int h = 0; h < 2; ++h) c.qf[ct][h] = tr_read(base + h * 128 + ct * 8);
    COMPILER_FENCE();
  } else if constexpr (op == QMM) {
    f32x4 d[4];
#pragma unroll
    for (int ct = 0; ct < 4; ++ct)
      d[ct] = mfma(c.xv, v4u{c.qf[ct][0].x, c.qf[ct][0].y, c.qf[ct][1].x, c.qf[ct][1].y}, f32x4{0.f, 0.f, 0.f, 0.f});
    c.tag = c.epoch * 0x9E3779B1u + 0x7F4A7C15u;
    asm volatile("" :: "s"(c.cpf[0]), "s"(c.cpf[1]));
    asm volatile("" : "+s"(c.tag));
    const f32x4 lo = (lane & 16) ? d[1] : d[0], hi = (lane & 16) ? d[3] : d[2];
    *(f32x2*)&lds[L_RED + w * 128 + lane * 2] = (lane & 32) ? f32x2{hi.x, hi.y} : f32x2{lo.x, lo.y};
    flag_set(lds, 0, w);
  } else if constexpr (op == QPUB) {
    flag_wait(lds, 0, lane);
    f32x2 t = {0.f, 0.f};
#pragma unroll
    for (int i = 0; i < 7; ++i) t += *(const f32x2*)&lds[L_RED + i * 128 + lane * 2];
    const float nx = dpp<XOR1>(t.x), ny = dpp<XOR1>(t.y);
    __builtin_amdgcn_raw_buffer_store_b64(granule((lane & 1) ? ny : t.x, (lane & 1) ? t.y : nx, c.tag), c.rw, c.q_st, 0, AUX_SC1);
  } else if constexpr (op == QLD) {
    c.qg[a] = __builtin_amdgcn_raw_buffer_load_b128(c.rw, c.q_ld + a * 1024, 0, AUX_SC1);
  } else if constexpr (op == QCHK) {
    const f32x4 q = await16(c.qg[0], c.rw, c.q_ld, c.tag) + await16(c.qg[1], c.rw, c.q_ld + 1024, c.tag);
    *(v2u*)&lds[L_TQ + w * 1024 + lane * 2] = v2u{cvtpk(q.x, q.y), cvtpk(q.z, q.w)};
    COMPILER_FENCE();
    c.qr = *(const v4u*)&lds[L_TQ + w * 1024 + (lane & 31) * 4];
  } else if constexpr (op == CK) {
    const v4u k = c.kb[a], q = c.qr;
    float x = dot2(k.x, q.x, dot2(k.z, q.z, 0.f)) + dot2(k.y, q.y, dot2(k.w, q.w, 0.f));
    if constexpr ((a & 1) == 1) x = add16(c.xs[0], x);
    if constexpr ((a & 3) == 3) x = tstep<ROR8>(c.xs[1], x, lane & 8);
    if constexpr ((a & 7) == 7) x = tstep<XOR7>(c.xs[2], x, lane & 4);
    if constexpr ((a & 15) == 15) x = tstep<XOR2>(c.xs[3], x, lane & 2);
    if constexpr ((a & 31) == 31) (a == 31 ? c.s0 : c.s1) = tstep<XOR1>(c.xs[4], x, lane & 1) * (0.0625f * 1.4426950408889634f);
    else c.xs[__builtin_ctz(~a)] = x;
  } else if constexpr (op == SM1) {
    const float m = wave_max(fmaxf(c.s0, c.s1));
    const float p0 = __builtin_amdgcn_exp2f(c.s0 - m), p1 = __builtin_amdgcn_exp2f(c.s1 - m);
    __bf16* pw = (__bf16*)&lds[L_P + w * 64] + (__builtin_bitreverse32(lane) >> 26);
    pw[0] = (__bf16)p0; pw[64] = (__bf16)p1;
    COMPILER_FENCE();
    c.mw = m; c.s0 = p0 + p1;
    c.pp = *(const v4u*)&lds[L_P + w * 64];
  } else if constexpr (op == SM2) {
    const float l = wave_sum(c.s0);
    lds[L_ML + 2 * w + (lane & 1)] = (lane & 1) ? l : c.mw;
    flag_set(lds, 1, w);
  } else if constexpr (op == MLPUB) {
    flag_wait(lds, 1, lane);
    const f32x2 ml = *(const f32x2*)&lds[L_ML + 2 * (lane & 7)];
    const float M = max8(ml.x), L = sum8(ml.y * __builtin_amdgcn_exp2f(ml.x - M));
    const v4u g = {__float_as_uint(M), c.tag, __float_as_uint(L), c.tag};
    __builtin_amdgcn_raw_buffer_store_b128(g, c.rw, c.ml_st, 0, AUX_SC1);
  } else if constexpr (op == CV) {
    const unsigned pr = c.pp[a & 3];
    const v4u am = {__builtin_amdgcn_perm(0u, pr, c.sel[0]), __builtin_amdgcn_perm(0u, pr, c.sel[1]),
                    __builtin_amdgcn_perm(0u, pr, c.sel[2]), __builtin_amdgcn_perm(0u, pr, c.sel[3])};
    c.acc = mfma(am, c.vb[a], c.acc);
    if constexpr ((a & 3) == 3 && a + 1 < N_V) c.pp = *(const v4u*)&lds[L_P + w * 64 + (a + 1)];
  } else if constexpr (op == YPF) {
    c.pf = __builtin_amdgcn_raw_buffer_load_b32(c.rw, c.y_pf, 0, AUX_SC1);
  } else if constexpr (op == PEERLD) {
    c.peer = __builtin_amdgcn_raw_buffer_load_b128(c.rw, c.ml_ld, 0, AUX_SC1);
  } else if constexpr (op == PEERCHK) {
    asm volatile("" :: "v"(c.pf));
    await_tags(c.peer, c.rw, c.ml_ld, c.tag);
    const float m = __uint_as_float(c.peer.x);
    float M = fmaxf(m, dpp<XOR1>(m)); M = fmaxf(M, dpp<XOR2>(M));
    float L = __uint_as_float(c.peer.z) * __builtin_amdgcn_exp2f(m - M); L += dpp<XOR1>(L); L += dpp<XOR2>(L);
    c.scale = __builtin_amdgcn_exp2f(c.mw - M) / L;
  } else if constexpr (op == TRW) {
#pragma unroll
    for (int i = 0; i < 4; ++i) *(v4u*)&lds[L_T + w * 1024 + i * 256 + lane * 4] = c.ob[4 * a + i];
    COMPILER_FENCE();
  } else if constexpr (op == TRR) {
    const float* base = &lds[L_T + w * 1024 + (lane >> 4) * 256 + ((lane >> 2) & 3) * 32 + (lane & 3) * 2];
#pragma unroll
    for (int ct = 0; ct < 4; ++ct)
#pragma unroll
      for (int h = 0; h < 2; ++h) c.of[a][ct][h] = tr_read(base + h * 128 + ct * 8);
    COMPILER_FENCE();
  }
  __builtin_amdgcn_sched_barrier(0);
}
template <int B, int... I> __device__ __forceinline__ void run_seq(Ctx& c, std::integer_sequence<int, I...>) { (step<B + I>(c), ...); }
template <int B, int E> __device__ __forceinline__ void run(Ctx& c) { run_seq<B>(c, std::make_integer_sequence<int, E - B>{}); }

struct Args { const __bf16* in[2][5]; __bf16* out; unsigned char* ws; };
static_assert(sizeof(Args) == 12 * 8, "no padding");

__global__ void __launch_bounds__(NTHR, 2) mha_decode(Args args) {
  __shared__ __attribute__((aligned(16))) float lds[L_END];
  Ctx c;
  const int bid = blockIdx.x, br = (bid >> 2) & 1;
  const __bf16 *px = args.in[br][0], *pk_ = args.in[br][1], *pv_ = args.in[br][2], *pwq = args.in[br][3], *pwo = args.in[br][4];
  __bf16* out = args.out;
  unsigned char* ws = args.ws;
  const unsigned long long tstart = __builtin_amdgcn_s_memrealtime();
  __builtin_amdgcn_sched_barrier(0);
  const int rk = bid >> 3, head = (bid & 3) * 4 + (rk >> 3), bh = br * NH + head;
  const int b = (rk >> 2) & 1, split = rk & 3, grp = br * 2 + b, prod = head * 4 + split;
  const int tid = threadIdx.x, lane = tid & 63, w = __builtin_amdgcn_readfirstlane(tid >> 6);
  const int off7 = w < 7 ? 0 : OOB;
  c.w = w; c.lane = lane; c.lds = lds;
  c.t0 = (unsigned)tstart;
  c.vo_q = ((lane >> 3) * (NH * DH * 2) + (lane & 7) * 16) | off7;
  c.so_q = ((224 * b + 32 * w) * NH * DH + head * DH + split * 64) * 2;
  c.vo_kv = lane * 16;
  c.so_kv = (b * NH + head) * (SK * DH * 2) + (((w & 1) << 11) | ((w & 2) << 12) | ((w & 4) << 14) | ((split & 1) << 14) | ((split & 2) << 18)) +
            (((head & 2) ? 0x68000 : 0) ^ ((head & 8) << 12) ^ ((((head >> 2) ^ b) & 1) << 20));
  c.vo_o = ((lane >> 3) * (DM * 2) + w * 128 + (lane & 7) * 16) | off7;
  c.so_o = (head * DH + 32 * (rk & 7)) * (DM * 2);
  c.orot = rk & 7;
  unsigned ep_off = br * 256;
  unsigned touch = ((b * NH + head) * (SK * DH) + (split * NW + w) * 32768) * 2;
  unsigned long long pc;
  unsigned len;
  asm volatile("s_getpc_b64 %0\n.Lmha_code_begin:\n\ts_mov_b32 %1, .Lmha_code_end-.Lmha_code_begin" : "=s"(pc), "=s"(len));
  unsigned long long pc0 = pc + min((rk * NW + w) * 64u, len), pc1 = pc + min((256 + rk * NW + w) * 64u, len);
  asm volatile("" : "+s"(c.so_q), "+s"(c.so_kv), "+s"(c.so_o), "+s"(c.orot), "+s"(ep_off), "+s"(touch), "+s"(pc0), "+s"(pc1),
                    "+v"(c.vo_q), "+v"(c.vo_kv), "+v"(c.vo_o));
  __builtin_amdgcn_sched_barrier(0);
  asm volatile("" : "+s"(px), "+s"(pk_), "+s"(pv_), "+s"(pwq), "+s"(pwo), "+s"(out), "+s"(ws) : "s"(tstart));
  c.rq = __builtin_amdgcn_make_buffer_rsrc((void*)pwq, 0, DM * NH * DH * 2, RSRC_W3);
  run<0, 1>(c);
  c.cpf[0] = *(const CONST_AS unsigned*)pc0;
  c.cpf[1] = *(const CONST_AS unsigned*)pc1;
  c.cpf[2] = *(const CONST_AS unsigned*)((const CONST_AS unsigned char*)pk_ + touch);
  c.epoch = *(const CONST_AS unsigned*)((const CONST_AS unsigned char*)ws + ep_off);
  c.epa = *(const CONST_AS unsigned*)ws;
  run<1, N_Q>(c);
  c.ro = __builtin_amdgcn_make_buffer_rsrc((void*)pwo, 0, NH * DH * DM * 2, RSRC_W3);
  c.rk = __builtin_amdgcn_make_buffer_rsrc((void*)pk_, 0, 2 * NH * SK * DH * 2, RSRC_W3);
  run<N_Q, N_FIRST>(c);
  c.rx = __builtin_amdgcn_make_buffer_rsrc((void*)px, 0, 2 * DM * 2, RSRC_W3);
  c.vo_x = (((lane & 1) * DM + 224 * b + 32 * w + 8 * (lane >> 4)) * 2) | off7;
  c.rv = __builtin_amdgcn_make_buffer_rsrc((void*)pv_, 0, 2 * NH * SK * DH * 2, RSRC_W3);
  c.pv = (const CONST_AS unsigned*)((const CONST_AS unsigned char*)pv_ + touch);
  unsigned long long own = (unsigned long long)(out + b * (2 * DM) + br * DM + prod * 16);
  asm volatile("" : "+s"(own));
  c.po = (const CONST_AS unsigned*)(prod < DM / 16 ? own : (unsigned long long)c.pv);
  c.rw = __builtin_amdgcn_make_buffer_rsrc((void*)ws, 0, WS_END, RSRC_W3);
  const rsrc_t rout = __builtin_amdgcn_make_buffer_rsrc((void*)out, 0, 4 * DM * 2, RSRC_W3);
  c.q_st = (WS_Q + bh * 4096 + (lane & 1) * 2048 + b * 1024 + split * 256 + (lane & ~1) * 4) | (w == 0 ? 0 : OOB);
  c.q_ld = WS_Q + bh * 4096 + b * 2048 + lane * 16;
  c.ml_st = (WS_ML + (grp * NH + head) * 128 + split * 16) | (tid == 0 ? 0 : OOB);
  c.ml_ld = WS_ML + (grp * NH + head) * 128 + (lane & 3) * 16;
  c.t_st = (WS_T + ((bid >> 5) * 8 + (bid & 7)) * 8) | (tid == 64 * 3 && ((rk + (rk >> 2)) & 3) == 0 ? 0 : OOB);
  c.y_pf = (WS_PKT + (grp * 64 + prod) * PKT_BYTES + lane * 128) | (tid < PKT_BYTES / 128 ? 0 : OOB);

  run<N_FIRST, S.n - N_LAST>(c);

  const int pk = WS_PKT + grp * 64 * PKT_BYTES;
  const int dq = 32 * (lane >> 5) + 2 * (lane & 15) + ((lane >> 4) & 1);
  int a_wr = (L_RED + w * A_STR + ((dq - 8 * c.orot) & 63)) * 4, a_rd = (L_RED + (lane & 15) * A_STR + 4 * (lane >> 4)) * 4;
  int idx = (lane & 8) ? 0xEEEE : 0x4444;
  int y_off = (pk + prod * PKT_BYTES + tid * 4) | ((lane & 1) ? OOB : 0);
  int p_off = pk + (lane >> 2) * PKT_BYTES + prod * 64 + (lane & 3) * 16;
  int o_off = ((b * (2 * DM) + br * DM + prod * 16 + (lane & 3) * 4 + (lane >> 4)) * 2) | ((lane & 12) ? OOB : 0);
  int e_off = (prod == 0 && b == 0 && lane == 0) ? (int)ep_off : OOB;
  asm volatile("" : "+v"(a_wr), "+v"(a_rd), "+v"(idx), "+v"(y_off), "+v"(p_off), "+v"(o_off), "+v"(e_off));
  unsigned due = origin(lds, N_K + N_V - 1, 0u) + due_at(N_K + N_V - 1) + END_AFTER;
  asm volatile("" : "+s"(due));
  f32x4 y[4];
#pragma unroll
  for (int ct = 0; ct < 4; ++ct) { y[ct] = f32x4{0.f, 0.f, 0.f, 0.f}; asm volatile("" : "+v"(y[ct])); }

  run<S.n - N_LAST, S.n>(c);
  LDS_AS unsigned char* const lb = (LDS_AS unsigned char*)lds;
  *(LDS_AS unsigned*)(lb + a_wr) = cvtpk(c.acc.x * c.scale, c.acc.y * c.scale);
  *(LDS_AS unsigned*)(lb + a_wr + 8 * A_STR * 4) = cvtpk(c.acc.z * c.scale, c.acc.w * c.scale);
  __syncthreads();
  if (__builtin_expect(w == NW - 1, 0)) {
    if (prod < DM / 16) {
      unsigned cnt;
      asm volatile(
          "s_mov_b32 %0, 0\n"
          "1:\n\t"
          "s_memrealtime vcc\n\t"
          "s_waitcnt lgkmcnt(0)\n\t"
          "s_sub_u32 vcc_lo, %9, vcc_lo\n\t"
          "s_cmp_lt_u32 vcc_lo, 0x100\n\t"
          "s_cbranch_scc1 1b\n"
          "2:\n\t"
          "buffer_load_dwordx4 v[0:3], %1, %4, 0 offen sc1\n\t"
          "buffer_load_dwordx4 v[4:7], %1, %4, %10 offen sc1\n\t"
          "buffer_load_dwordx4 v[8:11], %1, %4, %11 offen sc1\n\t"
          "buffer_load_dwordx4 v[12:15], %1, %4, %12 offen sc1\n\t"
          "s_add_u32 %0, %0, 1\n\t"
          "s_waitcnt vmcnt(3)\n\t"
          "v_xor_b32 v0, %6, v0\n\tv_xor_b32 v1, %7, v1\n\tv_xor_b32 v2, %6, v2\n\tv_xor_b32 v3, %7, v3\n\t"
          "v_or3_b32 v16, v0, v1, v2\n\t"
          "s_waitcnt vmcnt(2)\n\t"
          "v_xor_b32 v4, %6, v4\n\tv_xor_b32 v5, %7, v5\n\tv_xor_b32 v6, %6, v6\n\tv_xor_b32 v7, %7, v7\n\t"
          "v_or3_b32 v16, v16, v3, v4\n\tv_or3_b32 v17, v5, v6, v7\n\t"
          "v_pk_add_f32 v[0:1], v[0:1], v[4:5]\n\tv_pk_add_f32 v[2:3], v[2:3], v[6:7]\n\t"
          "s_waitcnt vmcnt(1)\n\t"
          "v_xor_b32 v8, %6, v8\n\tv_xor_b32 v9, %7, v9\n\tv_xor_b32 v10, %6, v10\n\tv_xor_b32 v11, %7, v11\n\t"
          "v_or3_b32 v16, v16, v8, v9\n\tv_or3_b32 v17, v17, v10, v11\n\t"
          "s_waitcnt vmcnt(0)\n\t"
          "v_xor_b32 v12, %6, v12\n\tv_xor_b32 v13, %7, v13\n\tv_xor_b32 v14, %6, v14\n\tv_xor_b32 v15, %7, v15\n\t"
          "v_or3_b32 v16, v16, v12, v13\n\tv_or3_b32 v17, v17, v14, v15\n\t"
          "v_pk_add_f32 v[8:9], v[8:9], v[12:13]\n\tv_pk_add_f32 v[10:11], v[10:11], v[14:15]\n\t"
          "v_or_b32 v16, v16, v17\n\t"
          "v_pk_add_f32 v[0:1], v[0:1], v[8:9]\n\tv_pk_add_f32 v[2:3], v[2:3], v[10:11]\n\t"
          "v_and_b32 v16, 0xff, v16\n\t"
          "v_cmp_ne_u32 vcc, 0, v16\n\t"
          "v_permlane32_swap_b32 v0, v2\n\tv_permlane32_swap_b32 v1, v3\n\t"
          "s_nop 0\n\t"
          "v_add_f32 v0, v0, v2\n\tv_add_f32 v1, v1, v3\n\t"
          "s_nop 1\n\t"
          "v_permlane16_swap_b32 v0, v1\n\t"
          "s_nop 0\n\t"
          "v_add_f32 v0, v0, v1\n\t"
          "s_nop 1\n\t"
          "v_add_f32_dpp v0, v0, v0 row_ror:4 row_mask:0xf bank_mask:0xf\n\t"
          "s_nop 1\n\t"
          "v_add_f32_dpp v0, v0, v0 row_ror:8 row_mask:0xf bank_mask:0xf\n\t"
          "s_cmp_lt_u32 %0, %13\n\t"
          "v_cvt_pk_bf16_f32 v0, v0, v0\n\t"
          "s_cbranch_vccz 4f\n\t"
          "s_cbranch_scc1 2b\n\t"
          "v_mov_b32 v0, 0x7fc0\n"
          "4:\n\t"
          "buffer_store_short v0, %2, %5, 0 offen\n\t"
          "v_mov_b32 v1, %8\n"
          ".Lmha_code_end:\n\t"
          "buffer_store_dword v1, %3, %4, 0 offen sc1"
          : "=&s"(cnt)
          : "v"(p_off), "v"(o_off), "v"(e_off), "s"(c.rw), "s"(rout), "s"(c.tag & 255u), "s"((c.tag >> 8) & 255u), "s"(c.tag), "s"(due),
            "s"(16 * PKT_BYTES), "s"(32 * PKT_BYTES), "s"(48 * PKT_BYTES), "s"(SPIN_CAP)
          : "scc", "vcc", "memory", "v0", "v1", "v2", "v3", "v4", "v5", "v6", "v7", "v8", "v9", "v10", "v11", "v12", "v13", "v14", "v15", "v16", "v17");
    }
    __builtin_amdgcn_endpgm();
  }
  v4u am[4];
#pragma unroll
  for (int ks = 0; ks < 4; ++ks) am[ks] = *(LDS_AS v4u*)(lb + a_rd + 64 * ks);
  __builtin_amdgcn_sched_barrier(0);
#pragma unroll
  for (int ks = 0; ks < 4; ++ks)
#pragma unroll
    for (int ct = 0; ct < 4; ++ct) {
      const v2u b0 = c.of[2 * ks][ct][0], b1 = c.of[2 * ks][ct][1], b2 = c.of[2 * ks + 1][ct][0], b3 = c.of[2 * ks + 1][ct][1];
      const v8u bm = {b0.x, b0.y, b1.x, b1.y, b2.x, b2.y, b3.x, b3.y};
      y[ct] = __builtin_amdgcn_smfmac_f32_16x16x64_bf16(__builtin_bit_cast(bf16x8, am[ks]), __builtin_bit_cast(bf16x16, bm), y[ct], idx, 0, 0);
    }
  float sm[4];
#pragma unroll
  for (int ct = 0; ct < 4; ++ct) sm[ct] = (y[ct].x + y[ct].y) + (y[ct].z + y[ct].w);
  const float yv = add32(add16(sm[0], sm[1]), add16(sm[2], sm[3]));
  __builtin_amdgcn_raw_buffer_store_b64(granule(yv, dpp<XOR1>(yv), c.tag), c.rw, y_off, 0, AUX_SC1);
}

}

extern "C" void kernel_launch(void* const* d_in, const int* in_sizes, int n_in, void* d_out, int out_size, void* d_ws,
                              size_t ws_size, hipStream_t stream) {
  (void)in_sizes; (void)n_in; (void)out_size; (void)ws_size;
  Args a;
  for (int i = 0; i < 10; ++i) a.in[i / 5][i % 5] = (const __bf16*)d_in[i];
  a.out = (__bf16*)d_out;
  a.ws = (unsigned char*)d_ws;
  mha_decode<<<256, NTHR, 0, stream>>>(a);
}
```

```cpp
#include <hip/hip_runtime.h>
#include <hip/hip_bf16.h>
#include <utility>

namespace {

typedef unsigned v2u __attribute__((ext_vector_type(2)));
typedef unsigned v4u __attribute__((ext_vector_type(4)));
typedef unsigned v8u __attribute__((ext_vector_type(8)));
typedef float f32x2 __attribute__((ext_vector_type(2)));
typedef float f32x4 __attribute__((ext_vector_type(4)));
typedef __bf16 bf16x2 __attribute__((ext_vector_type(2)));
typedef __bf16 bf16x8 __attribute__((ext_vector_type(8)));
typedef __bf16 bf16x16 __attribute__((ext_vector_type(16)));
typedef short i16x4 __attribute__((ext_vector_type(4)));
typedef __amdgpu_buffer_rsrc_t rsrc_t;
#define LDS_AS __attribute__((address_space(3)))
#define CONST_AS __attribute__((address_space(4)))
#define COMPILER_FENCE() asm volatile("" ::: "memory")

constexpr int DM = 448, NH = 16, SK = 4096, DH = 256;
constexpr int NW = 8, NTHR = NW * 64;
constexpr int N_Q = 4, N_K = 64, N_V = 64, N_O = 32;
constexpr unsigned SPIN_CAP = 1u << 20;
constexpr int RSRC_W3 = 0x00020000, AUX_NT = 2, AUX_SC1 = 16, OOB = 0x40000000;
constexpr int WS_PKT = 4096, PKT_BYTES = DM * 4, WS_Q = WS_PKT + 256 * PKT_BYTES, WS_ML = WS_Q + 64 * 2048,
              WS_T = WS_ML + 64 * 128, WS_END = WS_T + 3 * 512;
constexpr int A_STR = 68;
constexpr int L_RED = 0, L_T = L_RED + 16 * A_STR, L_TQ = L_T + NW * 1024, L_ML = L_TQ + NW * 1024, L_P = L_ML + 2 * NW,
              L_F = L_P + NW * 64, L_R = L_F + NW * 2, L_END = L_R + 4;

constexpr int WIN = 3;
constexpr int O_FIRST = 4;
constexpr int O_START = 16, O_EVERY = 3;
constexpr int X_AT = 2, QMM_AT = 6, QPUB_AT = 9, QLD_AT = 17, QCHK_AT = 21, YPF_AT = 100;
constexpr int N_FIRST = N_Q + O_FIRST + 1, N_LAST = 3;
constexpr int TAU = 430, ORIGIN0 = 255, MARGIN = 36, GATE_FROM = 23, GATE_AHEAD = 21, STAGGER = 24, END_AFTER = 118;
constexpr int LAG_FROM = 7, LAG_EVERY = 4, GAIN_UP = 9, BAND = 58, GAIN_DOWN = 4;
constexpr int N_T = 3, T_AT[N_T] = {10, 38, 78}, T_LOAD[N_T] = {8, 6, 5}, T_CHECK[N_T] = {12, 10, 8}, T_KEEP[N_T] = {0, 14, 8},
              T_OVER = 12, T_SLOW[N_T] = {0, 32, 36}, T_EXTRA[N_T] = {0, 18, 40}, T_PACE = 43;
constexpr int G_N = 4, G_B = T_AT[2], G_A = G_B - 8 - G_N + 1, G_LIMIT = -8, G_EXTRA = 30, G_PACE = 18;
constexpr int due_at(int j) { return j * TAU / 16; }

enum Op : int { IQ, IK, IV, IO, IX, QPUB, QLD, MLPUB, YPF, TPUB, TLD, PEERLD,
                WAITV, WAITB, WAITS, GATE, PROBE, LAG, TCHK, SETUP, QTR, QMM, QCHK, CK, SM1, SM2, CV, PEERCHK, TRW, TRR };
struct Sched { int n; int op[900]; int a[900]; };
constexpr Sched make_sched() {
  Sched s{};
  int vmk[256] = {}, vmi[256] = {}, nvm = 0;
  auto emit = [&](int op, int a) {
    s.op[s.n] = op; s.a[s.n] = a; ++s.n;
    if (op <= PEERLD) { vmk[nvm] = op; vmi[nvm] = a; ++nvm; }
  };
  int no = 0, nk = 0, nv = 0, nw = 0, nr = 0, sm = 0;
  int lk = 0, lv = 0, lo = 0;
  for (int j = 0; j < N_Q; ++j) emit(IQ, j);
  while (no < O_FIRST) emit(IO, no++);
  for (int j = 0; j < N_K + N_V; ++j) {
    if (j > 0) {
      int p = nvm, seen = 0;
      while (seen < WIN) { --p; if (vmk[p] <= IV) ++seen; }
      if (j > LAG_FROM) {
        const int k = (j - LAG_FROM - 1) % LAG_EVERY + 1;
        int p2 = p, p0 = p;
        do --p2; while (vmk[p2] > IV);
        do ++p0; while (vmk[p0] > IV);
        emit(WAITV, nvm - 1 - p2);
        emit(WAITB, (nvm - 1 - p) | (k << 8));
        if ((j >= G_A && j < G_A + G_N) || (j > G_B - G_N && j <= G_B)) emit(PROBE, j);
        if (j > GATE_FROM) emit(GATE, due_at(j)); else emit(WAITS, (nvm - 1 - p0) | (k << 8));
        p = p2;
      } else emit(WAITV, nvm - 1 - p);
      if (vmk[p] == IK) lk = vmi[p] + 1;
      if (vmk[p] == IV) { lk = N_K; lv = vmi[p] + 1; }
      for (int t = 0; t < p; ++t) if (vmk[t] == IO) lo = vmi[t] + 1;
    }
    if (j < N_K) emit(IK, j); else emit(IV, j - N_K);
    if (j == 0) emit(SETUP, 0);
    if (j >= O_START && (j - O_START) % O_EVERY == 0 && no < N_O) emit(IO, no++);
    if (j >= LAG_FROM && (j - LAG_FROM) % LAG_EVERY == 0 && j + 1 < N_K + N_V) emit(LAG, j);
    if (j == X_AT) emit(IX, 0);
    if (j == YPF_AT) emit(YPF, 0);
    for (int t = 0; t < N_T; ++t) {
      if (j == T_AT[t]) emit(TPUB, t);
      if (j == T_AT[t] + T_LOAD[t]) emit(TLD, t);
      if (j == T_AT[t] + T_CHECK[t]) emit(TCHK, t);
    }
    if (j == QMM_AT - 1) emit(QTR, 0);
    if (j == QMM_AT) emit(QMM, 0);
    if (j == QPUB_AT) emit(QPUB, 0);
    if (j == QLD_AT) { emit(QLD, 0); emit(QLD, 1); }
    if (j == QCHK_AT) emit(QCHK, 0);
    else if (j > QCHK_AT && nk < N_K) { for (int t = 0; t < 3 && nk < lk; ++t) emit(CK, nk++); }
    else if (nk == N_K && sm < 2) { emit(sm == 0 ? SM1 : SM2, 0); ++sm; }
    else if (sm == 2) {
      for (int t = 0; t < 3 && nv < lv; ++t) {
        emit(CV, nv++);
        if (nv == 12) emit(MLPUB, 0);
        if (nv == N_V - 20) emit(PEERLD, 0);
        if (nv == N_V - 10) emit(PEERCHK, 0);
      }
    }
    if (nr < nw) emit(TRR, nr++);
    else if (4 * nw + 4 <= lo) emit(TRW, nw++);
  }
  if (no != N_O || nr != N_O / 4 || nv < N_V - 10 || nv > N_V - N_LAST) s.n = -1;
  else while (nv < N_V) emit(CV, nv++);
  return s;
}
constexpr Sched S = make_sched();
static_assert(S.n > 0, "the schedule must finish everything but the last V loads inside the stream");

constexpr int pair_off(int g) { return ((g & 1) << 12) | ((g & 2) << 14) | ((g & 12) << 15) | ((g & 16) << 16); }

__device__ __forceinline__ float dot2(unsigned a, unsigned b, float c) {
  return __builtin_amdgcn_fdot2_f32_bf16(__builtin_bit_cast(bf16x2, a), __builtin_bit_cast(bf16x2, b), c, false);
}
__device__ __forceinline__ unsigned cvtpk(float lo, float hi) {
  const bf16x2 r = {(__bf16)lo, (__bf16)hi};
  return __builtin_bit_cast(unsigned, r);
}
template <int CTRL> __device__ __forceinline__ float dpp(float x) {
  return __builtin_bit_cast(float, __builtin_amdgcn_mov_dpp(__builtin_bit_cast(int, x), CTRL, 0xf, 0xf, true));
}
constexpr int XOR1 = 0xB1, XOR2 = 0x4E, XOR7 = 0x141, XOR15 = 0x140, ROR8 = 0x128;
__device__ __forceinline__ float sum8(float x) { x += dpp<XOR1>(x); x += dpp<XOR2>(x); x += dpp<XOR7>(x); return x; }
__device__ __forceinline__ float max8(float x) {
  x = fmaxf(x, dpp<XOR1>(x)); x = fmaxf(x, dpp<XOR2>(x)); x = fmaxf(x, dpp<XOR7>(x));
  return x;
}
__device__ __forceinline__ float add16(float a, float b) {
  const auto s = __builtin_amdgcn_permlane16_swap(__float_as_uint(a), __float_as_uint(b), false, false);
  return __uint_as_float(s[0]) + __uint_as_float(s[1]);
}
__device__ __forceinline__ float add32(float a, float b) {
  const auto s = __builtin_amdgcn_permlane32_swap(__float_as_uint(a), __float_as_uint(b), false, false);
  return __uint_as_float(s[0]) + __uint_as_float(s[1]);
}
template <int CTRL> __device__ __forceinline__ float tstep(float lo, float hi, bool sel) {
  const float a = lo + dpp<CTRL>(lo), b = hi + dpp<CTRL>(hi);
  return sel ? b : a;
}
__device__ __forceinline__ float wave_sum(float x) {
  x = sum8(x); x += dpp<XOR15>(x); x = add16(x, x);
  return add32(x, x);
}
__device__ __forceinline__ float wave_max(float x) {
  x = max8(x); x = fmaxf(x, dpp<XOR15>(x));
  const auto s = __builtin_amdgcn_permlane16_swap(__float_as_uint(x), __float_as_uint(x), false, false);
  x = fmaxf(__uint_as_float(s[0]), __uint_as_float(s[1]));
  const auto t = __builtin_amdgcn_permlane32_swap(__float_as_uint(x), __float_as_uint(x), false, false);
  return fmaxf(__uint_as_float(t[0]), __uint_as_float(t[1]));
}
__device__ __forceinline__ f32x4 mfma(v4u a, v4u b, f32x4 c) {
  return __builtin_amdgcn_mfma_f32_16x16x32_bf16(__builtin_bit_cast(bf16x8, a), __builtin_bit_cast(bf16x8, b), c, 0, 0, 0);
}
__device__ __forceinline__ v2u tr_read(const float* p) {
  return __builtin_bit_cast(v2u, __builtin_amdgcn_ds_read_tr16_b64_v4i16((LDS_AS i16x4*)p));
}
__device__ __forceinline__ void await_tags(v4u& g, rsrc_t r, int off, unsigned tag) {
  if (__builtin_expect(!__all((g.y == tag) & (g.w == tag)), 0)) {
    unsigned spins = 0;
    bool ok;
#pragma unroll 1
    do {
      COMPILER_FENCE();
      g = __builtin_amdgcn_raw_buffer_load_b128(r, off, 0, AUX_SC1);
      ok = __all((g.y == tag) & (g.w == tag));
    } while (!ok && ++spins < SPIN_CAP);
    if (!ok) g.x = 0x7fc00000u;
  }
}
__device__ __forceinline__ v2u granule(float a, float b, unsigned tag) {
  return v2u{(__float_as_uint(a) & ~255u) | (tag & 255u), (__float_as_uint(b) & ~255u) | ((tag >> 8) & 255u)};
}
__device__ __forceinline__ bool untag(v4u& g, unsigned tag) {
  const unsigned t0 = tag & 255u, t1 = (tag >> 8) & 255u;
  g ^= v4u{t0, t1, t0, t1};
  return __all(((g.x | g.y | g.z | g.w) & 255u) == 0u);
}
__device__ __forceinline__ f32x4 await16(v4u g, rsrc_t r, int off, unsigned tag) {
  if (__builtin_expect(!untag(g, tag), 0)) {
    unsigned spins = 0;
    bool ok;
#pragma unroll 1
    do {
      COMPILER_FENCE();
      g = __builtin_amdgcn_raw_buffer_load_b128(r, off, 0, AUX_SC1);
      ok = untag(g, tag);
    } while (!ok && ++spins < SPIN_CAP);
    if (!ok) g.x = 0x7fc00000u;
  }
  return __builtin_bit_cast(f32x4, g);
}
__device__ __forceinline__ void flag_set(float* lds, int k, int w) {
  asm volatile("s_waitcnt lgkmcnt(0)" ::: "memory");
  ((LDS_AS volatile unsigned*)lds)[L_F + k * NW + w] = 1u;
}
__device__ __forceinline__ void flag_wait(float* lds, int k, int lane) {
  while (!__all(((LDS_AS volatile unsigned*)lds)[L_F + k * NW + (lane & 7)] != 0u)) {}
  COMPILER_FENCE();
}
__device__ __forceinline__ void wait_until(unsigned t) {
  asm volatile("1:\n\ts_memrealtime vcc\n\ts_waitcnt lgkmcnt(0)\n\ts_sub_u32 vcc_lo, %0, vcc_lo\n\ts_cmp_lt_u32 vcc_lo, 0x100\n\ts_cbranch_scc1 1b"
               :: "s"(t) : "scc", "vcc", "memory");
}
__device__ __forceinline__ unsigned origin(float* lds, int j, unsigned none) {
  v2u v = *(LDS_AS volatile v2u*)&lds[L_R];
  asm volatile("" : "+v"(v));
  const unsigned org = __builtin_amdgcn_readfirstlane(v.x), x = __builtin_amdgcn_readfirstlane(v.y);
  return org ? org + ((min(j, (int)(x >> 8) & 255) - (int)(x & 255u)) * (int)(x >> 16) >> 4) : none;
}

struct Ctx {
  rsrc_t rq, rk, rv, ro, rx, rw;
  float* lds;
  int w, lane, vo_q, vo_kv, vo_o, vo_x, so_q, so_kv, so_o, orot, q_st, q_ld, ml_st, ml_ld, y_pf, boost, slow, t_st;
  unsigned tc, tagt, epa, gate;
  int grow;
  v2u tg;
  const CONST_AS unsigned *pv, *po;
  unsigned t0, epoch, tag, pf, cpf[3], sel[4];
  v4u qb[N_Q], kb[N_K], vb[N_V], ob[N_O], xv, qg[2], qr, pp, peer;
  v2u qf[4][2];
  v2u of[8][4][2];
  f32x4 acc;
  float xs[5], s0, s1, mw, scale;
};

template <int I> __device__ __forceinline__ void step(Ctx& c) {
  constexpr int op = S.op[I], a = S.a[I];
  float* const lds = c.lds;
  const int w = c.w, lane = c.lane;
  if constexpr (op == IQ) {
    c.qb[a] = __builtin_amdgcn_raw_buffer_load_b128(c.rq, c.vo_q, c.so_q + 8 * a * (NH * DH * 2), AUX_NT);
  } else if constexpr (op == IK) {
    c.kb[a] = __builtin_amdgcn_raw_buffer_load_b128(c.rk, c.vo_kv + (a & 1) * 1024, c.so_kv ^ pair_off(a >> 1), AUX_NT);
  } else if constexpr (op == IV) {
    c.vb[a] = __builtin_amdgcn_raw_buffer_load_b128(c.rv, c.vo_kv + (a & 1) * 1024, c.so_kv ^ pair_off(a >> 1), AUX_NT);
  } else if constexpr (op == IO) {
    c.ob[a] = __builtin_amdgcn_raw_buffer_load_b128(c.ro, c.vo_o, c.so_o + (a < 4 || c.orot + a / 4 < 8 ? 8 * a : 8 * a - DH) * (DM * 2), 0);
  } else if constexpr (op == IX) {
    c.xv = __builtin_amdgcn_raw_buffer_load_b128(c.rx, c.vo_x, 0, 0);
  } else if constexpr (op == WAITV) {
    __builtin_amdgcn_s_waitcnt((a & 15) | ((a >> 4) << 14) | 0x0F70);
  } else if constexpr (op == LAG) {
    const unsigned none = c.t0 - 65536, o = origin(lds, a + 2, none);
    const int late = (int)((unsigned)__builtin_amdgcn_s_memrealtime() - (o == none ? c.t0 + ORIGIN0 : o)) - due_at(a);
    c.gate = o - GATE_AHEAD + (w * STAGGER >> 3);
    c.boost = late * GAIN_UP >> 4;
    c.slow = (-late - BAND) * GAIN_DOWN >> 4;
  } else if constexpr (op == TPUB) {
    c.tc = (unsigned)__builtin_amdgcn_s_memrealtime();
    const unsigned g = a == 2 ? min(max(c.grow, -128), 127) + 128 : 0;
    __builtin_amdgcn_raw_buffer_store_b64(v2u{(c.tc << 8) | g, c.tagt}, c.rw, c.t_st, a * 512, AUX_SC1);
  } else if constexpr (op == TLD) {
    c.tg = __builtin_amdgcn_raw_buffer_load_b64(c.rw, (WS_T + lane * 8) | (w == NW - 1 ? 0 : OOB), a * 512, AUX_SC1);
  } else if constexpr (op == TCHK) {
    const bool ok = c.tg.y == c.tagt;
    const float d = wave_sum(ok ? (float)((int)((c.tg.x & ~255u) - (c.tc << 8)) >> 8) : (float)due_at(T_LOAD[a])), n = wave_sum(ok ? 1.f : 0.f);
    unsigned org = c.tc + (int)(d * (1.f / 64)) - due_at(T_AT[a]) + MARGIN;
    bool use = w == NW - 1 && n > 16.f;
    int pace = 0;
    if constexpr (a > 0) {
      const unsigned old = origin(lds, T_AT[a], org);
      const int debt = (int)(org - old) - (MARGIN - GATE_AHEAD + 3 * STAGGER / 8);
      const bool slow = debt > T_SLOW[a];
      bool grows = false;
      if constexpr (a == 2) grows = wave_sum(ok ? (float)(c.tg.x & 255u) - (float)(128 + G_LIMIT) : 0.f) > 0.f;
      use = use && (debt > T_KEEP[a] || grows);
      pace = slow ? T_PACE : grows ? G_PACE : 0;
      org = old + max(min(3 * (debt - T_KEEP[a]), debt + T_OVER), 0) + (slow ? T_EXTRA[a] : grows ? G_EXTRA : 0);
    }
    constexpr int last = T_AT[a] < N_K ? N_K + 2 : N_K + N_V;
    *(LDS_AS volatile v2u*)&lds[L_R + (use ? 0 : 2)] = v2u{org | 1u, (unsigned)(T_AT[a] | (last << 8) | (pace << 16))};
  } else if constexpr (op == PROBE) {
    const int m = min((int)((unsigned)__builtin_amdgcn_s_memrealtime() - (c.gate + due_at(a))), 60);
    c.grow = a == G_A ? -m : a < G_A + G_N ? c.grow - m : c.grow + m;
  } else if constexpr (op == GATE) {
    wait_until(c.gate + a);
  } else if constexpr (op == WAITS) {
    asm volatile("s_cmp_lt_i32 %0, %1\n\ts_cbranch_scc1 1f\n\ts_waitcnt vmcnt(%2)\n1:"
                 :: "s"(c.slow), "i"(a >> 8), "i"(a & 255) : "scc", "memory");
  } else if constexpr (op == WAITB) {
    asm volatile("s_cmp_ge_i32 %0, %1\n\ts_cbranch_scc1 1f\n\ts_waitcnt vmcnt(%2)\n1:"
                 :: "s"(c.boost), "i"(a >> 8), "i"(a & 255) : "scc", "memory");
  } else if constexpr (op == SETUP) {
    const int row = lane & 15, j = row & 7;
    const bool act = ((lane >> 4) & 1) == (row >> 3);
    const unsigned two = (lane >> 5) ? 0x0302u : 0x0100u;
    const unsigned s = (j & 1) ? ((two << 16) | 0x0c0cu) : (0x0c0c0000u | two);
#pragma unroll
    for (int i = 0; i < 4; ++i) c.sel[i] = (act && (j >> 1) == i) ? s : 0x0c0c0c0cu;
    c.acc = f32x4{0.f, 0.f, 0.f, 0.f};
    ((unsigned*)lds)[L_F + (lane & 1) * NW + w] = 0u;
    ((unsigned*)lds)[L_R + (lane & 1)] = 0u;
    asm volatile("" :: "s"(c.cpf[0]), "s"(c.cpf[1]), "s"(c.cpf[2]));
    c.tagt = c.epa * 0x9E3779B1u + 0x7F4A7C15u;
    __syncthreads();
    c.cpf[0] = *c.pv; c.cpf[1] = *c.po;
  } else if constexpr (op == QTR) {
#pragma unroll
    for (int i = 0; i < 4; ++i) *(v4u*)&lds[L_TQ + w * 1024 + i * 256 + lane * 4] = c.qb[i];
    COMPILER_FENCE();
    const float* base = &lds[L_TQ + w * 1024 + (lane >> 4) * 256 + ((lane >> 2) & 3) * 32 + (lane & 3) * 2];
#pragma unroll
    for (int ct = 0; ct < 4; ++ct)
#pragma unroll
      for (int h = 0; h < 2; ++h) c.qf[ct][h] = tr_read(base + h * 128 + ct * 8);
    COMPILER_FENCE();
  } else if constexpr (op == QMM) {
    f32x4 d[4];
#pragma unroll
    for (int ct = 0; ct < 4; ++ct)
      d[ct] = mfma(c.xv, v4u{c.qf[ct][0].x, c.qf[ct][0].y, c.qf[ct][1].x, c.qf[ct][1].y}, f32x4{0.f, 0.f, 0.f, 0.f});
    c.tag = c.epoch * 0x9E3779B1u + 0x7F4A7C15u;
    asm volatile("" :: "s"(c.cpf[0]), "s"(c.cpf[1]));
    asm volatile("" : "+s"(c.tag));
    const f32x4 lo = (lane & 16) ? d[1] : d[0], hi = (lane & 16) ? d[3] : d[2];
    *(f32x2*)&lds[L_RED + w * 128 + lane * 2] = (lane & 32) ? f32x2{hi.x, hi.y} : f32x2{lo.x, lo.y};
    flag_set(lds, 0, w);
  } else if constexpr (op == QPUB) {
    flag_wait(lds, 0, lane);
    f32x2 t = {0.f, 0.f};
#pragma unroll
    for (int i = 0; i < 7; ++i) t += *(const f32x2*)&lds[L_RED + i * 128 + lane * 2];
    const float nx = dpp<XOR1>(t.x), ny = dpp<XOR1>(t.y);
    __builtin_amdgcn_raw_buffer_store_b64(granule((lane & 1) ? ny : t.x, (lane & 1) ? t.y : nx, c.tag), c.rw, c.q_st, 0, AUX_SC1);
  } else if constexpr (op == QLD) {
    c.qg[a] = __builtin_amdgcn_raw_buffer_load_b128(c.rw, c.q_ld + a * 1024, 0, AUX_SC1);
  } else if constexpr (op == QCHK) {
    const f32x4 q = await16(c.qg[0], c.rw, c.q_ld, c.tag) + await16(c.qg[1], c.rw, c.q_ld + 1024, c.tag);
    *(v2u*)&lds[L_TQ + w * 1024 + lane * 2] = v2u{cvtpk(q.x, q.y), cvtpk(q.z, q.w)};
    COMPILER_FENCE();
    c.qr = *(const v4u*)&lds[L_TQ + w * 1024 + (lane & 31) * 4];
  } else if constexpr (op == CK) {
    const v4u k = c.kb[a], q = c.qr;
    float x = dot2(k.x, q.x, dot2(k.z, q.z, 0.f)) + dot2(k.y, q.y, dot2(k.w, q.w, 0.f));
    if constexpr ((a & 1) == 1) x = add16(c.xs[0], x);
    if constexpr ((a & 3) == 3) x = tstep<ROR8>(c.xs[1], x, lane & 8);
    if constexpr ((a & 7) == 7) x = tstep<XOR7>(c.xs[2], x, lane & 4);
    if constexpr ((a & 15) == 15) x = tstep<XOR2>(c.xs[3], x, lane & 2);
    if constexpr ((a & 31) == 31) (a == 31 ? c.s0 : c.s1) = tstep<XOR1>(c.xs[4], x, lane & 1) * (0.0625f * 1.4426950408889634f);
    else c.xs[__builtin_ctz(~a)] = x;
  } else if constexpr (op == SM1) {
    const float m = wave_max(fmaxf(c.s0, c.s1));
    const float p0 = __builtin_amdgcn_exp2f(c.s0 - m), p1 = __builtin_amdgcn_exp2f(c.s1 - m);
    __bf16* pw = (__bf16*)&lds[L_P + w * 64] + (__builtin_bitreverse32(lane) >> 26);
    pw[0] = (__bf16)p0; pw[64] = (__bf16)p1;
    COMPILER_FENCE();
    c.mw = m; c.s0 = p0 + p1;
    c.pp = *(const v4u*)&lds[L_P + w * 64];
  } else if constexpr (op == SM2) {
    const float l = wave_sum(c.s0);
    lds[L_ML + 2 * w + (lane & 1)] = (lane & 1) ? l : c.mw;
    flag_set(lds, 1, w);
  } else if constexpr (op == MLPUB) {
    flag_wait(lds, 1, lane);
    const f32x2 ml = *(const f32x2*)&lds[L_ML + 2 * (lane & 7)];
    const float M = max8(ml.x), L = sum8(ml.y * __builtin_amdgcn_exp2f(ml.x - M));
    const v4u g = {__float_as_uint(M), c.tag, __float_as_uint(L), c.tag};
    __builtin_amdgcn_raw_buffer_store_b128(g, c.rw, c.ml_st, 0, AUX_SC1);
  } else if constexpr (op == CV) {
    const unsigned pr = c.pp[a & 3];
    const v4u am = {__builtin_amdgcn_perm(0u, pr, c.sel[0]), __builtin_amdgcn_perm(0u, pr, c.sel[1]),
                    __builtin_amdgcn_perm(0u, pr, c.sel[2]), __builtin_amdgcn_perm(0u, pr, c.sel[3])};
    c.acc = mfma(am, c.vb[a], c.acc);
    if constexpr ((a & 3) == 3 && a + 1 < N_V) c.pp = *(const v4u*)&lds[L_P + w * 64 + (a + 1)];
  } else if constexpr (op == YPF) {
    c.pf = __builtin_amdgcn_raw_buffer_load_b32(c.rw, c.y_pf, 0, AUX_SC1);
  } else if constexpr (op == PEERLD) {
    c.peer = __builtin_amdgcn_raw_buffer_load_b128(c.rw, c.ml_ld, 0, AUX_SC1);
  } else if constexpr (op == PEERCHK) {
    asm volatile("" :: "v"(c.pf));
    await_tags(c.peer, c.rw, c.ml_ld, c.tag);
    const float m = __uint_as_float(c.peer.x);
    float M = fmaxf(m, dpp<XOR1>(m)); M = fmaxf(M, dpp<XOR2>(M));
    float L = __uint_as_float(c.peer.z) * __builtin_amdgcn_exp2f(m - M); L += dpp<XOR1>(L); L += dpp<XOR2>(L);
    c.scale = __builtin_amdgcn_exp2f(c.mw - M) / L;
  } else if constexpr (op == TRW) {
#pragma unroll
    for (int i = 0; i < 4; ++i) *(v4u*)&lds[L_T + w * 1024 + i * 256 + lane * 4] = c.ob[4 * a + i];
    COMPILER_FENCE();
  } else if constexpr (op == TRR) {
    const float* base = &lds[L_T + w * 1024 + (lane >> 4) * 256 + ((lane >> 2) & 3) * 32 + (lane & 3) * 2];
#pragma unroll
    for (int ct = 0; ct < 4; ++ct)
#pragma unroll
      for (int h = 0; h < 2; ++h) c.of[a][ct][h] = tr_read(base + h * 128 + ct * 8);
    COMPILER_FENCE();
  }
  __builtin_amdgcn_sched_barrier(0);
}
template <int B, int... I> __device__ __forceinline__ void run_seq(Ctx& c, std::integer_sequence<int, I...>) { (step<B + I>(c), ...); }
template <int B, int E> __device__ __forceinline__ void run(Ctx& c) { run_seq<B>(c, std::make_integer_sequence<int, E - B>{}); }

struct Args { const __bf16* in[2][5]; __bf16* out; unsigned char* ws; };
static_assert(sizeof(Args) == 12 * 8, "no padding");

__global__ void __launch_bounds__(NTHR, 2) mha_decode(Args args) {
  __shared__ __attribute__((aligned(16))) float lds[L_END];
  Ctx c;
  const int bid = blockIdx.x, br = (bid >> 2) & 1;
  const __bf16 *px = args.in[br][0], *pk_ = args.in[br][1], *pv_ = args.in[br][2], *pwq = args.in[br][3], *pwo = args.in[br][4];
  __bf16* out = args.out;
  unsigned char* ws = args.ws;
  const unsigned long long tstart = __builtin_amdgcn_s_memrealtime();
  __builtin_amdgcn_sched_barrier(0);
  const int rk = bid >> 3, head = (bid & 3) * 4 + (rk >> 3), bh = br * NH + head;
  const int b = (rk >> 2) & 1, split = rk & 3, grp = br * 2 + b, prod = head * 4 + split;
  const int tid = threadIdx.x, lane = tid & 63, w = __builtin_amdgcn_readfirstlane(tid >> 6);
  const int off7 = w < 7 ? 0 : OOB;
  c.w = w; c.lane = lane; c.lds = lds;
  c.t0 = (unsigned)tstart;
  c.vo_q = ((lane >> 3) * (NH * DH * 2) + (lane & 7) * 16) | off7;
  c.so_q = ((224 * b + 32 * w) * NH * DH + head * DH + split * 64) * 2;
  c.vo_kv = lane * 16;
  c.so_kv = (b * NH + head) * (SK * DH * 2) + (((w & 1) << 11) | ((w & 2) << 12) | ((w & 4) << 14) | ((split & 1) << 14) | ((split & 2) << 18)) +
            (((head & 2) ? 0x68000 : 0) ^ ((head & 8) << 12) ^ ((((head >> 2) ^ b) & 1) << 20));
  c.vo_o = ((lane >> 3) * (DM * 2) + w * 128 + (lane & 7) * 16) | off7;
  c.so_o = (head * DH + 32 * (rk & 7)) * (DM * 2);
  c.orot = rk & 7;
  unsigned ep_off = br * 256;
  unsigned touch = ((b * NH + head) * (SK * DH) + (split * NW + w) * 32768) * 2;
  unsigned long long pc;
  unsigned len;
  asm volatile("s_getpc_b64 %0\n.Lmha_code_begin:\n\ts_mov_b32 %1, .Lmha_code_end-.Lmha_code_begin" : "=s"(pc), "=s"(len));
  unsigned long long pc0 = pc + min((rk * NW + w) * 64u, len), pc1 = pc + min((256 + rk * NW + w) * 64u, len);
  asm volatile("" : "+s"(c.so_q), "+s"(c.so_kv), "+s"(c.so_o), "+s"(c.orot), "+s"(ep_off), "+s"(touch), "+s"(pc0), "+s"(pc1),
                    "+v"(c.vo_q), "+v"(c.vo_kv), "+v"(c.vo_o));
  __builtin_amdgcn_sched_barrier(0);
  asm volatile("" : "+s"(px), "+s"(pk_), "+s"(pv_), "+s"(pwq), "+s"(pwo), "+s"(out), "+s"(ws) : "s"(tstart));
  c.rq = __builtin_amdgcn_make_buffer_rsrc((void*)pwq, 0, DM * NH * DH * 2, RSRC_W3);
  run<0, 1>(c);
  c.cpf[0] = *(const CONST_AS unsigned*)pc0;
  c.cpf[1] = *(const CONST_AS unsigned*)pc1;
  c.cpf[2] = *(const CONST_AS unsigned*)((const CONST_AS unsigned char*)pk_ + touch);
  c.epoch = *(const CONST_AS unsigned*)((const CONST_AS unsigned char*)ws + ep_off);
  c.epa = *(const CONST_AS unsigned*)ws;
  run<1, N_Q>(c);
  c.ro = __builtin_amdgcn_make_buffer_rsrc((void*)pwo, 0, NH * DH * DM * 2, RSRC_W3);
  c.rk = __builtin_amdgcn_make_buffer_rsrc((void*)pk_, 0, 2 * NH * SK * DH * 2, RSRC_W3);
  run<N_Q, N_FIRST>(c);
  c.rx = __builtin_amdgcn_make_buffer_rsrc((void*)px, 0, 2 * DM * 2, RSRC_W3);
  c.vo_x = (((lane & 1) * DM + 224 * b + 32 * w + 8 * (lane >> 4)) * 2) | off7;
  c.rv = __builtin_amdgcn_make_buffer_rsrc((void*)pv_, 0, 2 * NH * SK * DH * 2, RSRC_W3);
  c.pv = (const CONST_AS unsigned*)((const CONST_AS unsigned char*)pv_ + touch);
  unsigned long long own = (unsigned long long)(out + b * (2 * DM) + br * DM + prod * 16);
  asm volatile("" : "+s"(own));
  c.po = (const CONST_AS unsigned*)(prod < DM / 16 ? own : (unsigned long long)c.pv);
  c.rw = __builtin_amdgcn_make_buffer_rsrc((void*)ws, 0, WS_END, RSRC_W3);
  const rsrc_t rout = __builtin_amdgcn_make_buffer_rsrc((void*)out, 0, 4 * DM * 2, RSRC_W3);
  c.q_st = (WS_Q + bh * 4096 + (lane & 1) * 2048 + b * 1024 + split * 256 + (lane & ~1) * 4) | (w == 0 ? 0 : OOB);
  c.q_ld = WS_Q + bh * 4096 + b * 2048 + lane * 16;
  c.ml_st = (WS_ML + (grp * NH + head) * 128 + split * 16) | (tid == 0 ? 0 : OOB);
  c.ml_ld = WS_ML + (grp * NH + head) * 128 + (lane & 3) * 16;
  c.t_st = (WS_T + ((bid >> 5) * 8 + (bid & 7)) * 8) | (tid == 64 * 3 && ((rk + (rk >> 2)) & 3) == 0 ? 0 : OOB);
  c.y_pf = (WS_PKT + (grp * 64 + prod) * PKT_BYTES + lane * 128) | (tid < PKT_BYTES / 128 ? 0 : OOB);

  run<N_FIRST, S.n - N_LAST>(c);

  const int pk = WS_PKT + grp * 64 * PKT_BYTES;
  const int dq = 32 * (lane >> 5) + 2 * (lane & 15) + ((lane >> 4) & 1);
  int a_wr = (L_RED + w * A_STR + ((dq - 8 * c.orot) & 63)) * 4, a_rd = (L_RED + (lane & 15) * A_STR + 4 * (lane >> 4)) * 4;
  int idx = (lane & 8) ? 0xEEEE : 0x4444;
  int y_off = (pk + prod * PKT_BYTES + tid * 4) | ((lane & 1) ? OOB : 0);
  int p_off = pk + (lane >> 2) * PKT_BYTES + prod * 64 + (lane & 3) * 16;
  int o_off = ((b * (2 * DM) + br * DM + prod * 16 + (lane & 3) * 4 + (lane >> 4)) * 2) | ((lane & 12) ? OOB : 0);
  int e_off = (prod == 0 && b == 0 && lane == 0) ? (int)ep_off : OOB;
  asm volatile("" : "+v"(a_wr), "+v"(a_rd), "+v"(idx), "+v"(y_off), "+v"(p_off), "+v"(o_off), "+v"(e_off));
  unsigned due = origin(lds, N_K + N_V - 1, 0u) + due_at(N_K + N_V - 1) + END_AFTER;
  asm volatile("" : "+s"(due));
  f32x4 y[4];
#pragma unroll
  for (int ct = 0; ct < 4; ++ct) { y[ct] = f32x4{0.f, 0.f, 0.f, 0.f}; asm volatile("" : "+v"(y[ct])); }

  run<S.n - N_LAST, S.n>(c);
  LDS_AS unsigned char* const lb = (LDS_AS unsigned char*)lds;
  *(LDS_AS unsigned*)(lb + a_wr) = cvtpk(c.acc.x * c.scale, c.acc.y * c.scale);
  *(LDS_AS unsigned*)(lb + a_wr + 8 * A_STR * 4) = cvtpk(c.acc.z * c.scale, c.acc.w * c.scale);
  __syncthreads();
  if (__builtin_expect(w == NW - 1, 0)) {
    if (prod < DM / 16) {
      unsigned cnt;
      asm volatile(
          "s_mov_b32 %0, 0\n"
          "1:\n\t"
          "s_memrealtime vcc\n\t"
          "s_waitcnt lgkmcnt(0)\n\t"
          "s_sub_u32 vcc_lo, %9, vcc_lo\n\t"
          "s_cmp_lt_u32 vcc_lo, 0x100\n\t"
          "s_cbranch_scc1 1b\n"
          "2:\n\t"
          "buffer_load_dwordx4 v[0:3], %1, %4, 0 offen sc1\n\t"
          "buffer_load_dwordx4 v[4:7], %1, %4, %10 offen sc1\n\t"
          "buffer_load_dwordx4 v[8:11], %1, %4, %11 offen sc1\n\t"
          "buffer_load_dwordx4 v[12:15], %1, %4, %12 offen sc1\n\t"
          "s_add_u32 %0, %0, 1\n\t"
          "s_waitcnt vmcnt(3)\n\t"
          "v_xor_b32 v0, %6, v0\n\tv_xor_b32 v1, %7, v1\n\tv_xor_b32 v2, %6, v2\n\tv_xor_b32 v3, %7, v3\n\t"
          "v_or3_b32 v16, v0, v1, v2\n\t"
          "s_waitcnt vmcnt(2)\n\t"
          "v_xor_b32 v4, %6, v4\n\tv_xor_b32 v5, %7, v5\n\tv_xor_b32 v6, %6, v6\n\tv_xor_b32 v7, %7, v7\n\t"
          "v_or3_b32 v16, v16, v3, v4\n\tv_or3_b32 v17, v5, v6, v7\n\t"
          "v_pk_add_f32 v[0:1], v[0:1], v[4:5]\n\tv_pk_add_f32 v[2:3], v[2:3], v[6:7]\n\t"
          "s_waitcnt vmcnt(1)\n\t"
          "v_xor_b32 v8, %6, v8\n\tv_xor_b32 v9, %7, v9\n\tv_xor_b32 v10, %6, v10\n\tv_xor_b32 v11, %7, v11\n\t"
          "v_or3_b32 v16, v16, v8, v9\n\tv_or3_b32 v17, v17, v10, v11\n\t"
          "s_waitcnt vmcnt(0)\n\t"
          "v_xor_b32 v12, %6, v12\n\tv_xor_b32 v13, %7, v13\n\tv_xor_b32 v14, %6, v14\n\tv_xor_b32 v15, %7, v15\n\t"
          "v_or3_b32 v16, v16, v12, v13\n\tv_or3_b32 v17, v17, v14, v15\n\t"
          "v_pk_add_f32 v[8:9], v[8:9], v[12:13]\n\tv_pk_add_f32 v[10:11], v[10:11], v[14:15]\n\t"
          "v_or_b32 v16, v16, v17\n\t"
          "v_pk_add_f32 v[0:1], v[0:1], v[8:9]\n\tv_pk_add_f32 v[2:3], v[2:3], v[10:11]\n\t"
          "v_and_b32 v16, 0xff, v16\n\t"
          "v_cmp_ne_u32 vcc, 0, v16\n\t"
          "v_permlane32_swap_b32 v0, v2\n\tv_permlane32_swap_b32 v1, v3\n\t"
          "s_nop 0\n\t"
          "v_add_f32 v0, v0, v2\n\tv_add_f32 v1, v1, v3\n\t"
          "s_nop 1\n\t"
          "v_permlane16_swap_b32 v0, v1\n\t"
          "s_nop 0\n\t"
          "v_add_f32 v0, v0, v1\n\t"
          "s_nop 1\n\t"
          "v_add_f32_dpp v0, v0, v0 row_ror:4 row_mask:0xf bank_mask:0xf\n\t"
          "s_nop 1\n\t"
          "v_add_f32_dpp v0, v0, v0 row_ror:8 row_mask:0xf bank_mask:0xf\n\t"
          "s_cmp_lt_u32 %0, %13\n\t"
          "v_cvt_pk_bf16_f32 v0, v0, v0\n\t"
          "s_cbranch_vccz 4f\n\t"
          "s_cbranch_scc1 2b\n\t"
          "v_mov_b32 v0, 0x7fc0\n"
          "4:\n\t"
          "buffer_store_short v0, %2, %5, 0 offen\n\t"
          "v_mov_b32 v1, %8\n"
          ".Lmha_code_end:\n\t"
          "buffer_store_dword v1, %3, %4, 0 offen sc1"
          : "=&s"(cnt)
          : "v"(p_off), "v"(o_off), "v"(e_off), "s"(c.rw), "s"(rout), "s"(c.tag & 255u), "s"((c.tag >> 8) & 255u), "s"(c.tag), "s"(due),
            "s"(16 * PKT_BYTES), "s"(32 * PKT_BYTES), "s"(48 * PKT_BYTES), "s"(SPIN_CAP)
          : "scc", "vcc", "memory", "v0", "v1", "v2", "v3", "v4", "v5", "v6", "v7", "v8", "v9", "v10", "v11", "v12", "v13", "v14", "v15", "v16", "v17");
    }
    __builtin_amdgcn_endpgm();
  }
  v4u am[4];
#pragma unroll
  for (int ks = 0; ks < 4; ++ks) am[ks] = *(LDS_AS v4u*)(lb + a_rd + 64 * ks);
  __builtin_amdgcn_sched_barrier(0);
#pragma unroll
  for (int ks = 0; ks < 4; ++ks)
#pragma unroll
    for (int ct = 0; ct < 4; ++ct) {
      const v2u b0 = c.of[2 * ks][ct][0], b1 = c.of[2 * ks][ct][1], b2 = c.of[2 * ks + 1][ct][0], b3 = c.of[2 * ks + 1][ct][1];
      const v8u bm = {b0.x, b0.y, b1.x, b1.y, b2.x, b2.y, b3.x, b3.y};
      y[ct] = __builtin_amdgcn_smfmac_f32_16x16x64_bf16(__builtin_bit_cast(bf16x8, am[ks]), __builtin_bit_cast(bf16x16, bm), y[ct], idx, 0, 0);
    }
  float sm[4];
#pragma unroll
  for (int ct = 0; ct < 4; ++ct) sm[ct] = (y[ct].x + y[ct].y) + (y[ct].z + y[ct].w);
  const float yv = add32(add16(sm[0], sm[1]), add16(sm[2], sm[3]));
  __builtin_amdgcn_raw_buffer_store_b64(granule(yv, dpp<XOR1>(yv), c.tag), c.rw, y_off, 0, AUX_SC1);
}

}

extern "C" void kernel_launch(void* const* d_in, const int* in_sizes, int n_in, void* d_out, int out_size, void* d_ws,
                              size_t ws_size, hipStream_t stream) {
  (void)in_sizes; (void)n_in; (void)out_size; (void)ws_size;
  Args a;
  for (int i = 0; i < 10; ++i) a.in[i / 5][i % 5] = (const __bf16*)d_in[i];
  a.out = (__bf16*)d_out;
  a.ws = (unsigned char*)d_ws;
  mha_decode<<<256, NTHR, 0, stream>>>(a);
}
```
